```python
import torch

def _softmax(x, axis=-1):
    return torch.softmax(x, dim=axis)
def _gelu(x):
    return torch.nn.functional.gelu(x, approximate="tanh")
def _silu(x):
    return torch.nn.functional.silu(x)
def _sigmoid(x):
    return torch.sigmoid(x)
def _layernorm(x, axis=-1, eps=1e-5):
    m = x.mean(dim=axis, keepdim=True)
    v = x.var(dim=axis, keepdim=True, unbiased=False)
    return (x - m) / torch.sqrt(v + eps)
def _scatter_add(tgt, idx, src, axis=0):
    return tgt.clone().index_add_(axis, idx, src)
def _segment_sum(src, seg, n):
    out = torch.zeros((n,) + src.shape[1:], dtype=src.dtype)
    return out.index_add_(0, seg, src)
def _pad(x, pw):
    flat = tuple(v for pair in reversed(pw) for v in pair)
    return torch.nn.functional.pad(x, flat)

kernel_name = 'gated_mix__spectral_gfnet__ssm_conv_s5__prompt'

def setup_inputs():

    _w = (lambda a: (lambda t: t.to(torch.bfloat16) if t.is_floating_point() else t)(torch.as_tensor(a)))
    def _A_setup():
        B, S, D = 1, 2048, 1024
        Df = D // 2 + 1
        return {
            "x": _w((lambda *s: torch.randn(*s, dtype=torch.bfloat16) * 0.05)(B, S, D)),
            "w_r": _w((lambda *s: torch.randn(*s, dtype=torch.bfloat16) * 0.8)(S, Df) * 0.02),
            "w_i": _w((lambda *s: torch.randn(*s, dtype=torch.bfloat16) * 0.8)(S, Df) * 0.02),
        }
    _A_in = _A_setup()

    def _B_setup():
        B, S, D = 1, 2048, 1024
        P = D
        Lambda_re = -0.5 * (lambda _x: torch.exp(torch.as_tensor(_x)))((lambda *s: torch.randn(*s, dtype=torch.bfloat16) * 0.2)(P))
        Lambda_im = (lambda *s: torch.randn(*s, dtype=torch.bfloat16) * 0.4)(P) * 1.0
        B_re = (lambda *s: torch.randn(*s, dtype=torch.bfloat16) * 0.2)(P, D) * (D ** -0.5)
        B_im = (lambda *s: torch.randn(*s, dtype=torch.bfloat16) * 0.2)(P, D) * (D ** -0.5)
        C_re = (lambda *s: torch.randn(*s, dtype=torch.bfloat16) * 0.2)(D, P) * (P ** -0.5)
        C_im = (lambda *s: torch.randn(*s, dtype=torch.bfloat16) * 0.2)(D, P) * (P ** -0.5)
        log_dt = (lambda *s: torch.randn(*s, dtype=torch.bfloat16) * 0.2)(P) * 0.3 - 2.0
        Dskip = (lambda *s: torch.randn(*s, dtype=torch.bfloat16) * 0.05)(D)
        return {
            "x": _w((lambda *s: torch.randn(*s, dtype=torch.bfloat16) * 0.05)(B, S, D)),
            "Lambda_re": _w(Lambda_re),
            "Lambda_im": _w(Lambda_im),
            "B_re": _w(B_re),
            "B_im": _w(B_im),
            "C_re": _w(C_re),
            "C_im": _w(C_im),
            "log_dt": _w(log_dt),
            "Dskip": _w(Dskip),
        }
    _B_in = _B_setup()

    return {
        "A_x": _A_in["x"],
        "A_w_r": _A_in["w_r"],
        "A_w_i": _A_in["w_i"],
        "B_x": _B_in["x"],
        "B_Lambda_re": _B_in["Lambda_re"],
        "B_Lambda_im": _B_in["Lambda_im"],
        "B_B_re": _B_in["B_re"],
        "B_B_im": _B_in["B_im"],
        "B_C_re": _B_in["C_re"],
        "B_C_im": _B_in["C_im"],
        "B_log_dt": _B_in["log_dt"],
        "B_Dskip": _B_in["Dskip"],
        "gate_w": _w((lambda *s: torch.randn(*s, dtype=torch.bfloat16) * 0.05)(1024) * 16.0),
    }

def reference(A_x, A_w_r, A_w_i, B_x, B_Lambda_re, B_Lambda_im, B_B_re, B_B_im, B_C_re, B_C_im, B_log_dt, B_Dskip, gate_w):
    def _A_ref(x, w_r, w_i):
        B, S, D = x.shape
        X = (lambda x, axes=(-2,-1): torch.fft.rfft2(x.float(), dim=axes))(x, axes=(-2, -1))
        W = w_r + 1j * w_i
        X = X * W
        y = (lambda x, s, axes=(-2,-1): torch.fft.irfft2(x, s=s, dim=axes).to(torch.bfloat16))(X, (S, D), axes=(-2, -1))
        return y

    def _B_ref(x, Lambda_re, Lambda_im, B_re, B_im, C_re, C_im, log_dt, Dskip):
        Bsz, L, H = x.shape
        Lam = Lambda_re + 1j * Lambda_im
        Bmat = B_re + 1j * B_im
        Cmat = C_re + 1j * C_im
        dt = torch.exp(log_dt)
        Lam_bar = torch.exp(dt * Lam)
        B_bar = ((Lam_bar - 1.0) / Lam)[:, None] * Bmat
        Bu = torch.einsum("bld,pd->blp", x + 0j, B_bar)
        h = torch.zeros_like(Bu[:, 0, :])
        hs = []
        for t in range(L):
            h = Lam_bar[None, :] * h + Bu[:, t, :]
            hs.append(h)
        hseq = torch.stack(hs, 1)
        y = 2.0 * torch.einsum("blp,dp->bld", hseq, Cmat).real + x * Dskip[None, None, :]
        return y

    x = A_x
    g = _sigmoid((x * gate_w).sum(-1, keepdims=True))
    a = _A_ref(x, A_w_r, A_w_i)
    b = _B_ref(x, B_Lambda_re, B_Lambda_im, B_B_re, B_B_im, B_C_re, B_C_im, B_log_dt, B_Dskip)
    return g * a + (1 - g) * b
```

```cpp
#define DBG_TS
#include <hip/hip_runtime.h>
#include <hip/hip_bf16.h>
#include <stdint.h>

typedef unsigned short u16;
typedef unsigned int u32;
typedef unsigned long long u64;
typedef __attribute__((ext_vector_type(8))) short s16x8;
typedef __attribute__((ext_vector_type(4))) short s16x4;
typedef __attribute__((ext_vector_type(8))) __bf16 bf16x8;
typedef __attribute__((ext_vector_type(2))) __bf16 bf16x2;
typedef __attribute__((ext_vector_type(2))) float f32x2;
typedef __attribute__((ext_vector_type(4))) float f32x4;
typedef __attribute__((ext_vector_type(16))) float f32x16;
typedef __attribute__((ext_vector_type(4))) u32 u32x4;
typedef __attribute__((ext_vector_type(2))) u32 u32x2;
#define DI __device__ __forceinline__
#define LDSP __attribute__((address_space(3)))
#define GLBP __attribute__((address_space(1)))
#define RLX __ATOMIC_RELAXED, __HIP_MEMORY_SCOPE_AGENT

struct HTab { u16 v[16384]; };
constexpr double c_sin(double x) { double t = x, s = x; for (int i = 1; i < 16; ++i) { t *= -x * x / ((2 * i) * (2 * i + 1)); s += t; } return s; }
constexpr double c_cos(double x) { double t = 1, s = 1; for (int i = 1; i < 16; ++i) { t *= -x * x / ((2 * i - 1) * (2 * i)); s += t; } return s; }
constexpr u16 c_bf16(double d) {
  if (d < 1e-9 && d > -1e-9) return 0;
  u32 b = __builtin_bit_cast(u32, (float)d);
  return (u16)((b + 0x7fffu + ((b >> 16) & 1u)) >> 16);
}
constexpr HTab make_htab() {
  HTab t{}; u16 c[256] = {};
  for (int n = 0; n < 256; ++n) { double x = 2.0 * 3.14159265358979323846 * (n < 128 ? n : n - 256) / 256.0; c[n] = c_bf16(c_sin(x) + c_cos(x)); }
  for (int i = 0; i < 16384; ++i) t.v[i] = c[((i >> 6) * (i & 63)) & 255];
  return t;
}
__device__ __attribute__((aligned(256))) const HTab g_htab = make_htab();

constexpr size_t WS_E = 0, WS_FA = 4096, WS_G = 16384, WS_AGG = 65536;
constexpr size_t WS_U = 1u << 20, WS_T = 5u << 20, WS_H = 9u << 20;
constexpr int LDS1 = 4 * 32768 + 4096 + 36 * 544, LDS2 = 32768 + 7 * 16384;
constexpr u32 SPIN_CAP = 1u << 20;

DI float bf2f(u32 h) { return __builtin_bit_cast(float, h << 16); }
DI u32 pk2(float a, float b) { f32x2 v = {a, b}; return __builtin_bit_cast(u32, __builtin_convertvector(v, bf16x2)); }
DI float rbf(float a) { return bf2f(pk2(a, a) & 0xffffu); }
DI void glds(const void* g, char* l) { __builtin_amdgcn_global_load_lds((const GLBP void*)g, (LDSP void*)l, 16, 0, 0); }
typedef __amdgpu_buffer_rsrc_t rsrc_t;
DI rsrc_t mkr(const void* p, int bytes) { return __builtin_amdgcn_make_buffer_rsrc((void*)p, 0, bytes, 0x00020000); }
DI void blds(rsrc_t r, u32 vo, u32 so, char* l) { __builtin_amdgcn_raw_ptr_buffer_load_lds(r, (LDSP void*)l, 16, vo, so, 0, 0); }
DI bf16x8 ldsr(u32 a, int off) { return *(const LDSP bf16x8*)(uintptr_t)(a + off); }
DI u32 ldsa(const char* p) { return (u32)(uintptr_t)(LDSP const char*)p; }
DI u32x2 trr(const char* p) { return __builtin_bit_cast(u32x2, __builtin_amdgcn_ds_read_tr16_b64_v4i16((LDSP s16x4*)p)); }
DI bf16x8 cat(u32x2 a, u32x2 b) { u32x4 v = {a.x, a.y, b.x, b.y}; return __builtin_bit_cast(bf16x8, v); }
DI bf16x8 pk8(f32x4 a, f32x4 b) { u32x4 v = {pk2(a[0], a[1]), pk2(a[2], a[3]), pk2(b[0], b[1]), pk2(b[2], b[3])}; return __builtin_bit_cast(bf16x8, v); }
DI f32x16 mma32(bf16x8 a, bf16x8 b, f32x16 c) { return __builtin_amdgcn_mfma_f32_32x32x16_bf16(a, b, c, 0, 0, 0); }
DI f32x4 mma16(bf16x8 a, bf16x8 b, f32x4 c) { return __builtin_amdgcn_mfma_f32_16x16x32_bf16(a, b, c, 0, 0, 0); }
DI float cosr(float rev) { return __builtin_amdgcn_cosf(rev); }
DI float sinr(float rev) { return __builtin_amdgcn_sinf(rev); }
template <int O> DI u32x4 ald4(const void* p) { u32x4 v; asm volatile("global_load_dwordx4 %0, %1, off offset:%2" : "=v"(v) : "v"(p), "n"(O) : "memory"); return v; }
DI u32 ald1(const void* p) { u32 v; asm volatile("global_load_dword %0, %1, off" : "=v"(v) : "v"(p) : "memory"); return v; }
DI u32 aldh(const void* p) { u32 v; asm volatile("global_load_ushort %0, %1, off" : "=v"(v) : "v"(p) : "memory"); return v; }
DI u64 ald2c(const void* p) { u64 v; asm volatile("global_load_dwordx2 %0, %1, off sc1" : "=v"(v) : "v"(p) : "memory"); return v; }
#define SB() __builtin_amdgcn_sched_barrier(0)
#define PIN(v) asm volatile("" : "+v"(v))
#define WAITV(n) asm volatile("s_waitcnt vmcnt(" #n ")" ::: "memory")
#define BAR() do { asm volatile("" ::: "memory"); __builtin_amdgcn_s_barrier(); asm volatile("" ::: "memory"); } while (0)
#define LBAR() do { asm volatile("s_waitcnt lgkmcnt(0)" ::: "memory"); __builtin_amdgcn_s_barrier(); asm volatile("" ::: "memory"); } while (0)
DI u64 mix64(u64 e) { u64 z = e + 0x9e3779b97f4a7c15ull; z = (z ^ (z >> 30)) * 0xbf58476d1ce4e5b9ull; z = (z ^ (z >> 27)) * 0x94d049bb133111ebull; z ^= z >> 31; return z == e ? ~e : z; }
DI void poll(const u64* f, bool act, u64 tag) {
  for (u32 s = 0; s < SPIN_CAP; ++s) {
    bool ok = true;
    if (act) ok = __hip_atomic_load(f, RLX) == tag;
    if (__all(ok)) break;
    __builtin_amdgcn_s_sleep(1);
  }
  __builtin_amdgcn_fence(__ATOMIC_ACQUIRE, "wavefront");
}
#define TS(n) do { if (threadIdx.x == 0 && (n) != 10) { ((u64*)(ws + (24u << 20)))[blockIdx.x * 16 + (n)] = __builtin_amdgcn_s_memrealtime(); } } while (0)
#define TL(n)
#define TM(n) TS(n)
#define HAUX 0
#define KMASK 31
struct cf { float r, i; };
DI cf cmul(cf a, cf b) { return {a.r * b.r - a.i * b.i, a.r * b.i + a.i * b.r}; }
DI cf cfma(cf a, cf b, cf c) { return {fmaf(a.r, b.r, fmaf(-a.i, b.i, c.r)), fmaf(a.r, b.i, fmaf(a.i, b.r, c.i))}; }

DI int pair_row(int r) { const int kk = r >> 1; return (r & 1) ? (kk ? 256 - kk : 128) : kk; }
DI void hartley_load(u32x4 (&f)[2][4], int kcol, int lane) {
  const u16* p0 = g_htab.v + kcol * 64 + 8 * (lane >> 5);
  f[0][0] = ald4<0>(p0); f[0][1] = ald4<32>(p0); f[0][2] = ald4<64>(p0); f[0][3] = ald4<96>(p0);
}
#define TRL(i, o) "ds_read_b64_tr_b16 %" #i ", %16 offset:" #o "\n\t"
template <bool X16> DI f32x16 hartley_mma(const char* sa, u32x4 (&f)[2][4], int kcol, int lane, int wn) {
#pragma unroll
  for (int i = 0; i < 4; ++i) {
    PIN(f[0][i]);
#pragma unroll
    for (int e = 0; e < 4; ++e) { const u32 o = X16 ? __builtin_amdgcn_ds_swizzle(f[0][i][e], 0x401f) : __builtin_amdgcn_mov_dpp(f[0][i][e], 0xB1, 0xf, 0xf, true); f[1][i][e] = (kcol & 1) ? o : f[0][i][e]; }
  }
  const int h = lane >> 5, blk = (lane >> 4) & 1, q = (lane & 15) >> 2, p = lane & 3;
  const int pp = ((p & 1) << 1) | (p >> 1);
  const u32 ap = (u32)(uintptr_t)(LDSP const char*)(sa + (8 * h + q) * 128 + 16 * ((4 * blk + 2 * wn + (pp >> 1)) ^ (q & 2)) + 8 * (pp & 1));
  f32x16 acc = {};
#pragma unroll
  for (int hf = 0; hf < 2; ++hf) {
    u32x2 t[16];
    asm volatile(TRL(0, 0) TRL(1, 512) TRL(2, 2048) TRL(3, 2560) TRL(4, 4096) TRL(5, 4608) TRL(6, 6144) TRL(7, 6656)
                 TRL(8, 8192) TRL(9, 8704) TRL(10, 10240) TRL(11, 10752) TRL(12, 12288) TRL(13, 12800) TRL(14, 14336) TRL(15, 14848) "s_waitcnt lgkmcnt(0)"
                 : "=&v"(t[0]), "=&v"(t[1]), "=&v"(t[2]), "=&v"(t[3]), "=&v"(t[4]), "=&v"(t[5]), "=&v"(t[6]), "=&v"(t[7]),
                   "=&v"(t[8]), "=&v"(t[9]), "=&v"(t[10]), "=&v"(t[11]), "=&v"(t[12]), "=&v"(t[13]), "=&v"(t[14]), "=&v"(t[15])
                 : "v"(ap + hf * 16384) : "memory");
#pragma unroll
    for (int i = 0; i < 8; ++i) {
      const int qq = 2 * hf + (i >> 2);
      const u32 sg = ((kcol * qq) & 2) ? 0x80008000u : 0u;
      acc = mma32(cat(t[2 * i], t[2 * i + 1]), __builtin_bit_cast(bf16x8, f[qq & 1][i & 3] ^ sg), acc);
    }
  }
  return acc;
}

struct TileD { int k, c, src; float w; };
DI TileD tile_desc(int tau, int k1, int kd1) {
  TileD t; const int i = tau >> 1, ka = tau & 1;
  t.c = kd1 + 2 * ka;
  if (k1 != 0) { t.k = k1 + 256 * i; t.src = -1; t.w = tau < 16 ? 1.f : 0.f; }
  else if (kd1) { t.k = 128 * ka + 256 * i; t.src = ka; t.w = tau < 16 ? 2.f : 0.f; }
  else { t.src = i >= 5; t.k = i >= 5 ? 128 + 256 * (i - 5) : 256 * i; t.w = tau >= 18 ? 0.f : ((i == 0 || i == 4) ? 1.f : 2.f); }
  return t;
}
DI float m0val(const TileD& t, int ri, int dl, int s2, int rs) {
  float a = (float)(s2 * t.k) * (-1.f / 2048.f) - 0.25f * (float)(dl * t.c + ri);
  if (t.src < 0) return cosr(a + (rs ? 0.125f : -0.125f));
  return (t.w == 0.f || t.src != rs) ? 0.f : cosr(a);
}
DI void m0_fwd(bf16x8& o, int n, int l15, int g, int k1, int kd1) {
  const TileD t = tile_desc(8 * n + (l15 >> 1), k1, kd1); f32x4 v0, v1;
#pragma unroll
  for (int j = 0; j < 4; ++j) { v0[j] = m0val(t, l15 & 1, 0, 2 * g + (j >> 1), j & 1); v1[j] = m0val(t, l15 & 1, 1, 2 * g + (j >> 1), j & 1); }
  o = pk8(v0, v1);
}
DI void m0_inv(bf16x8& o, int ks, int dl, int l15, int g, int k1, int kd1) {
  f32x4 v[2];
#pragma unroll
  for (int j = 0; j < 8; ++j) { const int pn = 32 * ks + 16 * (j >> 2) + 4 * g + (j & 3);
    v[j >> 2][j & 3] = m0val(tile_desc(pn >> 1, k1, kd1), pn & 1, dl, l15 >> 1, l15 & 1); }
  o = pk8(v[0], v[1]);
}

__global__ __launch_bounds__(1024) void k_front(const u16* __restrict__ x, const u16* __restrict__ wr, const u16* __restrict__ wi,
    const u16* __restrict__ lam_re, const u16* __restrict__ lam_im, const u16* __restrict__ b_re, const u16* __restrict__ b_im,
    const u16* __restrict__ log_dt, const u16* __restrict__ gate_w, const u16* __restrict__ c_re, const u16* __restrict__ c_im, char* __restrict__ ws) {
  extern __shared__ __attribute__((aligned(16))) char lds[];
  const int tid = threadIdx.x & 511, lane = tid & 63, w = __builtin_amdgcn_readfirstlane(tid >> 6), wn = w & 1, wm = w >> 1;
  const bool loader = __builtin_amdgcn_readfirstlane(threadIdx.x >> 9);
  const int bid = blockIdx.x, xcd = bid & 7, jj = bid >> 3;
  const int tt = (xcd >> 1) * 4 + (jj & 3), pt = (xcd & 1) * 8 + (jj >> 2);
  const int a_s2 = xcd, a_ct = jj >> 1, a_half = jj & 1;
  const int k1 = xcd * 16 + (jj >> 1), kd1 = jj & 1;
  u64* fa = (u64*)(ws + WS_FA);
  const rsrc_t ursrc = mkr(ws + WS_U, 4 << 20), xr = mkr(x, 4 << 20), br = mkr((w & 4) ? b_im : b_re, 2 << 20);

  TS(0);
  u64 tag = ald2c(ws + WS_E);
  const int l15 = lane & 15;
  if (loader) {
  const int lr = lane >> 3, lc = lane & 7;
  { const int r = 8 * w + lr, c = lc ^ (r & 2);
#pragma unroll
    for (int i = 0; i < 4; ++i) blds(xr, (a_s2 + 8 * r) * 2048 + 64 * a_ct + 16 * (c & 3) + 1024 * (c >> 2), i << 20, lds + 3 * 32768 + i * 8192 + w * 1024); }
  u32 vX, vB;
  { const int r = 8 * w + lr, c = lc ^ ((r >> 1) & 7);
    const int m = r & 31, tp = (r & ~31) + 16 * ((m >> 2) & 1) + (m & 3) + 4 * (m >> 3);
    vX = (128 * tt + tp) * 2048 + 16 * c; vB = (64 * pt + m) * 2048 + 16 * c; }
  auto issue = [&](int j, int i) {
    char* l = lds + (j & 3) * 32768 + i * 8192 + w * 1024;
    if (i < 2) blds(xr, vX, 128 * (j & KMASK) + i * 64 * 2048, l); else blds(br, vB, 128 * (j & KMASK) + (i - 2) * 32 * 2048, l);
  };
  auto wrow = [&](int slot) { return k1 ? (slot < 8 ? k1 + 256 * slot : 2048 - k1 - 256 * (slot - 8)) : 128 * slot; };
  auto issue_w = [&](int im, int i) {
    const int id = min(512 * i + tid, 1055), slot = id / 66;
    const int o = min(((wrow(slot) * 1026) & ~15) + 16 * (id - 66 * slot), 2048 * 1026 - 16);
    if (i < 2 || w == 0) glds((const char*)(im ? wi : wr) + o, lds + 32768 + im * 32768 + i * 8192 + w * 1024);
  };
  issue(0, 0); issue(0, 1); issue(0, 2); issue(0, 3);
  WAITV(4); BAR();
  PIN(tag); tag = mix64(tag);
#pragma unroll
  for (int i = 0; i < 8; ++i) issue(1 + (i >> 2), i & 3);
  const int g = lane >> 4, cc = kd1 + 2 * wn;
  const bool sp0 = (k1 == 0 && kd1 == 0);
  bf16x8 m0[2], mi[2], m0x, mix[2], fa_r, fa_i, fb_r, fb_i, gb_r, gb_i, ga_r, ga_i; f32x4 t1c, t1s, t2c, t2s;
  u32x4 q0, q1; u64 fl0, fl1;
  const float fl = (float)l15, fc = (float)cc;
  SB();
#pragma unroll
  for (int e = 0; e < 4; ++e) { const float a = (float)(4 * g + e) * (fc + 4.f * fl) * (-1.f / 64.f); t1c[e] = cosr(a); t1s[e] = sinr(a); }
  fa_r = pk8(t1c, -t1s); fa_i = pk8(t1s, t1c); PIN(fa_r); PIN(fa_i);
#pragma unroll
  for (int e = 0; e < 4; ++e) { const float a = (float)(4 * g + e) * fl * (-1.f / 16.f); t1c[e] = cosr(a); t1s[e] = sinr(a); }
  fb_r = pk8(t1c, -t1s); fb_i = pk8(t1s, t1c); gb_r = pk8(t1c, t1s); gb_i = pk8(-t1s, t1c); PIN(fb_r); PIN(fb_i); PIN(gb_r); PIN(gb_i);
#pragma unroll
  for (int e = 0; e < 4; ++e) { const float a = fl * (fc + 4.f * (float)(4 * g + e)) * (1.f / 64.f); t1c[e] = cosr(a); t1s[e] = sinr(a); }
  ga_r = pk8(t1c, -t1s); ga_i = pk8(t1s, t1c); PIN(ga_r); PIN(ga_i);
#pragma unroll
  for (int e = 0; e < 4; ++e) {
    const float fv = (float)(4 * g + e), a = fv * (fc + 4.f * fl) * (-1.f / 1024.f), b = fl * (fc + 4.f * fv) * (1.f / 1024.f);
    t1c[e] = cosr(a); t1s[e] = sinr(a); t2c[e] = cosr(b); t2s[e] = sinr(b);
  }
  PIN(t1c); PIN(t1s); PIN(t2c); PIN(t2s);
  const u32 ub = (u32)(2 * k1 + kd1) * 16384u;
    char* ui_ = lds; char* vi = lds + 135168;
    const int q = l15 >> 2, pq = lane & 3;
  const f32x4 z4 = {};
#pragma unroll
  for (int j = 0; j < 16; ++j) {
    if (j < 14) WAITV(8); else if (j == 14) WAITV(4); else WAITV(2);
    LBAR();
    if (j == 3 && tid == 0) __hip_atomic_store(fa + a_half * 128 + a_s2 * 16 + a_ct, tag, RLX);
    if (j == 13 && sp0) m0_fwd(m0x, 2, l15, g, 0, 0);
    if (j == 13) { if (k1) { m0_fwd(m0[0], 0, l15, g, k1, kd1); m0_fwd(m0[1], 1, l15, g, k1, kd1); } else { m0_fwd(m0[0], 0, l15, g, 0, kd1); m0_fwd(m0[1], 1, l15, g, 0, kd1); } PIN(m0[0]); PIN(m0[1]); }
    SB();
    if (j == 13) {
    *(u32x4*)(ui_ + w * 1056 + lane * 16) = q0; *(u32x4*)(ui_ + (8 + w) * 1056 + lane * 16) = q1;
    }
    if (j == 14)
    { const char* ap = ui_ + (4 * g + q) * 1056 + 64 * w + 16 * pq;
      bf16x8 a0 = cat(trr(ap), trr(ap + 512)), a1 = cat(trr(ap + 8), trr(ap + 520));
#pragma unroll
      for (int n = 0; n < 3; ++n) if (n < 2 || sp0) {
        const bf16x8 mm = n < 2 ? m0[n & 1] : m0x;
        bf16x8 o = pk8(mma16(a0, mm, z4), mma16(a1, mm, z4));
        if (n < 2 || l15 < 4) *(bf16x8*)(vi + (16 * n + l15) * 544 + 64 * w + 16 * g) = o;
      }
    }
    if (j == 12) {
      PIN(fl0); PIN(fl1);
      if (!__all(fl0 == tag && fl1 == tag)) { poll(fa + (k1 >> 6) * 128 + lane, true, tag); poll(fa + (k1 >> 6) * 128 + 64 + lane, true, tag); }
      __builtin_amdgcn_fence(__ATOMIC_ACQUIRE, "wavefront");
      q0 = __builtin_amdgcn_raw_buffer_load_b128(ursrc, ub + tid * 16, 0, 16); q1 = __builtin_amdgcn_raw_buffer_load_b128(ursrc, ub + 8192 + tid * 16, 0, 16);
    }
    SB();
    if (j < 13) { issue(j + 3, 0); issue(j + 3, 1); issue(j + 3, 2); issue(j + 3, 3); }
    else if (j > 13) { issue_w(j - 14, 0); issue_w(j - 14, 1); issue_w(j - 14, 2); }
    SB();
    if (j == 9) { fl0 = ald2c(fa + (k1 >> 6) * 128 + lane); fl1 = ald2c(fa + (k1 >> 6) * 128 + 64 + lane); }
    SB();
  }
  {
    if (k1) { m0_inv(mi[0], 0, 0, l15, g, k1, kd1); m0_inv(mi[1], 0, 1, l15, g, k1, kd1); } else { m0_inv(mi[0], 0, 0, l15, g, 0, kd1); m0_inv(mi[1], 0, 1, l15, g, 0, kd1); }
    PIN(mi[0]); PIN(mi[1]);
    if (sp0) { m0_inv(mix[0], 1, 0, l15, g, 0, 0); m0_inv(mix[1], 1, 1, l15, g, 0, 0); }
    SB(); WAITV(0);
    LBAR();
#pragma unroll
    for (int i = 0; i < 3; ++i) if (i < 2 || (sp0 && wm == 0)) {
      const int tau = wn + 2 * (wm + 4 * i);
      const TileD td = tile_desc(tau, k1, kd1);
      const float sc = td.w * (1.0f / 2097152.0f);
      const int nk = (2048 - td.k) & 2047;
      const char* wp = lds + 32768 + (k1 ? td.k >> 8 : td.k >> 7) * 1056 + ((td.k * 1026) & 15);
      const char* wq = lds + 32768 + (k1 ? 8 + (td.k >> 8) : nk >> 7) * 1056 + ((nk * 1026) & 15);
      f32x4 fr, fi;
#pragma unroll
      for (int r = 0; r < 4; ++r) {
        const int kd = cc + 4 * l15 + 64 * (4 * g + r);
        const char* a = kd <= 512 ? wp + 2 * kd : wq + 2 * (1024 - kd);
        fr[r] = bf2f(*(const u16*)a) * sc; fi[r] = bf2f(*(const u16*)(a + 32768)) * (kd > 512 ? -sc : sc);
        if (r == 0 && cc == 0) {
          const float er = bf2f(*(const u16*)(wq + 2 * (kd & 512))) * sc, ei = bf2f(*(const u16*)(wq + 32768 + 2 * (kd & 512))) * sc;
          if ((kd & 511) == 0) { fr[0] = 0.5f * (fr[0] + er); fi[0] = 0.5f * (fi[0] - ei); }
        }
      }
      char* pl = vi + tau * 1088;
      bf16x8 a = cat(trr(pl + (4 * g + q) * 32 + 8 * pq), trr(pl + 544 + (4 * g + q) * 32 + 8 * pq));
      f32x4 yr = mma16(a, fa_r, z4), yi = mma16(a, fa_i, z4);
      a = pk8(yr * t1c - yi * t1s, yr * t1s + yi * t1c);
      yr = mma16(fb_r, a, z4); yi = mma16(fb_i, a, z4);
      a = pk8(yr * fr - yi * fi, yr * fi + yi * fr);
      yr = mma16(a, gb_r, z4); yi = mma16(a, gb_i, z4);
      a = pk8(yr * t2c - yi * t2s, yr * t2s + yi * t2c);
      u32x4 o = __builtin_bit_cast(u32x4, pk8(mma16(a, ga_r, z4), mma16(a, ga_i, z4)));
      *(u32x2*)(pl + 32 * l15 + 8 * g) = u32x2{o.x, o.y}; *(u32x2*)(pl + 544 + 32 * l15 + 8 * g) = u32x2{o.z, o.w};
    }
    LBAR();
    f32x4 o[2][2] = {};
#pragma unroll
    for (int ks = 0; ks < 2; ++ks) if (ks == 0 || sp0) {
      const char* ap = vi + (ks ? 32 + q : 4 * g + q) * 544 + 64 * w + 16 * pq; const int st = ks ? 0 : 16 * 544;
      bf16x8 a0 = cat(trr(ap), trr(ap + st)), a1 = cat(trr(ap + 8), trr(ap + st + 8));
#pragma unroll
      for (int dl = 0; dl < 2; ++dl) { const bf16x8 mm = ks ? mix[dl] : mi[dl]; o[dl][0] = mma16(a0, mm, o[dl][0]); o[dl][1] = mma16(a1, mm, o[dl][1]); }
    }
#pragma unroll
    for (int dl = 0; dl < 2; ++dl) *(bf16x8*)(ws + WS_T + ub + l15 * 1024 + 512 * dl + 64 * w + 16 * g) = pk8(o[dl][0], o[dl][1]);
  }
  if (tid == 0) ((u64*)(ws + (24u << 20)))[blockIdx.x * 16 + 6] = __builtin_amdgcn_s_memrealtime();
  } else {
  const int kcol = pair_row(128 * a_half + 32 * wm + (lane & 31));
  u32x4 hf[2][4];
  hartley_load(hf, kcol, lane);
  const int p = 64 * pt + 32 * wn + (lane & 31), seg = 2 * wm + (lane >> 5);
  u32 q_dt = aldh(log_dt + p), q_lre = aldh(lam_re + p), q_lim = aldh(lam_im + p);
  {
    WAITV(0); BAR();
    TL(3);
    PIN(tag); PIN(q_dt); PIN(q_lre); PIN(q_lim);
    const f32x16 acc = hartley_mma<false>(lds + 3 * 32768, hf, kcol, lane, wn);
    const int r = 128 * a_half + 32 * wm + (lane & 31);
    const u32 off = (u32)(((r & ~1) * 16 + 2 * a_s2 + (r & 1)) * 1024 + 64 * a_ct + 32 * wn + 16 * (lane >> 5));
    u32x4 p0, p1;
#pragma unroll
    for (int i = 0; i < 4; ++i) {
      p0[i] = pk2(acc[2 * i] + acc[8 + 2 * i], acc[2 * i + 1] + acc[9 + 2 * i]);
      p1[i] = pk2(acc[2 * i] - acc[8 + 2 * i], acc[2 * i + 1] - acc[9 + 2 * i]);
    }
    __builtin_amdgcn_raw_buffer_store_b128(p0, ursrc, off, 0, 16);
    __builtin_amdgcn_raw_buffer_store_b128(p1, ursrc, off + 16384, 0, 16);
  }
  TS(1);
  f32x16 ur = {}, ui = {};
  cf lam, coef, l16;
  {
    const int h = lane >> 5, ra = 32 * wm + (lane & 31), rb = 64 * wn + (lane & 31), sw = (lane >> 1) & 7;
    bf16x8 fr[2][12];
    u32 aa[4], ab[4];
#pragma unroll
    for (int ks = 0; ks < 4; ++ks) { const int co = 16 * ((2 * ks + h) ^ sw); aa[ks] = ldsa(lds + ra * 128 + co); ab[ks] = ldsa(lds + rb * 128 + co); }
    tag = mix64(tag);
#pragma unroll
    for (int j = 0; j <= 16; ++j) {
      if (j == 3) WAITV(0);
      if (j < 16) LBAR();
      if (j == 0) TL(4); if (j == 3) TL(5); if (j == 6) TL(6); if (j == 11) TL(7);
      const int st = (j & 1) * 32768, adv = (j & 1) || j == 0 ? 0 : ((j & 2) ? 65536 : -65536);
#pragma unroll
      for (int ks = 0; ks < 4; ++ks) {
        if (!(j & 1)) { aa[ks] += adv; ab[ks] += adv; PIN(aa[ks]); PIN(ab[ks]); }
        if (j > 0) ur = mma32(fr[(j - 1) & 1][3 * ks], fr[(j - 1) & 1][3 * ks + 1], ur);
        SB();
        if (j < 16) { fr[j & 1][3 * ks] = ldsr(aa[ks], st); fr[j & 1][3 * ks + 1] = ldsr(ab[ks], st + 16384); }
        SB();
        if (j > 0) ui = mma32(fr[(j - 1) & 1][3 * ks], fr[(j - 1) & 1][3 * ks + 2], ui);
        SB();
        if (j < 16) fr[j & 1][3 * ks + 2] = ldsr(ab[ks], st + 20480);
        SB();
        if (j == 0 && ks == 3) {
          const float dt = rbf(expf(bf2f(q_dt))), mg = expf(dt * bf2f(q_lre)), an = dt * bf2f(q_lim);
          lam = {mg * cosf(an), mg * sinf(an)}; PIN(lam.r); PIN(lam.i);
        }
        if (j == 1 && ks == 0) {
          const float lre = bf2f(q_lre), lim = bf2f(q_lim), nr = lam.r - 1.f, ni = lam.i;
          const bool big = fabsf(lre) >= fabsf(lim);
          const float u = big ? lre : lim, v = big ? lim : lre, t = v / u, dn = u + v * t;
          coef = {2.f * ((big ? nr + ni * t : nr * t + ni) / dn), 2.f * ((big ? ni - nr * t : ni * t - nr) / dn)};
          l16 = lam;
#pragma unroll
          for (int i = 0; i < 4; ++i) l16 = cmul(l16, l16);
          PIN(coef.r); PIN(coef.i); PIN(l16.r); PIN(l16.i);
        }
        SB();
      }
    }
  }
  TS(2);
  u32x4 gx[2], gw[2];
    gx[0] = ald4<0>(x + (bid * 8 + w) * 1024 + 8 * lane); gx[1] = ald4<1024>(x + (bid * 8 + w) * 1024 + 8 * lane);
    gw[0] = ald4<0>(gate_w + 8 * lane); gw[1] = ald4<1024>(gate_w + 8 * lane);
    u32 warm = 0;
    if (w >= 6) warm = ald1(((w & 1) ? c_im : c_re) + bid * 4096 + 64 * lane);
#pragma unroll
  for (int i = 1; i < 16; ++i) { cf v = cfma(lam, {ur[i - 1], ui[i - 1]}, {ur[i], ui[i]}); ur[i] = v.r; ui[i] = v.i; }
  f32x2* segs = (f32x2*)(lds + 131072) + 32 * wn + (lane & 31);
  segs[seg * 64] = f32x2{ur[15], ui[15]};
  LBAR();
  const rsrc_t ar = mkr(ws + WS_AGG, 1 << 18);
  if (wm == 3 && tt < 15) {
    cf c = {0.f, 0.f};
#pragma unroll
    for (int j = 0; j < 8; ++j) { f32x2 e = segs[j * 64]; c = cfma(l16, c, {e.x, e.y}); }
    if (lane < 32) __builtin_amdgcn_raw_buffer_store_b128(u32x4{__builtin_bit_cast(u32, c.r), __builtin_bit_cast(u32, c.i), (u32)tag, (u32)(tag >> 32)}, ar, p * 16, tt * 16384, 16);
  }
  TM(3);
  {
    cf c = {0.f, 0.f}, lseg = coef;
#pragma unroll
    for (int j = 0; j < 7; ++j) { f32x2 e = segs[j * 64]; cf n = cfma(l16, c, {e.x, e.y}), m = cmul(lseg, l16); if (j < seg) { c = n; lseg = m; } }
    c = cmul(c, coef);
#pragma unroll
    for (int i = 0; i < 16; ++i) { const cf v = cmul(coef, {ur[i], ui[i]}); ur[i] = v.r; ui[i] = v.i; }
    PIN(ur); PIN(ui);
    {
      if (w >= 6) WAITV(1); else WAITV(0);
      float s = 0.f;
      PIN(gx[0]); PIN(gx[1]); PIN(gw[0]); PIN(gw[1]);
#pragma unroll
      for (int i = 0; i < 2; ++i)
#pragma unroll
        for (int e = 0; e < 4; ++e) {
          const u32 pr = pk2(bf2f(gx[i][e] & 0xffffu) * bf2f(gw[i][e] & 0xffffu), bf2f(gx[i][e] >> 16) * bf2f(gw[i][e] >> 16));
          s += bf2f(pr & 0xffffu) + bf2f(pr >> 16);
        }
#pragma unroll
      for (int o = 32; o > 0; o >>= 1) s += __shfl_xor(s, o);
      if (lane == 0) ((float*)(ws + WS_G))[bid * 8 + w] = rbf(1.f / (1.f + expf(-rbf(s))));
    }
    TM(4);
    if (tt > 0) {
      cf l128 = cmul(l16, l16); l128 = cmul(l128, l128); l128 = cmul(l128, l128);
      const cf l256 = cmul(l128, l128);
      const int j0 = tt - 1 - (lane >> 5);
      u32x4 v[8];
      for (u32 sp = 0; sp < SPIN_CAP; ++sp) {
        bool ok = true;
#pragma unroll
        for (int n = 0; n < 8; ++n) if (2 * n < tt) v[n] = __builtin_amdgcn_raw_buffer_load_b128(ar, max(j0 - 2 * n, 0) * 16384 + p * 16, 0, 16);
#pragma unroll
        for (int n = 0; n < 8; ++n) if (2 * n < tt) ok = ok && v[n].z == (u32)tag && v[n].w == (u32)(tag >> 32);
        if (__all(ok)) break;
        asm volatile("s_sleep 1" ::: "memory");
      }
      cf cb = {0.f, 0.f};
#pragma unroll
      for (int n = 7; n >= 0; --n) if (2 * n < tt) {
        const bool in = j0 - 2 * n >= 0;
        cb = cfma(l256, cb, {__uint_as_float(in ? v[n].x : 0u), __uint_as_float(in ? v[n].y : 0u)});
      }
      if (lane >= 32) cb = cmul(cb, l128);
      cb.r += __shfl_xor(cb.r, 32); cb.i += __shfl_xor(cb.i, 32);
      c = cfma(lseg, cb, c);
    }
    TM(5);
    WAITV(0); PIN(warm);
    BAR();
    const rsrc_t hr = mkr(ws + WS_H, 8 << 20);
    u32* tw = (u32*)(lds + 98304 + w * 4096) + (lane & 1) * 64 + ((lane >> 3) & 3) * 16 + (lane >> 5) * 8 + ((lane >> 1) & 3);
#pragma unroll
    for (int i = 0; i < 16; ++i) {
      c = cmul(c, lam);
      const u32 own = pk2(ur[i] + c.r, -(ui[i] + c.i)), oth = __builtin_amdgcn_mov_dpp(own, 0xB1, 0xf, 0xf, true);
      tw[(i & 7) * 128 + (i >> 3) * 4] = __builtin_amdgcn_perm(oth, own, (lane & 1) ? 0x03020706u : 0x05040100u);
    }
    const int k = 16 * tt + 4 * wm + (lane & 3), par = k >= 128, kk = par ? (256 - k) & 127 : k;
    const u32 ho = ((lane >> 5) << 20) + (kk >> 4) * 131072 + (2 * pt + ((lane >> 4) & 1)) * 4096 + (4 * wn + ((lane >> 2) & 3)) * 512 + par * 256 + (kk & 15) * 16;
#pragma unroll
    for (int r = 0; r < 4; ++r) __builtin_amdgcn_raw_buffer_store_b128(*(const u32x4*)(lds + 98304 + w * 4096 + r * 1024 + lane * 16), hr, ho, r << 21, HAUX);
  }
  }
  TM(7);
  if (bid == 1 && threadIdx.x < 256) {
    poll(fa + tid, true, tag);
  }
  if (bid == 1) { LBAR(); if (threadIdx.x == 0) __hip_atomic_store((u64*)(ws + WS_E), tag, RLX); }
}

__global__ __launch_bounds__(768) void k_back(const u16* __restrict__ x, const u16* __restrict__ c_re, const u16* __restrict__ c_im,
    const u16* __restrict__ dskip, const char* __restrict__ ws, float* __restrict__ out) {
  extern __shared__ __attribute__((aligned(16))) char lds[];
  const int tid = threadIdx.x & 511, lane = tid & 63, w = __builtin_amdgcn_readfirstlane(tid >> 6), wn = w & 1, wm = w >> 1;
  const bool loader = __builtin_amdgcn_readfirstlane(threadIdx.x >> 9);
  const int bid = blockIdx.x, xcd = bid & 7, jj = bid >> 3;
  const int rt = (xcd >> 1) * 4 + (jj & 3), ct = (xcd & 1) * 8 + (jj >> 2), s2 = rt >> 1, sh = rt & 1;
  const rsrc_t tr = mkr(ws + WS_T, 4 << 20), cr0 = mkr(c_re, 2 << 20), cr1 = mkr(c_im, 2 << 20);
  TS(8);
  if (loader) {
    const int lr = lane >> 3, lc = lane & 7, c = lc ^ (lr & 2);
#pragma unroll
    for (int i = 0; i < 8; ++i) { const int k = 32 * i + 8 * w + lr, kb = k < 128 ? k : (256 - k) & 127;
      blds(tr, kb * 32768 + (c >> 2) * 16384 + 16 * (c & 3), s2 * 2048 + 64 * ct + (i >= 4 ? 1024 : 0), lds + i * 4096 + w * 1024); }
    const int ra0 = 8 * w + lr, m = ra0 & 15, d = 32 * ct + 512 * ((ra0 >> 4) & 1) + 8 * ((m >> 2) & 1) + 4 * (m >> 3) + (m & 3);
    const u32 vC = d * 2048 + 16 * (lc ^ ((ra0 >> 1) & 7));
    auto issue = [&](int j) {
#pragma unroll
      for (int i = 0; i < 4; ++i) blds((i & 2) ? cr1 : cr0, vC, 128 * j + (i & 1) * 32768, lds + 32768 + (j % 7) * 16384 + i * 4096 + w * 1024);
    };
    issue(0);
    WAITV(4); BAR();
#pragma unroll
    for (int j = 1; j < 6; ++j) issue(j);
#pragma unroll
    for (int j = 0; j < 16; ++j) {
      if (j < 11) WAITV(20); else if (j == 11) WAITV(16); else if (j == 12) WAITV(12); else if (j == 13) WAITV(8); else if (j == 14) WAITV(4); else WAITV(0);
      BAR();
      if (j < 10) issue(j + 6);
    }
    BAR();
    return;
  }
  const int h = lane >> 5, kcol = pair_row(128 * sh + 32 * wm + 2 * (lane & 15) + ((lane >> 4) & 1)), srow = s2 + 8 * kcol, d0 = 32 * ct + 16 * wn + 8 * h;
  u32x4 hf[2][4];
  hartley_load(hf, kcol, lane);
  const char* hq = ws + WS_H + (size_t)(s2 * 8 + sh * 4 + wm) * 131072 + wn * 4096 + lane * 16;
  u32x4 hv[4][4];
  auto hload = [&](int j) { const char* a = hq + j * 8192; hv[j & 3][0] = ald4<0>(a); hv[j & 3][1] = ald4<1024>(a); hv[j & 3][2] = ald4<2048>(a); hv[j & 3][3] = ald4<3072>(a); };
  hload(0); hload(1);
  u32 ap[8];
  {
    WAITV(8); BAR();
    const f32x16 acc = hartley_mma<true>(lds, hf, kcol, lane, wn);
#pragma unroll
    for (int i = 0; i < 4; ++i) {
      ap[i] = pk2(acc[2 * i] + acc[8 + 2 * i], acc[2 * i + 1] + acc[9 + 2 * i]);
      ap[4 + i] = pk2(acc[2 * i] - acc[8 + 2 * i], acc[2 * i + 1] - acc[9 + 2 * i]);
    }
  }
  TS(9);
  hload(2);
  f32x16 acc = {}, acx = {};
  float gv; u32x4 xv[2], dv[2];
  {
    const int sw = (lane >> 1) & 7;
    bf16x8 fr[2][8];
    u32 aa[4], ab[4];
#pragma unroll
    for (int ks = 0; ks < 4; ++ks) { aa[ks] = ldsa(lds + 32768 + wn * 8192 + (lane & 31) * 128 + 16 * ((2 * ks + h) ^ sw)); ab[ks] = aa[ks] + 4096 * (1 - wn); aa[ks] += 4096 * wn; }
#pragma unroll
    for (int j = 0; j <= 16; ++j) {
      if (j < 16) LBAR();
      if (j > 0) {
        if (j < 15) WAITV(8); else if (j == 15) WAITV(9); else WAITV(5);
#pragma unroll
        for (int ks = 0; ks < 4; ++ks) PIN(hv[(j - 1) & 3][ks]);
        if (j < 14) hload(j + 2);
      }
      if (j == 14) {
        gv = __builtin_bit_cast(float, ald1((const float*)(ws + WS_G) + srow));
        xv[0] = ald4<0>(x + srow * 1024 + d0); xv[1] = ald4<1024>(x + srow * 1024 + d0); dv[0] = ald4<0>(dskip + d0); dv[1] = ald4<1024>(dskip + d0);
      }
      const int st = ((j % 7) % 3) * 16384, adv = j % 7 == 0 ? (j ? -98304 : 0) : ((j % 7) % 3 ? 0 : 49152);
#pragma unroll
      for (int ks = 0; ks < 4; ++ks) {
        if (adv || j == 0) { aa[ks] += adv; ab[ks] += adv; PIN(aa[ks]); PIN(ab[ks]); }
        if (j > 0) acc = mma32(fr[(j - 1) & 1][2 * ks], __builtin_bit_cast(bf16x8, hv[(j - 1) & 3][ks]), acc);
        SB();
        if (j < 16) fr[j & 1][2 * ks] = ldsr(aa[ks], st);
        SB();
        if (j > 0) acx = mma32(fr[(j - 1) & 1][2 * ks + 1], __builtin_bit_cast(bf16x8, hv[(j - 1) & 3][ks]), acx);
        SB();
        if (j < 16) fr[j & 1][2 * ks + 1] = ldsr(ab[ks], st);
        SB();
      }
    }
#pragma unroll
    for (int i = 0; i < 4; ++i) *(f32x4*)(lds + (w ^ 1) * 4096 + i * 1024 + lane * 16) = f32x4{acx[4 * i], acx[4 * i + 1], acx[4 * i + 2], acx[4 * i + 3]};
    LBAR();
#pragma unroll
    for (int i = 0; i < 4; ++i) { const f32x4 v = *(const f32x4*)(lds + w * 4096 + i * 1024 + lane * 16); acc[4 * i] += v[0]; acc[4 * i + 1] += v[1]; acc[4 * i + 2] += v[2]; acc[4 * i + 3] += v[3]; }
    WAITV(0); PIN(gv); PIN(xv[0]); PIN(xv[1]); PIN(dv[0]); PIN(dv[1]);
  }
  TS(10);
  const float og = rbf(1.f - gv);
#pragma unroll
  for (int i = 0; i < 2; ++i) {
    float o[8];
#pragma unroll
    for (int e = 0; e < 4; ++e) {
      const u32 sk = pk2(bf2f(xv[i][e] & 0xffffu) * bf2f(dv[i][e] & 0xffffu), bf2f(xv[i][e] >> 16) * bf2f(dv[i][e] >> 16));
      const u32 ga = pk2(gv * bf2f(ap[4 * i + e] & 0xffffu), gv * bf2f(ap[4 * i + e] >> 16));
      o[2 * e] = bf2f(ga & 0xffffu) + og * (acc[8 * i + 2 * e] + bf2f(sk & 0xffffu));
      o[2 * e + 1] = bf2f(ga >> 16) + og * (acc[8 * i + 2 * e + 1] + bf2f(sk >> 16));
    }
    float* op = out + srow * 1024 + d0 + 512 * i;
    *(f32x4*)op = f32x4{o[0], o[1], o[2], o[3]}; *(f32x4*)(op + 4) = f32x4{o[4], o[5], o[6], o[7]};
  }
  TS(11);
}

__global__ void k_probe(const char* ws) {
  const unsigned long long* t = (const unsigned long long*)(ws + (24u << 20));
  long long lo = 0x7fffffffffffffffll, hi = 0;
  for (int i = threadIdx.x; i < 256; i += 64) { long long x = t[i * 16 + 0], y = t[i * 16 + 0]; lo = min(lo, x); hi = max(hi, y); }
  for (int o = 32; o > 0; o >>= 1) { long long x = __shfl_xor(lo, o), y = __shfl_xor(hi, o); lo = min(lo, x); hi = max(hi, y); }
  long long d = (hi - lo) * 0; if (d < 0) d = 0; if (d > 100000) d = 100000;
  const long long e = __builtin_amdgcn_s_memrealtime() + d;
  while ((long long)__builtin_amdgcn_s_memrealtime() < e) __builtin_amdgcn_s_sleep(1);
}
extern "C" void kernel_launch(void* const* d_in, const int* in_sizes, int n_in, void* d_out, int out_size, void* d_ws, size_t ws_size, hipStream_t stream) {
  (void)in_sizes; (void)n_in; (void)out_size; (void)ws_size;
  (void)hipFuncSetAttribute((const void*)k_front, hipFuncAttributeMaxDynamicSharedMemorySize, LDS1);
  (void)hipFuncSetAttribute((const void*)k_back, hipFuncAttributeMaxDynamicSharedMemorySize, LDS2);
  const u16* const* in = (const u16* const*)d_in;
  k_front<<<256, 1024, LDS1, stream>>>(in[0], in[1], in[2], in[4], in[5], in[6], in[7], in[10], in[12], in[8], in[9], (char*)d_ws);
  k_back<<<256, 768, LDS2, stream>>>(in[0], in[8], in[9], in[11], (const char*)d_ws, (float*)d_out);
  k_probe<<<1, 64, 0, stream>>>((const char*)d_ws);
}
```

```cpp
#include <hip/hip_runtime.h>
#include <hip/hip_bf16.h>
#include <stdint.h>

typedef unsigned short u16;
typedef unsigned int u32;
typedef unsigned long long u64;
typedef __attribute__((ext_vector_type(8))) short s16x8;
typedef __attribute__((ext_vector_type(4))) short s16x4;
typedef __attribute__((ext_vector_type(8))) __bf16 bf16x8;
typedef __attribute__((ext_vector_type(2))) __bf16 bf16x2;
typedef __attribute__((ext_vector_type(2))) float f32x2;
typedef __attribute__((ext_vector_type(4))) float f32x4;
typedef __attribute__((ext_vector_type(16))) float f32x16;
typedef __attribute__((ext_vector_type(4))) u32 u32x4;
typedef __attribute__((ext_vector_type(2))) u32 u32x2;
#define DI __device__ __forceinline__
#define LDSP __attribute__((address_space(3)))
#define GLBP __attribute__((address_space(1)))
#define RLX __ATOMIC_RELAXED, __HIP_MEMORY_SCOPE_AGENT

struct HTab { u16 v[16384]; };
constexpr double c_sin(double x) { double t = x, s = x; for (int i = 1; i < 16; ++i) { t *= -x * x / ((2 * i) * (2 * i + 1)); s += t; } return s; }
constexpr double c_cos(double x) { double t = 1, s = 1; for (int i = 1; i < 16; ++i) { t *= -x * x / ((2 * i - 1) * (2 * i)); s += t; } return s; }
constexpr u16 c_bf16(double d) {
  if (d < 1e-9 && d > -1e-9) return 0;
  u32 b = __builtin_bit_cast(u32, (float)d);
  return (u16)((b + 0x7fffu + ((b >> 16) & 1u)) >> 16);
}
constexpr HTab make_htab() {
  HTab t{}; u16 c[256] = {};
  for (int n = 0; n < 256; ++n) { double x = 2.0 * 3.14159265358979323846 * (n < 128 ? n : n - 256) / 256.0; c[n] = c_bf16(c_sin(x) + c_cos(x)); }
  for (int i = 0; i < 16384; ++i) t.v[i] = c[((i >> 6) * (i & 63)) & 255];
  return t;
}
__device__ __attribute__((aligned(256))) const HTab g_htab = make_htab();

constexpr size_t WS_E = 0, WS_FA = 4096, WS_G = 16384, WS_AGG = 65536;
constexpr size_t WS_U = 1u << 20, WS_T = 5u << 20, WS_H = 9u << 20;
constexpr int LDS1 = 4 * 32768 + 4096 + 36 * 544, LDS2 = 32768 + 7 * 16384;
constexpr u32 SPIN_CAP = 1u << 20;

DI float bf2f(u32 h) { return __builtin_bit_cast(float, h << 16); }
DI u32 pk2(float a, float b) { f32x2 v = {a, b}; return __builtin_bit_cast(u32, __builtin_convertvector(v, bf16x2)); }
DI float rbf(float a) { return bf2f(pk2(a, a) & 0xffffu); }
DI void glds(const void* g, char* l) { __builtin_amdgcn_global_load_lds((const GLBP void*)g, (LDSP void*)l, 16, 0, 0); }
typedef __amdgpu_buffer_rsrc_t rsrc_t;
DI rsrc_t mkr(const void* p, int bytes) { return __builtin_amdgcn_make_buffer_rsrc((void*)p, 0, bytes, 0x00020000); }
DI void blds(rsrc_t r, u32 vo, u32 so, char* l) { __builtin_amdgcn_raw_ptr_buffer_load_lds(r, (LDSP void*)l, 16, vo, so, 0, 0); }
DI bf16x8 ldsr(u32 a, int off) { return *(const LDSP bf16x8*)(uintptr_t)(a + off); }
DI u32 ldsa(const char* p) { return (u32)(uintptr_t)(LDSP const char*)p; }
DI u32x2 trr(const char* p) { return __builtin_bit_cast(u32x2, __builtin_amdgcn_ds_read_tr16_b64_v4i16((LDSP s16x4*)p)); }
DI bf16x8 cat(u32x2 a, u32x2 b) { u32x4 v = {a.x, a.y, b.x, b.y}; return __builtin_bit_cast(bf16x8, v); }
DI bf16x8 pk8(f32x4 a, f32x4 b) { u32x4 v = {pk2(a[0], a[1]), pk2(a[2], a[3]), pk2(b[0], b[1]), pk2(b[2], b[3])}; return __builtin_bit_cast(bf16x8, v); }
DI f32x16 mma32(bf16x8 a, bf16x8 b, f32x16 c) { return __builtin_amdgcn_mfma_f32_32x32x16_bf16(a, b, c, 0, 0, 0); }
DI f32x4 mma16(bf16x8 a, bf16x8 b, f32x4 c) { return __builtin_amdgcn_mfma_f32_16x16x32_bf16(a, b, c, 0, 0, 0); }
DI float cosr(float rev) { return __builtin_amdgcn_cosf(rev); }
DI float sinr(float rev) { return __builtin_amdgcn_sinf(rev); }
template <int O> DI u32x4 ald4(const void* p) { u32x4 v; asm volatile("global_load_dwordx4 %0, %1, off offset:%2" : "=v"(v) : "v"(p), "n"(O) : "memory"); return v; }
DI u32 ald1(const void* p) { u32 v; asm volatile("global_load_dword %0, %1, off" : "=v"(v) : "v"(p) : "memory"); return v; }
DI u32 aldh(const void* p) { u32 v; asm volatile("global_load_ushort %0, %1, off" : "=v"(v) : "v"(p) : "memory"); return v; }
DI u64 ald2c(const void* p) { u64 v; asm volatile("global_load_dwordx2 %0, %1, off sc1" : "=v"(v) : "v"(p) : "memory"); return v; }
#define SB() __builtin_amdgcn_sched_barrier(0)
#define PIN(v) asm volatile("" : "+v"(v))
#define WAITV(n) asm volatile("s_waitcnt vmcnt(" #n ")" ::: "memory")
#define BAR() do { asm volatile("" ::: "memory"); __builtin_amdgcn_s_barrier(); asm volatile("" ::: "memory"); } while (0)
#define LBAR() do { asm volatile("s_waitcnt lgkmcnt(0)" ::: "memory"); __builtin_amdgcn_s_barrier(); asm volatile("" ::: "memory"); } while (0)
DI u64 mix64(u64 e) { u64 z = e + 0x9e3779b97f4a7c15ull; z = (z ^ (z >> 30)) * 0xbf58476d1ce4e5b9ull; z = (z ^ (z >> 27)) * 0x94d049bb133111ebull; z ^= z >> 31; return z == e ? ~e : z; }
DI void poll(const u64* f, bool act, u64 tag) {
  for (u32 s = 0; s < SPIN_CAP; ++s) {
    bool ok = true;
    if (act) ok = __hip_atomic_load(f, RLX) == tag;
    if (__all(ok)) break;
    __builtin_amdgcn_s_sleep(1);
  }
  __builtin_amdgcn_fence(__ATOMIC_ACQUIRE, "wavefront");
}
#define TS(n)
#define TL(n)
#define TM(n) TS(n)
#define HAUX 0
#define KMASK 31
struct cf { float r, i; };
DI cf cmul(cf a, cf b) { return {a.r * b.r - a.i * b.i, a.r * b.i + a.i * b.r}; }
DI cf cfma(cf a, cf b, cf c) { return {fmaf(a.r, b.r, fmaf(-a.i, b.i, c.r)), fmaf(a.r, b.i, fmaf(a.i, b.r, c.i))}; }

DI int pair_row(int r) { const int kk = r >> 1; return (r & 1) ? (kk ? 256 - kk : 128) : kk; }
DI void hartley_load(u32x4 (&f)[2][4], int kcol, int lane) {
  const u16* p0 = g_htab.v + kcol * 64 + 8 * (lane >> 5);
  f[0][0] = ald4<0>(p0); f[0][1] = ald4<32>(p0); f[0][2] = ald4<64>(p0); f[0][3] = ald4<96>(p0);
}
#define TRL(i, o) "ds_read_b64_tr_b16 %" #i ", %16 offset:" #o "\n\t"
template <bool X16> DI f32x16 hartley_mma(const char* sa, u32x4 (&f)[2][4], int kcol, int lane, int wn) {
#pragma unroll
  for (int i = 0; i < 4; ++i) {
    PIN(f[0][i]);
#pragma unroll
    for (int e = 0; e < 4; ++e) { const u32 o = X16 ? __builtin_amdgcn_ds_swizzle(f[0][i][e], 0x401f) : __builtin_amdgcn_mov_dpp(f[0][i][e], 0xB1, 0xf, 0xf, true); f[1][i][e] = (kcol & 1) ? o : f[0][i][e]; }
  }
  const int h = lane >> 5, blk = (lane >> 4) & 1, q = (lane & 15) >> 2, p = lane & 3;
  const int pp = ((p & 1) << 1) | (p >> 1);
  const u32 ap = (u32)(uintptr_t)(LDSP const char*)(sa + (8 * h + q) * 128 + 16 * ((4 * blk + 2 * wn + (pp >> 1)) ^ (q & 2)) + 8 * (pp & 1));
  f32x16 acc = {};
#pragma unroll
  for (int hf = 0; hf < 2; ++hf) {
    u32x2 t[16];
    asm volatile(TRL(0, 0) TRL(1, 512) TRL(2, 2048) TRL(3, 2560) TRL(4, 4096) TRL(5, 4608) TRL(6, 6144) TRL(7, 6656)
                 TRL(8, 8192) TRL(9, 8704) TRL(10, 10240) TRL(11, 10752) TRL(12, 12288) TRL(13, 12800) TRL(14, 14336) TRL(15, 14848) "s_waitcnt lgkmcnt(0)"
                 : "=&v"(t[0]), "=&v"(t[1]), "=&v"(t[2]), "=&v"(t[3]), "=&v"(t[4]), "=&v"(t[5]), "=&v"(t[6]), "=&v"(t[7]),
                   "=&v"(t[8]), "=&v"(t[9]), "=&v"(t[10]), "=&v"(t[11]), "=&v"(t[12]), "=&v"(t[13]), "=&v"(t[14]), "=&v"(t[15])
                 : "v"(ap + hf * 16384) : "memory");
#pragma unroll
    for (int i = 0; i < 8; ++i) {
      const int qq = 2 * hf + (i >> 2);
      const u32 sg = ((kcol * qq) & 2) ? 0x80008000u : 0u;
      acc = mma32(cat(t[2 * i], t[2 * i + 1]), __builtin_bit_cast(bf16x8, f[qq & 1][i & 3] ^ sg), acc);
    }
  }
  return acc;
}

struct TileD { int k, c, src; float w; };
DI TileD tile_desc(int tau, int k1, int kd1) {
  TileD t; const int i = tau >> 1, ka = tau & 1;
  t.c = kd1 + 2 * ka;
  if (k1 != 0) { t.k = k1 + 256 * i; t.src = -1; t.w = tau < 16 ? 1.f : 0.f; }
  else if (kd1) { t.k = 128 * ka + 256 * i; t.src = ka; t.w = tau < 16 ? 2.f : 0.f; }
  else { t.src = i >= 5; t.k = i >= 5 ? 128 + 256 * (i - 5) : 256 * i; t.w = tau >= 18 ? 0.f : ((i == 0 || i == 4) ? 1.f : 2.f); }
  return t;
}
DI float m0val(const TileD& t, int ri, int dl, int s2, int rs) {
  float a = (float)(s2 * t.k) * (-1.f / 2048.f) - 0.25f * (float)(dl * t.c + ri);
  if (t.src < 0) return cosr(a + (rs ? 0.125f : -0.125f));
  return (t.w == 0.f || t.src != rs) ? 0.f : cosr(a);
}
DI void m0_fwd(bf16x8& o, int n, int l15, int g, int k1, int kd1) {
  const TileD t = tile_desc(8 * n + (l15 >> 1), k1, kd1); f32x4 v0, v1;
#pragma unroll
  for (int j = 0; j < 4; ++j) { v0[j] = m0val(t, l15 & 1, 0, 2 * g + (j >> 1), j & 1); v1[j] = m0val(t, l15 & 1, 1, 2 * g + (j >> 1), j & 1); }
  o = pk8(v0, v1);
}
DI void m0_inv(bf16x8& o, int ks, int dl, int l15, int g, int k1, int kd1) {
  f32x4 v[2];
#pragma unroll
  for (int j = 0; j < 8; ++j) { const int pn = 32 * ks + 16 * (j >> 2) + 4 * g + (j & 3);
    v[j >> 2][j & 3] = m0val(tile_desc(pn >> 1, k1, kd1), pn & 1, dl, l15 >> 1, l15 & 1); }
  o = pk8(v[0], v[1]);
}

__global__ __launch_bounds__(1024) void k_front(const u16* __restrict__ x, const u16* __restrict__ wr, const u16* __restrict__ wi,
    const u16* __restrict__ lam_re, const u16* __restrict__ lam_im, const u16* __restrict__ b_re, const u16* __restrict__ b_im,
    const u16* __restrict__ log_dt, const u16* __restrict__ gate_w, const u16* __restrict__ c_re, const u16* __restrict__ c_im, char* __restrict__ ws) {
  extern __shared__ __attribute__((aligned(16))) char lds[];
  const int tid = threadIdx.x & 511, lane = tid & 63, w = __builtin_amdgcn_readfirstlane(tid >> 6), wn = w & 1, wm = w >> 1;
  const bool loader = __builtin_amdgcn_readfirstlane(threadIdx.x >> 9);
  const int bid = blockIdx.x, xcd = bid & 7, jj = bid >> 3;
  const int tt = (xcd >> 1) * 4 + (jj & 3), pt = (xcd & 1) * 8 + (jj >> 2);
  const int a_s2 = xcd, a_ct = jj >> 1, a_half = jj & 1;
  const int k1 = xcd * 16 + (jj >> 1), kd1 = jj & 1;
  u64* fa = (u64*)(ws + WS_FA);
  const rsrc_t ursrc = mkr(ws + WS_U, 4 << 20), xr = mkr(x, 4 << 20), br = mkr((w & 4) ? b_im : b_re, 2 << 20);

  TS(0);
  u64 tag = ald2c(ws + WS_E);
  const int l15 = lane & 15;
  if (loader) {
  const int lr = lane >> 3, lc = lane & 7;
  { const int r = 8 * w + lr, c = lc ^ (r & 2);
#pragma unroll
    for (int i = 0; i < 4; ++i) blds(xr, (a_s2 + 8 * r) * 2048 + 64 * a_ct + 16 * (c & 3) + 1024 * (c >> 2), i << 20, lds + 3 * 32768 + i * 8192 + w * 1024); }
  u32 vX, vB;
  { const int r = 8 * w + lr, c = lc ^ ((r >> 1) & 7);
    const int m = r & 31, tp = (r & ~31) + 16 * ((m >> 2) & 1) + (m & 3) + 4 * (m >> 3);
    vX = (128 * tt + tp) * 2048 + 16 * c; vB = (64 * pt + m) * 2048 + 16 * c; }
  auto issue = [&](int j, int i) {
    char* l = lds + (j & 3) * 32768 + i * 8192 + w * 1024;
    if (i < 2) blds(xr, vX, 128 * (j & KMASK) + i * 64 * 2048, l); else blds(br, vB, 128 * (j & KMASK) + (i - 2) * 32 * 2048, l);
  };
  auto wrow = [&](int slot) { return k1 ? (slot < 8 ? k1 + 256 * slot : 2048 - k1 - 256 * (slot - 8)) : 128 * slot; };
  auto issue_w = [&](int im, int i) {
    const int id = min(512 * i + tid, 1055), slot = id / 66;
    const int o = min(((wrow(slot) * 1026) & ~15) + 16 * (id - 66 * slot), 2048 * 1026 - 16);
    if (i < 2 || w == 0) glds((const char*)(im ? wi : wr) + o, lds + 32768 + im * 32768 + i * 8192 + w * 1024);
  };
  issue(0, 0); issue(0, 1); issue(0, 2); issue(0, 3);
  WAITV(4); BAR();
  PIN(tag); tag = mix64(tag);
#pragma unroll
  for (int i = 0; i < 8; ++i) issue(1 + (i >> 2), i & 3);
  const int g = lane >> 4, cc = kd1 + 2 * wn;
  const bool sp0 = (k1 == 0 && kd1 == 0);
  bf16x8 m0[2], mi[2], m0x, mix[2], fa_r, fa_i, fb_r, fb_i, gb_r, gb_i, ga_r, ga_i; f32x4 t1c, t1s, t2c, t2s;
  u32x4 q0, q1; u64 fl0, fl1;
  const float fl = (float)l15, fc = (float)cc;
  SB();
#pragma unroll
  for (int e = 0; e < 4; ++e) { const float a = (float)(4 * g + e) * (fc + 4.f * fl) * (-1.f / 64.f); t1c[e] = cosr(a); t1s[e] = sinr(a); }
  fa_r = pk8(t1c, -t1s); fa_i = pk8(t1s, t1c); PIN(fa_r); PIN(fa_i);
#pragma unroll
  for (int e = 0; e < 4; ++e) { const float a = (float)(4 * g + e) * fl * (-1.f / 16.f); t1c[e] = cosr(a); t1s[e] = sinr(a); }
  fb_r = pk8(t1c, -t1s); fb_i = pk8(t1s, t1c); gb_r = pk8(t1c, t1s); gb_i = pk8(-t1s, t1c); PIN(fb_r); PIN(fb_i); PIN(gb_r); PIN(gb_i);
#pragma unroll
  for (int e = 0; e < 4; ++e) { const float a = fl * (fc + 4.f * (float)(4 * g + e)) * (1.f / 64.f); t1c[e] = cosr(a); t1s[e] = sinr(a); }
  ga_r = pk8(t1c, -t1s); ga_i = pk8(t1s, t1c); PIN(ga_r); PIN(ga_i);
#pragma unroll
  for (int e = 0; e < 4; ++e) {
    const float fv = (float)(4 * g + e), a = fv * (fc + 4.f * fl) * (-1.f / 1024.f), b = fl * (fc + 4.f * fv) * (1.f / 1024.f);
    t1c[e] = cosr(a); t1s[e] = sinr(a); t2c[e] = cosr(b); t2s[e] = sinr(b);
  }
  PIN(t1c); PIN(t1s); PIN(t2c); PIN(t2s);
  const u32 ub = (u32)(2 * k1 + kd1) * 16384u;
    char* ui_ = lds; char* vi = lds + 135168;
    const int q = l15 >> 2, pq = lane & 3;
  const f32x4 z4 = {};
#pragma unroll
  for (int j = 0; j < 16; ++j) {
    if (j < 14) WAITV(8); else if (j == 14) WAITV(4); else WAITV(2);
    LBAR();
    if (j == 3 && tid == 0) __hip_atomic_store(fa + a_half * 128 + a_s2 * 16 + a_ct, tag, RLX);
    if (j == 13 && sp0) m0_fwd(m0x, 2, l15, g, 0, 0);
    if (j == 13) { if (k1) { m0_fwd(m0[0], 0, l15, g, k1, kd1); m0_fwd(m0[1], 1, l15, g, k1, kd1); } else { m0_fwd(m0[0], 0, l15, g, 0, kd1); m0_fwd(m0[1], 1, l15, g, 0, kd1); } PIN(m0[0]); PIN(m0[1]); }
    SB();
    if (j == 13) {
    *(u32x4*)(ui_ + w * 1056 + lane * 16) = q0; *(u32x4*)(ui_ + (8 + w) * 1056 + lane * 16) = q1;
    }
    if (j == 14)
    { const char* ap = ui_ + (4 * g + q) * 1056 + 64 * w + 16 * pq;
      bf16x8 a0 = cat(trr(ap), trr(ap + 512)), a1 = cat(trr(ap + 8), trr(ap + 520));
#pragma unroll
      for (int n = 0; n < 3; ++n) if (n < 2 || sp0) {
        const bf16x8 mm = n < 2 ? m0[n & 1] : m0x;
        bf16x8 o = pk8(mma16(a0, mm, z4), mma16(a1, mm, z4));
        if (n < 2 || l15 < 4) *(bf16x8*)(vi + (16 * n + l15) * 544 + 64 * w + 16 * g) = o;
      }
    }
    if (j == 12) {
      PIN(fl0); PIN(fl1);
      if (!__all(fl0 == tag && fl1 == tag)) { poll(fa + (k1 >> 6) * 128 + lane, true, tag); poll(fa + (k1 >> 6) * 128 + 64 + lane, true, tag); }
      __builtin_amdgcn_fence(__ATOMIC_ACQUIRE, "wavefront");
      q0 = __builtin_amdgcn_raw_buffer_load_b128(ursrc, ub + tid * 16, 0, 16); q1 = __builtin_amdgcn_raw_buffer_load_b128(ursrc, ub + 8192 + tid * 16, 0, 16);
    }
    SB();
    if (j < 13) { issue(j + 3, 0); issue(j + 3, 1); issue(j + 3, 2); issue(j + 3, 3); }
    else if (j > 13) { issue_w(j - 14, 0); issue_w(j - 14, 1); issue_w(j - 14, 2); }
    SB();
    if (j == 9) { fl0 = ald2c(fa + (k1 >> 6) * 128 + lane); fl1 = ald2c(fa + (k1 >> 6) * 128 + 64 + lane); }
    SB();
  }
  {
    if (k1) { m0_inv(mi[0], 0, 0, l15, g, k1, kd1); m0_inv(mi[1], 0, 1, l15, g, k1, kd1); } else { m0_inv(mi[0], 0, 0, l15, g, 0, kd1); m0_inv(mi[1], 0, 1, l15, g, 0, kd1); }
    PIN(mi[0]); PIN(mi[1]);
    if (sp0) { m0_inv(mix[0], 1, 0, l15, g, 0, 0); m0_inv(mix[1], 1, 1, l15, g, 0, 0); }
    SB(); WAITV(0);
    LBAR();
#pragma unroll
    for (int i = 0; i < 3; ++i) if (i < 2 || (sp0 && wm == 0)) {
      const int tau = wn + 2 * (wm + 4 * i);
      const TileD td = tile_desc(tau, k1, kd1);
      const float sc = td.w * (1.0f / 2097152.0f);
      const int nk = (2048 - td.k) & 2047;
      const char* wp = lds + 32768 + (k1 ? td.k >> 8 : td.k >> 7) * 1056 + ((td.k * 1026) & 15);
      const char* wq = lds + 32768 + (k1 ? 8 + (td.k >> 8) : nk >> 7) * 1056 + ((nk * 1026) & 15);
      f32x4 fr, fi;
#pragma unroll
      for (int r = 0; r < 4; ++r) {
        const int kd = cc + 4 * l15 + 64 * (4 * g + r);
        const char* a = kd <= 512 ? wp + 2 * kd : wq + 2 * (1024 - kd);
        fr[r] = bf2f(*(const u16*)a) * sc; fi[r] = bf2f(*(const u16*)(a + 32768)) * (kd > 512 ? -sc : sc);
        if (r == 0 && cc == 0) {
          const float er = bf2f(*(const u16*)(wq + 2 * (kd & 512))) * sc, ei = bf2f(*(const u16*)(wq + 32768 + 2 * (kd & 512))) * sc;
          if ((kd & 511) == 0) { fr[0] = 0.5f * (fr[0] + er); fi[0] = 0.5f * (fi[0] - ei); }
        }
      }
      char* pl = vi + tau * 1088;
      bf16x8 a = cat(trr(pl + (4 * g + q) * 32 + 8 * pq), trr(pl + 544 + (4 * g + q) * 32 + 8 * pq));
      f32x4 yr = mma16(a, fa_r, z4), yi = mma16(a, fa_i, z4);
      a = pk8(yr * t1c - yi * t1s, yr * t1s + yi * t1c);
      yr = mma16(fb_r, a, z4); yi = mma16(fb_i, a, z4);
      a = pk8(yr * fr - yi * fi, yr * fi + yi * fr);
      yr = mma16(a, gb_r, z4); yi = mma16(a, gb_i, z4);
      a = pk8(yr * t2c - yi * t2s, yr * t2s + yi * t2c);
      u32x4 o = __builtin_bit_cast(u32x4, pk8(mma16(a, ga_r, z4), mma16(a, ga_i, z4)));
      *(u32x2*)(pl + 32 * l15 + 8 * g) = u32x2{o.x, o.y}; *(u32x2*)(pl + 544 + 32 * l15 + 8 * g) = u32x2{o.z, o.w};
    }
    LBAR();
    f32x4 o[2][2] = {};
#pragma unroll
    for (int ks = 0; ks < 2; ++ks) if (ks == 0 || sp0) {
      const char* ap = vi + (ks ? 32 + q : 4 * g + q) * 544 + 64 * w + 16 * pq; const int st = ks ? 0 : 16 * 544;
      bf16x8 a0 = cat(trr(ap), trr(ap + st)), a1 = cat(trr(ap + 8), trr(ap + st + 8));
#pragma unroll
      for (int dl = 0; dl < 2; ++dl) { const bf16x8 mm = ks ? mix[dl] : mi[dl]; o[dl][0] = mma16(a0, mm, o[dl][0]); o[dl][1] = mma16(a1, mm, o[dl][1]); }
    }
#pragma unroll
    for (int dl = 0; dl < 2; ++dl) *(bf16x8*)(ws + WS_T + ub + l15 * 1024 + 512 * dl + 64 * w + 16 * g) = pk8(o[dl][0], o[dl][1]);
  }
  } else {
  const int kcol = pair_row(128 * a_half + 32 * wm + (lane & 31));
  u32x4 hf[2][4];
  hartley_load(hf, kcol, lane);
  const int p = 64 * pt + 32 * wn + (lane & 31), seg = 2 * wm + (lane >> 5);
  u32 q_dt = aldh(log_dt + p), q_lre = aldh(lam_re + p), q_lim = aldh(lam_im + p);
  {
    WAITV(0); BAR();
    TL(3);
    PIN(tag); PIN(q_dt); PIN(q_lre); PIN(q_lim);
    const f32x16 acc = hartley_mma<false>(lds + 3 * 32768, hf, kcol, lane, wn);
    const int r = 128 * a_half + 32 * wm + (lane & 31);
    const u32 off = (u32)(((r & ~1) * 16 + 2 * a_s2 + (r & 1)) * 1024 + 64 * a_ct + 32 * wn + 16 * (lane >> 5));
    u32x4 p0, p1;
#pragma unroll
    for (int i = 0; i < 4; ++i) {
      p0[i] = pk2(acc[2 * i] + acc[8 + 2 * i], acc[2 * i + 1] + acc[9 + 2 * i]);
      p1[i] = pk2(acc[2 * i] - acc[8 + 2 * i], acc[2 * i + 1] - acc[9 + 2 * i]);
    }
    __builtin_amdgcn_raw_buffer_store_b128(p0, ursrc, off, 0, 16);
    __builtin_amdgcn_raw_buffer_store_b128(p1, ursrc, off + 16384, 0, 16);
  }
  TS(1);
  f32x16 ur = {}, ui = {};
  cf lam, coef, l16;
  {
    const int h = lane >> 5, ra = 32 * wm + (lane & 31), rb = 64 * wn + (lane & 31), sw = (lane >> 1) & 7;
    bf16x8 fr[2][12];
    u32 aa[4], ab[4];
#pragma unroll
    for (int ks = 0; ks < 4; ++ks) { const int co = 16 * ((2 * ks + h) ^ sw); aa[ks] = ldsa(lds + ra * 128 + co); ab[ks] = ldsa(lds + rb * 128 + co); }
    tag = mix64(tag);
#pragma unroll
    for (int j = 0; j <= 16; ++j) {
      if (j == 3) WAITV(0);
      if (j < 16) LBAR();
      if (j == 0) TL(4); if (j == 3) TL(5); if (j == 6) TL(6); if (j == 11) TL(7);
      const int st = (j & 1) * 32768, adv = (j & 1) || j == 0 ? 0 : ((j & 2) ? 65536 : -65536);
#pragma unroll
      for (int ks = 0; ks < 4; ++ks) {
        if (!(j & 1)) { aa[ks] += adv; ab[ks] += adv; PIN(aa[ks]); PIN(ab[ks]); }
        if (j > 0) ur = mma32(fr[(j - 1) & 1][3 * ks], fr[(j - 1) & 1][3 * ks + 1], ur);
        SB();
        if (j < 16) { fr[j & 1][3 * ks] = ldsr(aa[ks], st); fr[j & 1][3 * ks + 1] = ldsr(ab[ks], st + 16384); }
        SB();
        if (j > 0) ui = mma32(fr[(j - 1) & 1][3 * ks], fr[(j - 1) & 1][3 * ks + 2], ui);
        SB();
        if (j < 16) fr[j & 1][3 * ks + 2] = ldsr(ab[ks], st + 20480);
        SB();
        if (j == 0 && ks == 3) {
          const float dt = rbf(expf(bf2f(q_dt))), mg = expf(dt * bf2f(q_lre)), an = dt * bf2f(q_lim);
          lam = {mg * cosf(an), mg * sinf(an)}; PIN(lam.r); PIN(lam.i);
        }
        if (j == 1 && ks == 0) {
          const float lre = bf2f(q_lre), lim = bf2f(q_lim), nr = lam.r - 1.f, ni = lam.i;
          const bool big = fabsf(lre) >= fabsf(lim);
          const float u = big ? lre : lim, v = big ? lim : lre, t = v / u, dn = u + v * t;
          coef = {2.f * ((big ? nr + ni * t : nr * t + ni) / dn), 2.f * ((big ? ni - nr * t : ni * t - nr) / dn)};
          l16 = lam;
#pragma unroll
          for (int i = 0; i < 4; ++i) l16 = cmul(l16, l16);
          PIN(coef.r); PIN(coef.i); PIN(l16.r); PIN(l16.i);
        }
        SB();
      }
    }
  }
  TS(2);
  u32x4 gx[2], gw[2];
    gx[0] = ald4<0>(x + (bid * 8 + w) * 1024 + 8 * lane); gx[1] = ald4<1024>(x + (bid * 8 + w) * 1024 + 8 * lane);
    gw[0] = ald4<0>(gate_w + 8 * lane); gw[1] = ald4<1024>(gate_w + 8 * lane);
    u32 warm = 0;
    if (w >= 6) warm = ald1(((w & 1) ? c_im : c_re) + bid * 4096 + 64 * lane);
#pragma unroll
  for (int i = 1; i < 16; ++i) { cf v = cfma(lam, {ur[i - 1], ui[i - 1]}, {ur[i], ui[i]}); ur[i] = v.r; ui[i] = v.i; }
  f32x2* segs = (f32x2*)(lds + 131072) + 32 * wn + (lane & 31);
  segs[seg * 64] = f32x2{ur[15], ui[15]};
  LBAR();
  const rsrc_t ar = mkr(ws + WS_AGG, 1 << 18);
  if (wm == 3 && tt < 15) {
    cf c = {0.f, 0.f};
#pragma unroll
    for (int j = 0; j < 8; ++j) { f32x2 e = segs[j * 64]; c = cfma(l16, c, {e.x, e.y}); }
    if (lane < 32) __builtin_amdgcn_raw_buffer_store_b128(u32x4{__builtin_bit_cast(u32, c.r), __builtin_bit_cast(u32, c.i), (u32)tag, (u32)(tag >> 32)}, ar, p * 16, tt * 16384, 16);
  }
  TM(3);
  {
    cf c = {0.f, 0.f}, lseg = coef;
#pragma unroll
    for (int j = 0; j < 7; ++j) { f32x2 e = segs[j * 64]; cf n = cfma(l16, c, {e.x, e.y}), m = cmul(lseg, l16); if (j < seg) { c = n; lseg = m; } }
    c = cmul(c, coef);
#pragma unroll
    for (int i = 0; i < 16; ++i) { const cf v = cmul(coef, {ur[i], ui[i]}); ur[i] = v.r; ui[i] = v.i; }
    PIN(ur); PIN(ui);
    {
      if (w >= 6) WAITV(1); else WAITV(0);
      float s = 0.f;
      PIN(gx[0]); PIN(gx[1]); PIN(gw[0]); PIN(gw[1]);
#pragma unroll
      for (int i = 0; i < 2; ++i)
#pragma unroll
        for (int e = 0; e < 4; ++e) {
          const u32 pr = pk2(bf2f(gx[i][e] & 0xffffu) * bf2f(gw[i][e] & 0xffffu), bf2f(gx[i][e] >> 16) * bf2f(gw[i][e] >> 16));
          s += bf2f(pr & 0xffffu) + bf2f(pr >> 16);
        }
#pragma unroll
      for (int o = 32; o > 0; o >>= 1) s += __shfl_xor(s, o);
      if (lane == 0) ((float*)(ws + WS_G))[bid * 8 + w] = rbf(1.f / (1.f + expf(-rbf(s))));
    }
    TM(4);
    if (tt > 0) {
      cf l128 = cmul(l16, l16); l128 = cmul(l128, l128); l128 = cmul(l128, l128);
      const cf l256 = cmul(l128, l128);
      const int j0 = tt - 1 - (lane >> 5);
      u32x4 v[8];
      for (u32 sp = 0; sp < SPIN_CAP; ++sp) {
        bool ok = true;
#pragma unroll
        for (int n = 0; n < 8; ++n) if (2 * n < tt) v[n] = __builtin_amdgcn_raw_buffer_load_b128(ar, max(j0 - 2 * n, 0) * 16384 + p * 16, 0, 16);
#pragma unroll
        for (int n = 0; n < 8; ++n) if (2 * n < tt) ok = ok && v[n].z == (u32)tag && v[n].w == (u32)(tag >> 32);
        if (__all(ok)) break;
        asm volatile("s_sleep 1" ::: "memory");
      }
      cf cb = {0.f, 0.f};
#pragma unroll
      for (int n = 7; n >= 0; --n) if (2 * n < tt) {
        const bool in = j0 - 2 * n >= 0;
        cb = cfma(l256, cb, {__uint_as_float(in ? v[n].x : 0u), __uint_as_float(in ? v[n].y : 0u)});
      }
      if (lane >= 32) cb = cmul(cb, l128);
      cb.r += __shfl_xor(cb.r, 32); cb.i += __shfl_xor(cb.i, 32);
      c = cfma(lseg, cb, c);
    }
    TM(5);
    WAITV(0); PIN(warm);
    BAR();
    const rsrc_t hr = mkr(ws + WS_H, 8 << 20);
    u32* tw = (u32*)(lds + 98304 + w * 4096) + (lane & 1) * 64 + ((lane >> 3) & 3) * 16 + (lane >> 5) * 8 + ((lane >> 1) & 3);
#pragma unroll
    for (int i = 0; i < 16; ++i) {
      c = cmul(c, lam);
      const u32 own = pk2(ur[i] + c.r, -(ui[i] + c.i)), oth = __builtin_amdgcn_mov_dpp(own, 0xB1, 0xf, 0xf, true);
      tw[(i & 7) * 128 + (i >> 3) * 4] = __builtin_amdgcn_perm(oth, own, (lane & 1) ? 0x03020706u : 0x05040100u);
    }
    const int k = 16 * tt + 4 * wm + (lane & 3), par = k >= 128, kk = par ? (256 - k) & 127 : k;
    const u32 ho = ((lane >> 5) << 20) + (kk >> 4) * 131072 + (2 * pt + ((lane >> 4) & 1)) * 4096 + (4 * wn + ((lane >> 2) & 3)) * 512 + par * 256 + (kk & 15) * 16;
#pragma unroll
    for (int r = 0; r < 4; ++r) __builtin_amdgcn_raw_buffer_store_b128(*(const u32x4*)(lds + 98304 + w * 4096 + r * 1024 + lane * 16), hr, ho, r << 21, HAUX);
  }
  }
  TM(7);
  if (bid == 1 && threadIdx.x < 256) {
    poll(fa + tid, true, tag);
  }
  if (bid == 1) { LBAR(); if (threadIdx.x == 0) __hip_atomic_store((u64*)(ws + WS_E), tag, RLX); }
}

__global__ __launch_bounds__(768) void k_back(const u16* __restrict__ x, const u16* __restrict__ c_re, const u16* __restrict__ c_im,
    const u16* __restrict__ dskip, const char* __restrict__ ws, float* __restrict__ out) {
  extern __shared__ __attribute__((aligned(16))) char lds[];
  const int tid = threadIdx.x & 511, lane = tid & 63, w = __builtin_amdgcn_readfirstlane(tid >> 6), wn = w & 1, wm = w >> 1;
  const bool loader = __builtin_amdgcn_readfirstlane(threadIdx.x >> 9);
  const int bid = blockIdx.x, xcd = bid & 7, jj = bid >> 3;
  const int rt = (xcd >> 1) * 4 + (jj & 3), ct = (xcd & 1) * 8 + (jj >> 2), s2 = rt >> 1, sh = rt & 1;
  const rsrc_t tr = mkr(ws + WS_T, 4 << 20), cr0 = mkr(c_re, 2 << 20), cr1 = mkr(c_im, 2 << 20);
  TS(8);
  if (loader) {
    const int lr = lane >> 3, lc = lane & 7, c = lc ^ (lr & 2);
#pragma unroll
    for (int i = 0; i < 8; ++i) { const int k = 32 * i + 8 * w + lr, kb = k < 128 ? k : (256 - k) & 127;
      blds(tr, kb * 32768 + (c >> 2) * 16384 + 16 * (c & 3), s2 * 2048 + 64 * ct + (i >= 4 ? 1024 : 0), lds + i * 4096 + w * 1024); }
    const int ra0 = 8 * w + lr, m = ra0 & 15, d = 32 * ct + 512 * ((ra0 >> 4) & 1) + 8 * ((m >> 2) & 1) + 4 * (m >> 3) + (m & 3);
    const u32 vC = d * 2048 + 16 * (lc ^ ((ra0 >> 1) & 7));
    auto issue = [&](int j) {
#pragma unroll
      for (int i = 0; i < 4; ++i) blds((i & 2) ? cr1 : cr0, vC, 128 * j + (i & 1) * 32768, lds + 32768 + (j % 7) * 16384 + i * 4096 + w * 1024);
    };
    issue(0);
    WAITV(4); BAR();
#pragma unroll
    for (int j = 1; j < 6; ++j) issue(j);
#pragma unroll
    for (int j = 0; j < 16; ++j) {
      if (j < 11) WAITV(20); else if (j == 11) WAITV(16); else if (j == 12) WAITV(12); else if (j == 13) WAITV(8); else if (j == 14) WAITV(4); else WAITV(0);
      BAR();
      if (j < 10) issue(j + 6);
    }
    BAR();
    return;
  }
  const int h = lane >> 5, kcol = pair_row(128 * sh + 32 * wm + 2 * (lane & 15) + ((lane >> 4) & 1)), srow = s2 + 8 * kcol, d0 = 32 * ct + 16 * wn + 8 * h;
  u32x4 hf[2][4];
  hartley_load(hf, kcol, lane);
  const char* hq = ws + WS_H + (size_t)(s2 * 8 + sh * 4 + wm) * 131072 + wn * 4096 + lane * 16;
  u32x4 hv[4][4];
  auto hload = [&](int j) { const char* a = hq + j * 8192; hv[j & 3][0] = ald4<0>(a); hv[j & 3][1] = ald4<1024>(a); hv[j & 3][2] = ald4<2048>(a); hv[j & 3][3] = ald4<3072>(a); };
  hload(0); hload(1);
  u32 ap[8];
  {
    WAITV(8); BAR();
    const f32x16 acc = hartley_mma<true>(lds, hf, kcol, lane, wn);
#pragma unroll
    for (int i = 0; i < 4; ++i) {
      ap[i] = pk2(acc[2 * i] + acc[8 + 2 * i], acc[2 * i + 1] + acc[9 + 2 * i]);
      ap[4 + i] = pk2(acc[2 * i] - acc[8 + 2 * i], acc[2 * i + 1] - acc[9 + 2 * i]);
    }
  }
  TS(9);
  hload(2);
  f32x16 acc = {}, acx = {};
  float gv; u32x4 xv[2], dv[2];
  {
    const int sw = (lane >> 1) & 7;
    bf16x8 fr[2][8];
    u32 aa[4], ab[4];
#pragma unroll
    for (int ks = 0; ks < 4; ++ks) { aa[ks] = ldsa(lds + 32768 + wn * 8192 + (lane & 31) * 128 + 16 * ((2 * ks + h) ^ sw)); ab[ks] = aa[ks] + 4096 * (1 - wn); aa[ks] += 4096 * wn; }
#pragma unroll
    for (int j = 0; j <= 16; ++j) {
      if (j < 16) LBAR();
      if (j > 0) {
        if (j < 15) WAITV(8); else if (j == 15) WAITV(9); else WAITV(5);
#pragma unroll
        for (int ks = 0; ks < 4; ++ks) PIN(hv[(j - 1) & 3][ks]);
        if (j < 14) hload(j + 2);
      }
      if (j == 14) {
        gv = __builtin_bit_cast(float, ald1((const float*)(ws + WS_G) + srow));
        xv[0] = ald4<0>(x + srow * 1024 + d0); xv[1] = ald4<1024>(x + srow * 1024 + d0); dv[0] = ald4<0>(dskip + d0); dv[1] = ald4<1024>(dskip + d0);
      }
      const int st = ((j % 7) % 3) * 16384, adv = j % 7 == 0 ? (j ? -98304 : 0) : ((j % 7) % 3 ? 0 : 49152);
#pragma unroll
      for (int ks = 0; ks < 4; ++ks) {
        if (adv || j == 0) { aa[ks] += adv; ab[ks] += adv; PIN(aa[ks]); PIN(ab[ks]); }
        if (j > 0) acc = mma32(fr[(j - 1) & 1][2 * ks], __builtin_bit_cast(bf16x8, hv[(j - 1) & 3][ks]), acc);
        SB();
        if (j < 16) fr[j & 1][2 * ks] = ldsr(aa[ks], st);
        SB();
        if (j > 0) acx = mma32(fr[(j - 1) & 1][2 * ks + 1], __builtin_bit_cast(bf16x8, hv[(j - 1) & 3][ks]), acx);
        SB();
        if (j < 16) fr[j & 1][2 * ks + 1] = ldsr(ab[ks], st);
        SB();
      }
    }
#pragma unroll
    for (int i = 0; i < 4; ++i) *(f32x4*)(lds + (w ^ 1) * 4096 + i * 1024 + lane * 16) = f32x4{acx[4 * i], acx[4 * i + 1], acx[4 * i + 2], acx[4 * i + 3]};
    LBAR();
#pragma unroll
    for (int i = 0; i < 4; ++i) { const f32x4 v = *(const f32x4*)(lds + w * 4096 + i * 1024 + lane * 16); acc[4 * i] += v[0]; acc[4 * i + 1] += v[1]; acc[4 * i + 2] += v[2]; acc[4 * i + 3] += v[3]; }
    WAITV(0); PIN(gv); PIN(xv[0]); PIN(xv[1]); PIN(dv[0]); PIN(dv[1]);
  }
  TS(10);
  const float og = rbf(1.f - gv);
#pragma unroll
  for (int i = 0; i < 2; ++i) {
    float o[8];
#pragma unroll
    for (int e = 0; e < 4; ++e) {
      const u32 sk = pk2(bf2f(xv[i][e] & 0xffffu) * bf2f(dv[i][e] & 0xffffu), bf2f(xv[i][e] >> 16) * bf2f(dv[i][e] >> 16));
      const u32 ga = pk2(gv * bf2f(ap[4 * i + e] & 0xffffu), gv * bf2f(ap[4 * i + e] >> 16));
      o[2 * e] = bf2f(ga & 0xffffu) + og * (acc[8 * i + 2 * e] + bf2f(sk & 0xffffu));
      o[2 * e + 1] = bf2f(ga >> 16) + og * (acc[8 * i + 2 * e + 1] + bf2f(sk >> 16));
    }
    float* op = out + srow * 1024 + d0 + 512 * i;
    *(f32x4*)op = f32x4{o[0], o[1], o[2], o[3]}; *(f32x4*)(op + 4) = f32x4{o[4], o[5], o[6], o[7]};
  }
  TS(11);
}

__global__ __launch_bounds__(512) void k_probe(const char* p) {
  extern __shared__ __attribute__((aligned(16))) char lds[];
  const int lane = threadIdx.x & 63, w = __builtin_amdgcn_readfirstlane(threadIdx.x >> 6);
  const rsrc_t xr = mkr(p, 2 << 20);
  const long long t0 = __builtin_amdgcn_s_memrealtime();
#pragma unroll 1
  for (int j = 0; j < 256 / 8; ++j) { blds(xr, lane * 16, blockIdx.x * 256 * 1024 + (j * 8 + w) * 1024, lds + ((j * 8 + w) & 63) * 1024); WAITV(7); }
  WAITV(0); BAR();
  const long long t1 = __builtin_amdgcn_s_memrealtime();
  const long long e = t1 + (t1 - t0) * 0;
  while ((long long)__builtin_amdgcn_s_memrealtime() < e) __builtin_amdgcn_s_sleep(1);
}
extern "C" void kernel_launch(void* const* d_in, const int* in_sizes, int n_in, void* d_out, int out_size, void* d_ws, size_t ws_size, hipStream_t stream) {
  (void)in_sizes; (void)n_in; (void)out_size; (void)ws_size;
  (void)hipFuncSetAttribute((const void*)k_front, hipFuncAttributeMaxDynamicSharedMemorySize, LDS1);
  (void)hipFuncSetAttribute((const void*)k_back, hipFuncAttributeMaxDynamicSharedMemorySize, LDS2);
  const u16* const* in = (const u16* const*)d_in;
  k_probe<<<8, 512, 65536, stream>>>((const char*)d_in[3]);
  k_front<<<256, 1024, LDS1, stream>>>(in[0], in[1], in[2], in[4], in[5], in[6], in[7], in[10], in[12], in[8], in[9], (char*)d_ws);
  k_back<<<256, 768, LDS2, stream>>>(in[0], in[8], in[9], in[11], (const char*)d_ws, (float*)d_out);
}
```

```cpp
#include <hip/hip_runtime.h>
#include <hip/hip_bf16.h>
#include <stdint.h>

typedef unsigned short u16;
typedef unsigned int u32;
typedef unsigned long long u64;
typedef __attribute__((ext_vector_type(8))) short s16x8;
typedef __attribute__((ext_vector_type(4))) short s16x4;
typedef __attribute__((ext_vector_type(8))) __bf16 bf16x8;
typedef __attribute__((ext_vector_type(2))) __bf16 bf16x2;
typedef __attribute__((ext_vector_type(2))) float f32x2;
typedef __attribute__((ext_vector_type(4))) float f32x4;
typedef __attribute__((ext_vector_type(16))) float f32x16;
typedef __attribute__((ext_vector_type(4))) u32 u32x4;
typedef __attribute__((ext_vector_type(2))) u32 u32x2;
#define DI __device__ __forceinline__
#define LDSP __attribute__((address_space(3)))
#define GLBP __attribute__((address_space(1)))
#define RLX __ATOMIC_RELAXED, __HIP_MEMORY_SCOPE_AGENT

struct HTab { u16 v[16384]; };
constexpr double c_sin(double x) { double t = x, s = x; for (int i = 1; i < 16; ++i) { t *= -x * x / ((2 * i) * (2 * i + 1)); s += t; } return s; }
constexpr double c_cos(double x) { double t = 1, s = 1; for (int i = 1; i < 16; ++i) { t *= -x * x / ((2 * i - 1) * (2 * i)); s += t; } return s; }
constexpr u16 c_bf16(double d) {
  if (d < 1e-9 && d > -1e-9) return 0;
  u32 b = __builtin_bit_cast(u32, (float)d);
  return (u16)((b + 0x7fffu + ((b >> 16) & 1u)) >> 16);
}
constexpr HTab make_htab() {
  HTab t{}; u16 c[256] = {};
  for (int n = 0; n < 256; ++n) { double x = 2.0 * 3.14159265358979323846 * (n < 128 ? n : n - 256) / 256.0; c[n] = c_bf16(c_sin(x) + c_cos(x)); }
  for (int i = 0; i < 16384; ++i) t.v[i] = c[((i >> 6) * (i & 63)) & 255];
  return t;
}
__device__ __attribute__((aligned(256))) const HTab g_htab = make_htab();

constexpr size_t WS_E = 0, WS_FA = 4096, WS_G = 16384, WS_AGG = 65536;
constexpr size_t WS_U = 1u << 20, WS_T = 5u << 20, WS_H = 9u << 20;
constexpr int LDS1 = 4 * 32768 + 4096 + 36 * 544, LDS2 = 32768 + 7 * 16384;
constexpr u32 SPIN_CAP = 1u << 20;

DI float bf2f(u32 h) { return __builtin_bit_cast(float, h << 16); }
DI u32 pk2(float a, float b) { f32x2 v = {a, b}; return __builtin_bit_cast(u32, __builtin_convertvector(v, bf16x2)); }
DI float rbf(float a) { return bf2f(pk2(a, a) & 0xffffu); }
DI void glds(const void* g, char* l) { __builtin_amdgcn_global_load_lds((const GLBP void*)g, (LDSP void*)l, 16, 0, 0); }
typedef __amdgpu_buffer_rsrc_t rsrc_t;
DI rsrc_t mkr(const void* p, int bytes) { return __builtin_amdgcn_make_buffer_rsrc((void*)p, 0, bytes, 0x00020000); }
DI void blds(rsrc_t r, u32 vo, u32 so, char* l) { __builtin_amdgcn_raw_ptr_buffer_load_lds(r, (LDSP void*)l, 16, vo, so, 0, 0); }
DI bf16x8 ldsr(u32 a, int off) { return *(const LDSP bf16x8*)(uintptr_t)(a + off); }
DI u32 ldsa(const char* p) { return (u32)(uintptr_t)(LDSP const char*)p; }
DI u32x2 trr(const char* p) { return __builtin_bit_cast(u32x2, __builtin_amdgcn_ds_read_tr16_b64_v4i16((LDSP s16x4*)p)); }
DI bf16x8 cat(u32x2 a, u32x2 b) { u32x4 v = {a.x, a.y, b.x, b.y}; return __builtin_bit_cast(bf16x8, v); }
DI bf16x8 pk8(f32x4 a, f32x4 b) { u32x4 v = {pk2(a[0], a[1]), pk2(a[2], a[3]), pk2(b[0], b[1]), pk2(b[2], b[3])}; return __builtin_bit_cast(bf16x8, v); }
DI f32x16 mma32(bf16x8 a, bf16x8 b, f32x16 c) { return __builtin_amdgcn_mfma_f32_32x32x16_bf16(a, b, c, 0, 0, 0); }
DI f32x4 mma16(bf16x8 a, bf16x8 b, f32x4 c) { return __builtin_amdgcn_mfma_f32_16x16x32_bf16(a, b, c, 0, 0, 0); }
DI float cosr(float rev) { return __builtin_amdgcn_cosf(rev); }
DI float sinr(float rev) { return __builtin_amdgcn_sinf(rev); }
template <int O> DI u32x4 ald4(const void* p) { u32x4 v; asm volatile("global_load_dwordx4 %0, %1, off offset:%2" : "=v"(v) : "v"(p), "n"(O) : "memory"); return v; }
DI u32 ald1(const void* p) { u32 v; asm volatile("global_load_dword %0, %1, off" : "=v"(v) : "v"(p) : "memory"); return v; }
DI u32 aldh(const void* p) { u32 v; asm volatile("global_load_ushort %0, %1, off" : "=v"(v) : "v"(p) : "memory"); return v; }
DI u64 ald2c(const void* p) { u64 v; asm volatile("global_load_dwordx2 %0, %1, off sc1" : "=v"(v) : "v"(p) : "memory"); return v; }
#define SB() __builtin_amdgcn_sched_barrier(0)
#define PIN(v) asm volatile("" : "+v"(v))
#define WAITV(n) asm volatile("s_waitcnt vmcnt(" #n ")" ::: "memory")
#define BAR() do { asm volatile("" ::: "memory"); __builtin_amdgcn_s_barrier(); asm volatile("" ::: "memory"); } while (0)
#define LBAR() do { asm volatile("s_waitcnt lgkmcnt(0)" ::: "memory"); __builtin_amdgcn_s_barrier(); asm volatile("" ::: "memory"); } while (0)
DI u64 mix64(u64 e) { u64 z = e + 0x9e3779b97f4a7c15ull; z = (z ^ (z >> 30)) * 0xbf58476d1ce4e5b9ull; z = (z ^ (z >> 27)) * 0x94d049bb133111ebull; z ^= z >> 31; return z == e ? ~e : z; }
DI void poll(const u64* f, bool act, u64 tag) {
  for (u32 s = 0; s < SPIN_CAP; ++s) {
    bool ok = true;
    if (act) ok = __hip_atomic_load(f, RLX) == tag;
    if (__all(ok)) break;
    __builtin_amdgcn_s_sleep(1);
  }
  __builtin_amdgcn_fence(__ATOMIC_ACQUIRE, "wavefront");
}
#define TS(n)
#define TL(n)
#define TM(n) TS(n)
#define HAUX 0
#define KMASK 31
struct cf { float r, i; };
DI cf cmul(cf a, cf b) { return {a.r * b.r - a.i * b.i, a.r * b.i + a.i * b.r}; }
DI cf cfma(cf a, cf b, cf c) { return {fmaf(a.r, b.r, fmaf(-a.i, b.i, c.r)), fmaf(a.r, b.i, fmaf(a.i, b.r, c.i))}; }

DI int pair_row(int r) { const int kk = r >> 1; return (r & 1) ? (kk ? 256 - kk : 128) : kk; }
DI void hartley_load(u32x4 (&f)[2][4], int kcol, int lane) {
  const u16* p0 = g_htab.v + kcol * 64 + 8 * (lane >> 5);
  f[0][0] = ald4<0>(p0); f[0][1] = ald4<32>(p0); f[0][2] = ald4<64>(p0); f[0][3] = ald4<96>(p0);
}
#define TRL(i, o) "ds_read_b64_tr_b16 %" #i ", %16 offset:" #o "\n\t"
template <bool X16> DI f32x16 hartley_mma(const char* sa, u32x4 (&f)[2][4], int kcol, int lane, int wn) {
#pragma unroll
  for (int i = 0; i < 4; ++i) {
    PIN(f[0][i]);
#pragma unroll
    for (int e = 0; e < 4; ++e) { const u32 o = X16 ? __builtin_amdgcn_ds_swizzle(f[0][i][e], 0x401f) : __builtin_amdgcn_mov_dpp(f[0][i][e], 0xB1, 0xf, 0xf, true); f[1][i][e] = (kcol & 1) ? o : f[0][i][e]; }
  }
  const int h = lane >> 5, blk = (lane >> 4) & 1, q = (lane & 15) >> 2, p = lane & 3;
  const int pp = ((p & 1) << 1) | (p >> 1);
  const u32 ap = (u32)(uintptr_t)(LDSP const char*)(sa + (8 * h + q) * 128 + 16 * ((4 * blk + 2 * wn + (pp >> 1)) ^ (q & 2)) + 8 * (pp & 1));
  f32x16 acc = {};
#pragma unroll
  for (int hf = 0; hf < 2; ++hf) {
    u32x2 t[16];
    asm volatile(TRL(0, 0) TRL(1, 512) TRL(2, 2048) TRL(3, 2560) TRL(4, 4096) TRL(5, 4608) TRL(6, 6144) TRL(7, 6656)
                 TRL(8, 8192) TRL(9, 8704) TRL(10, 10240) TRL(11, 10752) TRL(12, 12288) TRL(13, 12800) TRL(14, 14336) TRL(15, 14848) "s_waitcnt lgkmcnt(0)"
                 : "=&v"(t[0]), "=&v"(t[1]), "=&v"(t[2]), "=&v"(t[3]), "=&v"(t[4]), "=&v"(t[5]), "=&v"(t[6]), "=&v"(t[7]),
                   "=&v"(t[8]), "=&v"(t[9]), "=&v"(t[10]), "=&v"(t[11]), "=&v"(t[12]), "=&v"(t[13]), "=&v"(t[14]), "=&v"(t[15])
                 : "v"(ap + hf * 16384) : "memory");
#pragma unroll
    for (int i = 0; i < 8; ++i) {
      const int qq = 2 * hf + (i >> 2);
      const u32 sg = ((kcol * qq) & 2) ? 0x80008000u : 0u;
      acc = mma32(cat(t[2 * i], t[2 * i + 1]), __builtin_bit_cast(bf16x8, f[qq & 1][i & 3] ^ sg), acc);
    }
  }
  return acc;
}

struct TileD { int k, c, src; float w; };
DI TileD tile_desc(int tau, int k1, int kd1) {
  TileD t; const int i = tau >> 1, ka = tau & 1;
  t.c = kd1 + 2 * ka;
  if (k1 != 0) { t.k = k1 + 256 * i; t.src = -1; t.w = tau < 16 ? 1.f : 0.f; }
  else if (kd1) { t.k = 128 * ka + 256 * i; t.src = ka; t.w = tau < 16 ? 2.f : 0.f; }
  else { t.src = i >= 5; t.k = i >= 5 ? 128 + 256 * (i - 5) : 256 * i; t.w = tau >= 18 ? 0.f : ((i == 0 || i == 4) ? 1.f : 2.f); }
  return t;
}
DI float m0val(const TileD& t, int ri, int dl, int s2, int rs) {
  float a = (float)(s2 * t.k) * (-1.f / 2048.f) - 0.25f * (float)(dl * t.c + ri);
  if (t.src < 0) return cosr(a + (rs ? 0.125f : -0.125f));
  return (t.w == 0.f || t.src != rs) ? 0.f : cosr(a);
}
DI void m0_fwd(bf16x8& o, int n, int l15, int g, int k1, int kd1) {
  const TileD t = tile_desc(8 * n + (l15 >> 1), k1, kd1); f32x4 v0, v1;
#pragma unroll
  for (int j = 0; j < 4; ++j) { v0[j] = m0val(t, l15 & 1, 0, 2 * g + (j >> 1), j & 1); v1[j] = m0val(t, l15 & 1, 1, 2 * g + (j >> 1), j & 1); }
  o = pk8(v0, v1);
}
DI void m0_inv(bf16x8& o, int ks, int dl, int l15, int g, int k1, int kd1) {
  f32x4 v[2];
#pragma unroll
  for (int j = 0; j < 8; ++j) { const int pn = 32 * ks + 16 * (j >> 2) + 4 * g + (j & 3);
    v[j >> 2][j & 3] = m0val(tile_desc(pn >> 1, k1, kd1), pn & 1, dl, l15 >> 1, l15 & 1); }
  o = pk8(v[0], v[1]);
}

__global__ __launch_bounds__(1024) void k_front(const u16* __restrict__ x, const u16* __restrict__ wr, const u16* __restrict__ wi,
    const u16* __restrict__ lam_re, const u16* __restrict__ lam_im, const u16* __restrict__ b_re, const u16* __restrict__ b_im,
    const u16* __restrict__ log_dt, const u16* __restrict__ gate_w, const u16* __restrict__ c_re, const u16* __restrict__ c_im, char* __restrict__ ws) {
  extern __shared__ __attribute__((aligned(16))) char lds[];
  const int tid = threadIdx.x & 511, lane = tid & 63, w = __builtin_amdgcn_readfirstlane(tid >> 6), wn = w & 1, wm = w >> 1;
  const bool loader = __builtin_amdgcn_readfirstlane(threadIdx.x >> 9);
  const int bid = blockIdx.x, xcd = bid & 7, jj = bid >> 3;
  const int tt = (xcd >> 1) * 4 + (jj & 3), pt = (xcd & 1) * 8 + (jj >> 2);
  const int a_s2 = xcd, a_ct = jj >> 1, a_half = jj & 1;
  const int k1 = xcd * 16 + (jj >> 1), kd1 = jj & 1;
  u64* fa = (u64*)(ws + WS_FA);
  const rsrc_t ursrc = mkr(ws + WS_U, 4 << 20), xr = mkr(x, 4 << 20), br = mkr((w & 4) ? b_im : b_re, 2 << 20);

  TS(0);
  u64 tag = ald2c(ws + WS_E);
  const int l15 = lane & 15;
  if (loader) {
  const int lr = lane >> 3, lc = lane & 7;
  { const int r = 8 * w + lr, c = lc ^ (r & 2);
#pragma unroll
    for (int i = 0; i < 4; ++i) blds(xr, (a_s2 + 8 * r) * 2048 + 64 * a_ct + 16 * (c & 3) + 1024 * (c >> 2), i << 20, lds + 3 * 32768 + i * 8192 + w * 1024); }
  u32 vX, vB;
  { const int r = 8 * w + lr, c = lc ^ ((r >> 1) & 7);
    const int m = r & 31, tp = (r & ~31) + 16 * ((m >> 2) & 1) + (m & 3) + 4 * (m >> 3);
    vX = (128 * tt + tp) * 2048 + 16 * c; vB = (64 * pt + m) * 2048 + 16 * c; }
  auto issue = [&](int j, int i) {
    char* l = lds + (j & 3) * 32768 + i * 8192 + w * 1024;
    if (i < 2) blds(xr, vX, 128 * (j & KMASK) + i * 64 * 2048, l); else blds(br, vB, 128 * (j & KMASK) + (i - 2) * 32 * 2048, l);
  };
  auto wrow = [&](int slot) { return k1 ? (slot < 8 ? k1 + 256 * slot : 2048 - k1 - 256 * (slot - 8)) : 128 * slot; };
  auto issue_w = [&](int im, int i) {
    const int id = min(512 * i + tid, 1055), slot = id / 66;
    const int o = min(((wrow(slot) * 1026) & ~15) + 16 * (id - 66 * slot), 2048 * 1026 - 16);
    if (i < 2 || w == 0) glds((const char*)(im ? wi : wr) + o, lds + 32768 + im * 32768 + i * 8192 + w * 1024);
  };
  issue(0, 0); issue(0, 1); issue(0, 2); issue(0, 3);
  WAITV(4); BAR();
  PIN(tag); tag = mix64(tag);
#pragma unroll
  for (int i = 0; i < 8; ++i) issue(1 + (i >> 2), i & 3);
  const int g = lane >> 4, cc = kd1 + 2 * wn;
  const bool sp0 = (k1 == 0 && kd1 == 0);
  bf16x8 m0[2], mi[2], m0x, mix[2], fa_r, fa_i, fb_r, fb_i, gb_r, gb_i, ga_r, ga_i; f32x4 t1c, t1s, t2c, t2s;
  u32x4 q0, q1; u64 fl0, fl1;
  const float fl = (float)l15, fc = (float)cc;
  SB();
#pragma unroll
  for (int e = 0; e < 4; ++e) { const float a = (float)(4 * g + e) * (fc + 4.f * fl) * (-1.f / 64.f); t1c[e] = cosr(a); t1s[e] = sinr(a); }
  fa_r = pk8(t1c, -t1s); fa_i = pk8(t1s, t1c); PIN(fa_r); PIN(fa_i);
#pragma unroll
  for (int e = 0; e < 4; ++e) { const float a = (float)(4 * g + e) * fl * (-1.f / 16.f); t1c[e] = cosr(a); t1s[e] = sinr(a); }
  fb_r = pk8(t1c, -t1s); fb_i = pk8(t1s, t1c); gb_r = pk8(t1c, t1s); gb_i = pk8(-t1s, t1c); PIN(fb_r); PIN(fb_i); PIN(gb_r); PIN(gb_i);
#pragma unroll
  for (int e = 0; e < 4; ++e) { const float a = fl * (fc + 4.f * (float)(4 * g + e)) * (1.f / 64.f); t1c[e] = cosr(a); t1s[e] = sinr(a); }
  ga_r = pk8(t1c, -t1s); ga_i = pk8(t1s, t1c); PIN(ga_r); PIN(ga_i);
#pragma unroll
  for (int e = 0; e < 4; ++e) {
    const float fv = (float)(4 * g + e), a = fv * (fc + 4.f * fl) * (-1.f / 1024.f), b = fl * (fc + 4.f * fv) * (1.f / 1024.f);
    t1c[e] = cosr(a); t1s[e] = sinr(a); t2c[e] = cosr(b); t2s[e] = sinr(b);
  }
  PIN(t1c); PIN(t1s); PIN(t2c); PIN(t2s);
  const u32 ub = (u32)(2 * k1 + kd1) * 16384u;
    char* ui_ = lds; char* vi = lds + 135168;
    const int q = l15 >> 2, pq = lane & 3;
  const f32x4 z4 = {};
#pragma unroll
  for (int j = 0; j < 16; ++j) {
    if (j < 14) WAITV(8); else if (j == 14) WAITV(4); else WAITV(2);
    LBAR();
    if (j == 3 && tid == 0) __hip_atomic_store(fa + a_half * 128 + a_s2 * 16 + a_ct, tag, RLX);
    if (j == 13 && sp0) m0_fwd(m0x, 2, l15, g, 0, 0);
    if (j == 13) { if (k1) { m0_fwd(m0[0], 0, l15, g, k1, kd1); m0_fwd(m0[1], 1, l15, g, k1, kd1); } else { m0_fwd(m0[0], 0, l15, g, 0, kd1); m0_fwd(m0[1], 1, l15, g, 0, kd1); } PIN(m0[0]); PIN(m0[1]); }
    SB();
    if (j == 13) {
    *(u32x4*)(ui_ + w * 1056 + lane * 16) = q0; *(u32x4*)(ui_ + (8 + w) * 1056 + lane * 16) = q1;
    }
    if (j == 14)
    { const char* ap = ui_ + (4 * g + q) * 1056 + 64 * w + 16 * pq;
      bf16x8 a0 = cat(trr(ap), trr(ap + 512)), a1 = cat(trr(ap + 8), trr(ap + 520));
#pragma unroll
      for (int n = 0; n < 3; ++n) if (n < 2 || sp0) {
        const bf16x8 mm = n < 2 ? m0[n & 1] : m0x;
        bf16x8 o = pk8(mma16(a0, mm, z4), mma16(a1, mm, z4));
        if (n < 2 || l15 < 4) *(bf16x8*)(vi + (16 * n + l15) * 544 + 64 * w + 16 * g) = o;
      }
    }
    if (j == 12) {
      PIN(fl0); PIN(fl1);
      if (!__all(fl0 == tag && fl1 == tag)) { poll(fa + (k1 >> 6) * 128 + lane, true, tag); poll(fa + (k1 >> 6) * 128 + 64 + lane, true, tag); }
      __builtin_amdgcn_fence(__ATOMIC_ACQUIRE, "wavefront");
      q0 = __builtin_amdgcn_raw_buffer_load_b128(ursrc, ub + tid * 16, 0, 16); q1 = __builtin_amdgcn_raw_buffer_load_b128(ursrc, ub + 8192 + tid * 16, 0, 16);
    }
    SB();
    if (j < 13) { issue(j + 3, 0); issue(j + 3, 1); issue(j + 3, 2); issue(j + 3, 3); }
    else if (j > 13) { issue_w(j - 14, 0); issue_w(j - 14, 1); issue_w(j - 14, 2); }
    SB();
    if (j == 9) { fl0 = ald2c(fa + (k1 >> 6) * 128 + lane); fl1 = ald2c(fa + (k1 >> 6) * 128 + 64 + lane); }
    SB();
  }
  {
    if (k1) { m0_inv(mi[0], 0, 0, l15, g, k1, kd1); m0_inv(mi[1], 0, 1, l15, g, k1, kd1); } else { m0_inv(mi[0], 0, 0, l15, g, 0, kd1); m0_inv(mi[1], 0, 1, l15, g, 0, kd1); }
    PIN(mi[0]); PIN(mi[1]);
    if (sp0) { m0_inv(mix[0], 1, 0, l15, g, 0, 0); m0_inv(mix[1], 1, 1, l15, g, 0, 0); }
    SB(); WAITV(0);
    LBAR();
#pragma unroll
    for (int i = 0; i < 3; ++i) if (i < 2 || (sp0 && wm == 0)) {
      const int tau = wn + 2 * (wm + 4 * i);
      const TileD td = tile_desc(tau, k1, kd1);
      const float sc = td.w * (1.0f / 2097152.0f);
      const int nk = (2048 - td.k) & 2047;
      const char* wp = lds + 32768 + (k1 ? td.k >> 8 : td.k >> 7) * 1056 + ((td.k * 1026) & 15);
      const char* wq = lds + 32768 + (k1 ? 8 + (td.k >> 8) : nk >> 7) * 1056 + ((nk * 1026) & 15);
      f32x4 fr, fi;
#pragma unroll
      for (int r = 0; r < 4; ++r) {
        const int kd = cc + 4 * l15 + 64 * (4 * g + r);
        const char* a = kd <= 512 ? wp + 2 * kd : wq + 2 * (1024 - kd);
        fr[r] = bf2f(*(const u16*)a) * sc; fi[r] = bf2f(*(const u16*)(a + 32768)) * (kd > 512 ? -sc : sc);
        if (r == 0 && cc == 0) {
          const float er = bf2f(*(const u16*)(wq + 2 * (kd & 512))) * sc, ei = bf2f(*(const u16*)(wq + 32768 + 2 * (kd & 512))) * sc;
          if ((kd & 511) == 0) { fr[0] = 0.5f * (fr[0] + er); fi[0] = 0.5f * (fi[0] - ei); }
        }
      }
      char* pl = vi + tau * 1088;
      bf16x8 a = cat(trr(pl + (4 * g + q) * 32 + 8 * pq), trr(pl + 544 + (4 * g + q) * 32 + 8 * pq));
      f32x4 yr = mma16(a, fa_r, z4), yi = mma16(a, fa_i, z4);
      a = pk8(yr * t1c - yi * t1s, yr * t1s + yi * t1c);
      yr = mma16(fb_r, a, z4); yi = mma16(fb_i, a, z4);
      a = pk8(yr * fr - yi * fi, yr * fi + yi * fr);
      yr = mma16(a, gb_r, z4); yi = mma16(a, gb_i, z4);
      a = pk8(yr * t2c - yi * t2s, yr * t2s + yi * t2c);
      u32x4 o = __builtin_bit_cast(u32x4, pk8(mma16(a, ga_r, z4), mma16(a, ga_i, z4)));
      *(u32x2*)(pl + 32 * l15 + 8 * g) = u32x2{o.x, o.y}; *(u32x2*)(pl + 544 + 32 * l15 + 8 * g) = u32x2{o.z, o.w};
    }
    LBAR();
    f32x4 o[2][2] = {};
#pragma unroll
    for (int ks = 0; ks < 2; ++ks) if (ks == 0 || sp0) {
      const char* ap = vi + (ks ? 32 + q : 4 * g + q) * 544 + 64 * w + 16 * pq; const int st = ks ? 0 : 16 * 544;
      bf16x8 a0 = cat(trr(ap), trr(ap + st)), a1 = cat(trr(ap + 8), trr(ap + st + 8));
#pragma unroll
      for (int dl = 0; dl < 2; ++dl) { const bf16x8 mm = ks ? mix[dl] : mi[dl]; o[dl][0] = mma16(a0, mm, o[dl][0]); o[dl][1] = mma16(a1, mm, o[dl][1]); }
    }
#pragma unroll
    for (int dl = 0; dl < 2; ++dl) *(bf16x8*)(ws + WS_T + ub + l15 * 1024 + 512 * dl + 64 * w + 16 * g) = pk8(o[dl][0], o[dl][1]);
  }
  } else {
  const int kcol = pair_row(128 * a_half + 32 * wm + (lane & 31));
  u32x4 hf[2][4];
  hartley_load(hf, kcol, lane);
  const int p = 64 * pt + 32 * wn + (lane & 31), seg = 2 * wm + (lane >> 5);
  u32 q_dt = aldh(log_dt + p), q_lre = aldh(lam_re + p), q_lim = aldh(lam_im + p);
  {
    WAITV(0); BAR();
    TL(3);
    PIN(tag); PIN(q_dt); PIN(q_lre); PIN(q_lim);
    const f32x16 acc = hartley_mma<false>(lds + 3 * 32768, hf, kcol, lane, wn);
    const int r = 128 * a_half + 32 * wm + (lane & 31);
    const u32 off = (u32)(((r & ~1) * 16 + 2 * a_s2 + (r & 1)) * 1024 + 64 * a_ct + 32 * wn + 16 * (lane >> 5));
    u32x4 p0, p1;
#pragma unroll
    for (int i = 0; i < 4; ++i) {
      p0[i] = pk2(acc[2 * i] + acc[8 + 2 * i], acc[2 * i + 1] + acc[9 + 2 * i]);
      p1[i] = pk2(acc[2 * i] - acc[8 + 2 * i], acc[2 * i + 1] - acc[9 + 2 * i]);
    }
    __builtin_amdgcn_raw_buffer_store_b128(p0, ursrc, off, 0, 16);
    __builtin_amdgcn_raw_buffer_store_b128(p1, ursrc, off + 16384, 0, 16);
  }
  TS(1);
  f32x16 ur = {}, ui = {};
  cf lam, coef, l16;
  {
    const int h = lane >> 5, ra = 32 * wm + (lane & 31), rb = 64 * wn + (lane & 31), sw = (lane >> 1) & 7;
    bf16x8 fr[2][12];
    u32 aa[4], ab[4];
#pragma unroll
    for (int ks = 0; ks < 4; ++ks) { const int co = 16 * ((2 * ks + h) ^ sw); aa[ks] = ldsa(lds + ra * 128 + co); ab[ks] = ldsa(lds + rb * 128 + co); }
    tag = mix64(tag);
#pragma unroll
    for (int j = 0; j <= 16; ++j) {
      if (j == 3) WAITV(0);
      if (j < 16) LBAR();
      if (j == 0) TL(4); if (j == 3) TL(5); if (j == 6) TL(6); if (j == 11) TL(7);
      const int st = (j & 1) * 32768, adv = (j & 1) || j == 0 ? 0 : ((j & 2) ? 65536 : -65536);
#pragma unroll
      for (int ks = 0; ks < 4; ++ks) {
        if (!(j & 1)) { aa[ks] += adv; ab[ks] += adv; PIN(aa[ks]); PIN(ab[ks]); }
        if (j > 0) ur = mma32(fr[(j - 1) & 1][3 * ks], fr[(j - 1) & 1][3 * ks + 1], ur);
        SB();
        if (j < 16) { fr[j & 1][3 * ks] = ldsr(aa[ks], st); fr[j & 1][3 * ks + 1] = ldsr(ab[ks], st + 16384); }
        SB();
        if (j > 0) ui = mma32(fr[(j - 1) & 1][3 * ks], fr[(j - 1) & 1][3 * ks + 2], ui);
        SB();
        if (j < 16) fr[j & 1][3 * ks + 2] = ldsr(ab[ks], st + 20480);
        SB();
        if (j == 0 && ks == 3) {
          const float dt = rbf(expf(bf2f(q_dt))), mg = expf(dt * bf2f(q_lre)), an = dt * bf2f(q_lim);
          lam = {mg * cosf(an), mg * sinf(an)}; PIN(lam.r); PIN(lam.i);
        }
        if (j == 1 && ks == 0) {
          const float lre = bf2f(q_lre), lim = bf2f(q_lim), nr = lam.r - 1.f, ni = lam.i;
          const bool big = fabsf(lre) >= fabsf(lim);
          const float u = big ? lre : lim, v = big ? lim : lre, t = v / u, dn = u + v * t;
          coef = {2.f * ((big ? nr + ni * t : nr * t + ni) / dn), 2.f * ((big ? ni - nr * t : ni * t - nr) / dn)};
          l16 = lam;
#pragma unroll
          for (int i = 0; i < 4; ++i) l16 = cmul(l16, l16);
          PIN(coef.r); PIN(coef.i); PIN(l16.r); PIN(l16.i);
        }
        SB();
      }
    }
  }
  TS(2);
  u32x4 gx[2], gw[2];
    gx[0] = ald4<0>(x + (bid * 8 + w) * 1024 + 8 * lane); gx[1] = ald4<1024>(x + (bid * 8 + w) * 1024 + 8 * lane);
    gw[0] = ald4<0>(gate_w + 8 * lane); gw[1] = ald4<1024>(gate_w + 8 * lane);
    u32 warm = 0;
    if (w >= 6) warm = ald1(((w & 1) ? c_im : c_re) + bid * 4096 + 64 * lane);
#pragma unroll
  for (int i = 1; i < 16; ++i) { cf v = cfma(lam, {ur[i - 1], ui[i - 1]}, {ur[i], ui[i]}); ur[i] = v.r; ui[i] = v.i; }
  f32x2* segs = (f32x2*)(lds + 131072) + 32 * wn + (lane & 31);
  segs[seg * 64] = f32x2{ur[15], ui[15]};
  LBAR();
  const rsrc_t ar = mkr(ws + WS_AGG, 1 << 18);
  if (wm == 3 && tt < 15) {
    cf c = {0.f, 0.f};
#pragma unroll
    for (int j = 0; j < 8; ++j) { f32x2 e = segs[j * 64]; c = cfma(l16, c, {e.x, e.y}); }
    if (lane < 32) __builtin_amdgcn_raw_buffer_store_b128(u32x4{__builtin_bit_cast(u32, c.r), __builtin_bit_cast(u32, c.i), (u32)tag, (u32)(tag >> 32)}, ar, p * 16, tt * 16384, 16);
  }
  TM(3);
  {
    cf c = {0.f, 0.f}, lseg = coef;
#pragma unroll
    for (int j = 0; j < 7; ++j) { f32x2 e = segs[j * 64]; cf n = cfma(l16, c, {e.x, e.y}), m = cmul(lseg, l16); if (j < seg) { c = n; lseg = m; } }
    c = cmul(c, coef);
#pragma unroll
    for (int i = 0; i < 16; ++i) { const cf v = cmul(coef, {ur[i], ui[i]}); ur[i] = v.r; ui[i] = v.i; }
    PIN(ur); PIN(ui);
    {
      if (w >= 6) WAITV(1); else WAITV(0);
      float s = 0.f;
      PIN(gx[0]); PIN(gx[1]); PIN(gw[0]); PIN(gw[1]);
#pragma unroll
      for (int i = 0; i < 2; ++i)
#pragma unroll
        for (int e = 0; e < 4; ++e) {
          const u32 pr = pk2(bf2f(gx[i][e] & 0xffffu) * bf2f(gw[i][e] & 0xffffu), bf2f(gx[i][e] >> 16) * bf2f(gw[i][e] >> 16));
          s += bf2f(pr & 0xffffu) + bf2f(pr >> 16);
        }
#pragma unroll
      for (int o = 32; o > 0; o >>= 1) s += __shfl_xor(s, o);
      if (lane == 0) ((float*)(ws + WS_G))[bid * 8 + w] = rbf(1.f / (1.f + expf(-rbf(s))));
    }
    TM(4);
    if (tt > 0) {
      cf l128 = cmul(l16, l16); l128 = cmul(l128, l128); l128 = cmul(l128, l128);
      const cf l256 = cmul(l128, l128);
      const int j0 = tt - 1 - (lane >> 5);
      u32x4 v[8];
      for (u32 sp = 0; sp < SPIN_CAP; ++sp) {
        bool ok = true;
#pragma unroll
        for (int n = 0; n < 8; ++n) if (2 * n < tt) v[n] = __builtin_amdgcn_raw_buffer_load_b128(ar, max(j0 - 2 * n, 0) * 16384 + p * 16, 0, 16);
#pragma unroll
        for (int n = 0; n < 8; ++n) if (2 * n < tt) ok = ok && v[n].z == (u32)tag && v[n].w == (u32)(tag >> 32);
        if (__all(ok)) break;
        asm volatile("s_sleep 1" ::: "memory");
      }
      cf cb = {0.f, 0.f};
#pragma unroll
      for (int n = 7; n >= 0; --n) if (2 * n < tt) {
        const bool in = j0 - 2 * n >= 0;
        cb = cfma(l256, cb, {__uint_as_float(in ? v[n].x : 0u), __uint_as_float(in ? v[n].y : 0u)});
      }
      if (lane >= 32) cb = cmul(cb, l128);
      cb.r += __shfl_xor(cb.r, 32); cb.i += __shfl_xor(cb.i, 32);
      c = cfma(lseg, cb, c);
    }
    TM(5);
    WAITV(0); PIN(warm);
    BAR();
    const rsrc_t hr = mkr(ws + WS_H, 8 << 20);
    u32* tw = (u32*)(lds + 98304 + w * 4096) + (lane & 1) * 64 + ((lane >> 3) & 3) * 16 + (lane >> 5) * 8 + ((lane >> 1) & 3);
#pragma unroll
    for (int i = 0; i < 16; ++i) {
      c = cmul(c, lam);
      const u32 own = pk2(ur[i] + c.r, -(ui[i] + c.i)), oth = __builtin_amdgcn_mov_dpp(own, 0xB1, 0xf, 0xf, true);
      tw[(i & 7) * 128 + (i >> 3) * 4] = __builtin_amdgcn_perm(oth, own, (lane & 1) ? 0x03020706u : 0x05040100u);
    }
    const int k = 16 * tt + 4 * wm + (lane & 3), par = k >= 128, kk = par ? (256 - k) & 127 : k;
    const u32 ho = ((lane >> 5) << 20) + (kk >> 4) * 131072 + (2 * pt + ((lane >> 4) & 1)) * 4096 + (4 * wn + ((lane >> 2) & 3)) * 512 + par * 256 + (kk & 15) * 16;
#pragma unroll
    for (int r = 0; r < 4; ++r) __builtin_amdgcn_raw_buffer_store_b128(*(const u32x4*)(lds + 98304 + w * 4096 + r * 1024 + lane * 16), hr, ho, r << 21, HAUX);
  }
  }
  TM(7);
  if (bid == 1 && threadIdx.x < 256) {
    poll(fa + tid, true, tag);
  }
  if (bid == 1) { LBAR(); if (threadIdx.x == 0) __hip_atomic_store((u64*)(ws + WS_E), tag, RLX); }
}

__global__ __launch_bounds__(768) void k_back(const u16* __restrict__ x, const u16* __restrict__ c_re, const u16* __restrict__ c_im,
    const u16* __restrict__ dskip, const char* __restrict__ ws, float* __restrict__ out) {
  extern __shared__ __attribute__((aligned(16))) char lds[];
  const int tid = threadIdx.x & 511, lane = tid & 63, w = __builtin_amdgcn_readfirstlane(tid >> 6), wn = w & 1, wm = w >> 1;
  const bool loader = __builtin_amdgcn_readfirstlane(threadIdx.x >> 9);
  const int bid = blockIdx.x, xcd = bid & 7, jj = bid >> 3;
  const int rt = (xcd >> 1) * 4 + (jj & 3), ct = (xcd & 1) * 8 + (jj >> 2), s2 = rt >> 1, sh = rt & 1;
  const rsrc_t tr = mkr(ws + WS_T, 4 << 20), cr0 = mkr(c_re, 2 << 20), cr1 = mkr(c_im, 2 << 20);
  TS(8);
  if (loader) {
    const int lr = lane >> 3, lc = lane & 7, c = lc ^ (lr & 2);
#pragma unroll
    for (int i = 0; i < 8; ++i) { const int k = 32 * i + 8 * w + lr, kb = k < 128 ? k : (256 - k) & 127;
      blds(tr, kb * 32768 + (c >> 2) * 16384 + 16 * (c & 3), s2 * 2048 + 64 * ct + (i >= 4 ? 1024 : 0), lds + i * 4096 + w * 1024); }
    const int ra0 = 8 * w + lr, m = ra0 & 15, d = 32 * ct + 512 * ((ra0 >> 4) & 1) + 8 * ((m >> 2) & 1) + 4 * (m >> 3) + (m & 3);
    const u32 vC = d * 2048 + 16 * (lc ^ ((ra0 >> 1) & 7));
    auto issue = [&](int j) {
#pragma unroll
      for (int i = 0; i < 4; ++i) blds((i & 2) ? cr1 : cr0, vC, 128 * j + (i & 1) * 32768, lds + 32768 + (j % 7) * 16384 + i * 4096 + w * 1024);
    };
    issue(0);
    WAITV(4); BAR();
#pragma unroll
    for (int j = 1; j < 6; ++j) issue(j);
#pragma unroll
    for (int j = 0; j < 16; ++j) {
      if (j < 11) WAITV(20); else if (j == 11) WAITV(16); else if (j == 12) WAITV(12); else if (j == 13) WAITV(8); else if (j == 14) WAITV(4); else WAITV(0);
      BAR();
      if (j < 10) issue(j + 6);
    }
    BAR();
    return;
  }
  const int h = lane >> 5, kcol = pair_row(128 * sh + 32 * wm + 2 * (lane & 15) + ((lane >> 4) & 1)), srow = s2 + 8 * kcol, d0 = 32 * ct + 16 * wn + 8 * h;
  u32x4 hf[2][4];
  hartley_load(hf, kcol, lane);
  const char* hq = ws + WS_H + (size_t)(s2 * 8 + sh * 4 + wm) * 131072 + wn * 4096 + lane * 16;
  u32x4 hv[4][4];
  auto hload = [&](int j) { const char* a = hq + j * 8192; hv[j & 3][0] = ald4<0>(a); hv[j & 3][1] = ald4<1024>(a); hv[j & 3][2] = ald4<2048>(a); hv[j & 3][3] = ald4<3072>(a); };
  hload(0); hload(1);
  u32 ap[8];
  {
    WAITV(8); BAR();
    const f32x16 acc = hartley_mma<true>(lds, hf, kcol, lane, wn);
#pragma unroll
    for (int i = 0; i < 4; ++i) {
      ap[i] = pk2(acc[2 * i] + acc[8 + 2 * i], acc[2 * i + 1] + acc[9 + 2 * i]);
      ap[4 + i] = pk2(acc[2 * i] - acc[8 + 2 * i], acc[2 * i + 1] - acc[9 + 2 * i]);
    }
  }
  TS(9);
  hload(2);
  f32x16 acc = {}, acx = {};
  float gv; u32x4 xv[2], dv[2];
  {
    const int sw = (lane >> 1) & 7;
    bf16x8 fr[2][8];
    u32 aa[4], ab[4];
#pragma unroll
    for (int ks = 0; ks < 4; ++ks) { aa[ks] = ldsa(lds + 32768 + wn * 8192 + (lane & 31) * 128 + 16 * ((2 * ks + h) ^ sw)); ab[ks] = aa[ks] + 4096 * (1 - wn); aa[ks] += 4096 * wn; }
#pragma unroll
    for (int j = 0; j <= 16; ++j) {
      if (j < 16) LBAR();
      if (j > 0) {
        if (j < 15) WAITV(8); else if (j == 15) WAITV(9); else WAITV(5);
#pragma unroll
        for (int ks = 0; ks < 4; ++ks) PIN(hv[(j - 1) & 3][ks]);
        if (j < 14) hload(j + 2);
      }
      if (j == 14) {
        gv = __builtin_bit_cast(float, ald1((const float*)(ws + WS_G) + srow));
        xv[0] = ald4<0>(x + srow * 1024 + d0); xv[1] = ald4<1024>(x + srow * 1024 + d0); dv[0] = ald4<0>(dskip + d0); dv[1] = ald4<1024>(dskip + d0);
      }
      const int st = ((j % 7) % 3) * 16384, adv = j % 7 == 0 ? (j ? -98304 : 0) : ((j % 7) % 3 ? 0 : 49152);
#pragma unroll
      for (int ks = 0; ks < 4; ++ks) {
        if (adv || j == 0) { aa[ks] += adv; ab[ks] += adv; PIN(aa[ks]); PIN(ab[ks]); }
        if (j > 0) acc = mma32(fr[(j - 1) & 1][2 * ks], __builtin_bit_cast(bf16x8, hv[(j - 1) & 3][ks]), acc);
        SB();
        if (j < 16) fr[j & 1][2 * ks] = ldsr(aa[ks], st);
        SB();
        if (j > 0) acx = mma32(fr[(j - 1) & 1][2 * ks + 1], __builtin_bit_cast(bf16x8, hv[(j - 1) & 3][ks]), acx);
        SB();
        if (j < 16) fr[j & 1][2 * ks + 1] = ldsr(ab[ks], st);
        SB();
      }
    }
#pragma unroll
    for (int i = 0; i < 4; ++i) *(f32x4*)(lds + (w ^ 1) * 4096 + i * 1024 + lane * 16) = f32x4{acx[4 * i], acx[4 * i + 1], acx[4 * i + 2], acx[4 * i + 3]};
    LBAR();
#pragma unroll
    for (int i = 0; i < 4; ++i) { const f32x4 v = *(const f32x4*)(lds + w * 4096 + i * 1024 + lane * 16); acc[4 * i] += v[0]; acc[4 * i + 1] += v[1]; acc[4 * i + 2] += v[2]; acc[4 * i + 3] += v[3]; }
    WAITV(0); PIN(gv); PIN(xv[0]); PIN(xv[1]); PIN(dv[0]); PIN(dv[1]);
  }
  TS(10);
  const float og = rbf(1.f - gv);
#pragma unroll
  for (int i = 0; i < 2; ++i) {
    float o[8];
#pragma unroll
    for (int e = 0; e < 4; ++e) {
      const u32 sk = pk2(bf2f(xv[i][e] & 0xffffu) * bf2f(dv[i][e] & 0xffffu), bf2f(xv[i][e] >> 16) * bf2f(dv[i][e] >> 16));
      const u32 ga = pk2(gv * bf2f(ap[4 * i + e] & 0xffffu), gv * bf2f(ap[4 * i + e] >> 16));
      o[2 * e] = bf2f(ga & 0xffffu) + og * (acc[8 * i + 2 * e] + bf2f(sk & 0xffffu));
      o[2 * e + 1] = bf2f(ga >> 16) + og * (acc[8 * i + 2 * e + 1] + bf2f(sk >> 16));
    }
    float* op = out + srow * 1024 + d0 + 512 * i;
    *(f32x4*)op = f32x4{o[0], o[1], o[2], o[3]}; *(f32x4*)(op + 4) = f32x4{o[4], o[5], o[6], o[7]};
  }
  TS(11);
}

__global__ __launch_bounds__(512) void k_probe(const char* p0, const char* p1, const char* p2, const char* p3, const char* p4, const char* p5) {
  extern __shared__ __attribute__((aligned(16))) char lds[];
  const int lane = threadIdx.x & 63, w = __builtin_amdgcn_readfirstlane(threadIdx.x >> 6);
  const int b = blockIdx.x, q = b >> 5;
  const char* p = q < 2 ? p0 + (q << 21) : q < 4 ? p1 + ((q - 2) << 21) : q == 4 ? p2 : q == 5 ? p3 : q == 6 ? p4 : p5;
  const rsrc_t xr = mkr(p, 2 << 20);
  const long long t0 = __builtin_amdgcn_s_memrealtime();
#pragma unroll 1
  for (int j = 0; j < 64 / 8; ++j) { blds(xr, lane * 16, (b & 31) * 65536 + (j * 8 + w) * 1024, lds + ((j * 8 + w) & 63) * 1024); }
  WAITV(0); BAR();
  const long long t1 = __builtin_amdgcn_s_memrealtime();
  const long long e = t1 + (t1 - t0) * 0;
  while ((long long)__builtin_amdgcn_s_memrealtime() < e) __builtin_amdgcn_s_sleep(1);
}
extern "C" void kernel_launch(void* const* d_in, const int* in_sizes, int n_in, void* d_out, int out_size, void* d_ws, size_t ws_size, hipStream_t stream) {
  (void)in_sizes; (void)n_in; (void)out_size; (void)ws_size;
  (void)hipFuncSetAttribute((const void*)k_front, hipFuncAttributeMaxDynamicSharedMemorySize, LDS1);
  (void)hipFuncSetAttribute((const void*)k_back, hipFuncAttributeMaxDynamicSharedMemorySize, LDS2);
  const u16* const* in = (const u16* const*)d_in;
  k_probe<<<256, 512, 65536, stream>>>((const char*)d_in[0], (const char*)d_in[3], (const char*)d_in[6], (const char*)d_in[7], (const char*)d_in[8], (const char*)d_in[9]);
  k_front<<<256, 1024, LDS1, stream>>>(in[0], in[1], in[2], in[4], in[5], in[6], in[7], in[10], in[12], in[8], in[9], (char*)d_ws);
  k_back<<<256, 768, LDS2, stream>>>(in[0], in[8], in[9], in[11], (const char*)d_ws, (float*)d_out);
}
```

```cpp
#include <hip/hip_runtime.h>
#include <hip/hip_bf16.h>
#include <stdint.h>

typedef unsigned short u16;
typedef unsigned int u32;
typedef unsigned long long u64;
typedef __attribute__((ext_vector_type(4))) short s16x4;
typedef __attribute__((ext_vector_type(8))) __bf16 bf16x8;
typedef __attribute__((ext_vector_type(2))) __bf16 bf16x2;
typedef __attribute__((ext_vector_type(2))) float f32x2;
typedef __attribute__((ext_vector_type(4))) float f32x4;
typedef __attribute__((ext_vector_type(4))) u32 u32x4;
typedef __attribute__((ext_vector_type(2))) u32 u32x2;
#define DI __device__ __forceinline__
#define LDSP __attribute__((address_space(3)))
#define RLX __ATOMIC_RELAXED, __HIP_MEMORY_SCOPE_AGENT

constexpr u32 WS_E = 0, WS_FU = 4096, WS_FT = 8192, WS_G = 16384, WS_AGG = 65536;
constexpr u32 WS_U = 1u << 20, WS_T = 5u << 20, WS_H = 9u << 20, WS_END = 17u << 20;
constexpr u32 L_RA = 98304, L_W = 98304, L_WS = 17408, L_V = 133120, L_CH = 5264, L_PS = 144, L_SEG = 154176, L_SYNC = 158272, L_TOT = 163840;
constexpr u32 SPIN_CAP = 1u << 18, SENT = 0xffffffffu;

DI float bf2f(u32 h) { return __builtin_bit_cast(float, h << 16); }
DI float bfhi(u32 h) { return __builtin_bit_cast(float, h & 0xffff0000u); }
DI u32 pk2(float a, float b) { f32x2 v = {a, b}; return __builtin_bit_cast(u32, __builtin_convertvector(v, bf16x2)); }
DI u32 pk2(f32x2 v) { return __builtin_bit_cast(u32, __builtin_convertvector(v, bf16x2)); }
DI float rbf(float a) { return bf2f(pk2(a, a) & 0xffffu); }
typedef __amdgpu_buffer_rsrc_t rsrc_t;
DI rsrc_t mkr(const void* p, u32 bytes) { return __builtin_amdgcn_make_buffer_rsrc((void*)p, 0, bytes, 0x00020000); }
DI void blds(rsrc_t r, u32 vo, u32 so, u32 l) { asm volatile("s_mov_b32 m0, %3\n\ts_nop 0\n\tbuffer_load_dwordx4 %0, %1, %2 offen lds" :: "v"(vo), "s"(r), "s"(so), "s"(l) : "memory"); }
template <class T> DI T lr(u32 a) { return *(const LDSP T*)(uintptr_t)a; }
template <class T> DI void lw(u32 a, T v) { *(LDSP T*)(uintptr_t)a = v; }
DI u32x2 trr(u32 a) { return __builtin_bit_cast(u32x2, __builtin_amdgcn_ds_read_tr16_b64_v4i16((LDSP s16x4*)(uintptr_t)a)); }
DI bf16x8 cat(u32x2 a, u32x2 b) { u32x4 v = {a.x, a.y, b.x, b.y}; return __builtin_bit_cast(bf16x8, v); }
DI bf16x8 pk8(f32x4 a, f32x4 b) { u32x4 v = {pk2(a[0], a[1]), pk2(a[2], a[3]), pk2(b[0], b[1]), pk2(b[2], b[3])}; return __builtin_bit_cast(bf16x8, v); }
DI f32x4 mma(bf16x8 a, bf16x8 b, f32x4 c) { return __builtin_amdgcn_mfma_f32_16x16x32_bf16(a, b, c, 0, 0, 0); }
DI void mmz(f32x4& c, bf16x8 a, bf16x8 b) { asm volatile("v_mfma_f32_16x16x32_bf16 %0, %1, %2, 0" : "=&v"(c) : "v"(a), "v"(b)); }
DI void mmc(f32x4& c, bf16x8 a, bf16x8 b) { asm volatile("v_mfma_f32_16x16x32_bf16 %0, %1, %2, %0" : "+v"(c) : "v"(a), "v"(b)); }
DI bf16x8 asf(u32x4 v) { return __builtin_bit_cast(bf16x8, v); }
DI float cosr(float rev) { return __builtin_amdgcn_cosf(rev); }
DI float sinr(float rev) { return __builtin_amdgcn_sinf(rev); }
template <int O> DI u32x4 bl4(rsrc_t r, u32 vo, u32 so) { u32x4 v; asm volatile("buffer_load_dwordx4 %0, %1, %2, %3 offen offset:%4" : "=v"(v) : "v"(vo), "s"(r), "s"(so), "n"(O) : "memory"); return v; }
template <int O> DI u32x4 bl4c(rsrc_t r, u32 vo, u32 so) { u32x4 v; asm volatile("buffer_load_dwordx4 %0, %1, %2, %3 offen offset:%4 sc1" : "=v"(v) : "v"(vo), "s"(r), "s"(so), "n"(O) : "memory"); return v; }
DI u32 bl1c(rsrc_t r, u32 vo, u32 so) { u32 v; asm volatile("buffer_load_dword %0, %1, %2, %3 offen sc1" : "=v"(v) : "v"(vo), "s"(r), "s"(so) : "memory"); return v; }
DI u32 blh(rsrc_t r, u32 vo) { u32 v; asm volatile("buffer_load_ushort %0, %1, %2, 0 offen" : "=v"(v) : "v"(vo), "s"(r) : "memory"); return v; }
DI u32 gl1(const void* p) { u32 v; asm volatile("global_load_dword %0, %1, off" : "=v"(v) : "v"(p) : "memory"); return v; }
DI u32 glh(const void* p) { u32 v; asm volatile("global_load_ushort %0, %1, off" : "=v"(v) : "v"(p) : "memory"); return v; }
template <int O> DI u32x4 gl4(const void* p) { u32x4 v; asm volatile("global_load_dwordx4 %0, %1, off offset:%2" : "=v"(v) : "v"(p), "n"(O) : "memory"); return v; }
DI void st4c(u32x4 v, rsrc_t r, u32 vo, u32 so) { asm volatile("buffer_store_dwordx4 %0, %1, %2, %3 offen sc1\n\ts_nop 1" :: "v"(v), "v"(vo), "s"(r), "s"(so) : "memory"); }
DI void st2c(u64 v, rsrc_t r, u32 vo) { asm volatile("buffer_store_dwordx2 %0, %1, %2, 0 offen sc1" :: "v"(v), "v"(vo), "s"(r) : "memory"); }
DI void st4(u32x4 v, rsrc_t r, u32 vo, u32 so) { asm volatile("buffer_store_dwordx4 %0, %1, %2, %3 offen\n\ts_nop 1" :: "v"(v), "v"(vo), "s"(r), "s"(so) : "memory"); }
#define NOUNROLL _Pragma("unroll 1")
#define SB() __builtin_amdgcn_sched_barrier(0)
#define PINS(v) asm volatile("" : "+s"(v))
#define PIN(v) asm volatile("" : "+v"(v))
#define NOPS() do { SB(); asm volatile("s_nop 7\n\ts_nop 1"); SB(); } while (0)
#define WAITV(n) asm volatile("s_waitcnt vmcnt(%0)" :: "n"(n) : "memory")
#define WAITL(n) asm volatile("s_waitcnt lgkmcnt(%0)" :: "n"(n) : "memory")
#define FENCE() asm volatile("" ::: "memory")
#define BAR() do { FENCE(); __builtin_amdgcn_s_barrier(); FENCE(); } while (0)
#define LBAR() do { WAITL(0); __builtin_amdgcn_s_barrier(); FENCE(); } while (0)
DI u64 mix64(u64 e) { u64 z = e + 0x9e3779b97f4a7c15ull; z = (z ^ (z >> 30)) * 0xbf58476d1ce4e5b9ull; z = (z ^ (z >> 27)) * 0x94d049bb133111ebull; z ^= z >> 31; return z == e ? ~e : z; }
DI u32 umax3(u32 a, u32 b, u32 c) { return max(max(a, b), c); }
DI void gwait(u32 words, u32 phase) {
  NOUNROLL for (u32 s = 0; s < SPIN_CAP; ++s) {
    u32x4 v; asm volatile("ds_read_b128 %0, %1\n\ts_waitcnt lgkmcnt(0)" : "=v"(v) : "v"(words) : "memory");
    if (__builtin_amdgcn_readfirstlane(min(min(v.x, v.y), min(v.z, v.w))) >= phase) break;
  }
  FENCE();
}
DI void gpost(u32 words, int wq, u32 phase) { FENCE(); lw<u32>(words + 4 * wq, phase); FENCE(); }
DI void gbar(u32 words, int wq, u32 phase) { gpost(words, wq, phase); gwait(words, phase); }
DI bool tagged(u32x4 a, u32x4 b, u64 tag) { const u32 lo = (u32)tag, hi = (u32)(tag >> 32); return __all((((a.x ^ lo) | (a.y ^ hi) | (a.z ^ lo)) | ((a.w ^ hi) | (b.x ^ lo) | (b.y ^ hi)) | ((b.z ^ lo) | (b.w ^ hi))) == 0); }
DI void pollflags(rsrc_t wr, u32 off, int lane, u64 tag) {
  NOUNROLL for (u32 s = 0; s < SPIN_CAP; ++s) {
    u32x4 a = bl4c<0>(wr, lane * 16, off), b = bl4c<1024>(wr, lane * 16, off);
    WAITV(0); PIN(a); PIN(b);
    if (tagged(a, b, tag)) break;
    __builtin_amdgcn_s_sleep(1);
  }
}
#define TSW(n, t) do {} while (0)
#define TS(n) TSW(n, 0)
#define TX(n) TSW(n, 256)
#define ROT1 ((ti + pj + 8 * (xcd & 1) + 4 * (xcd >> 1)) & 15)
#define ROT2 (2 * (tt >> 2))
#define PRE 0
#define JF 5
#define WIN 3
struct c2 { f32x2 r, i; };
DI f32x2 fma2(f32x2 a, f32x2 b, f32x2 c) { return __builtin_elementwise_fma(a, b, c); }
DI c2 cfma(c2 a, c2 b, c2 c) { return {fma2(a.r, b.r, fma2(-a.i, b.i, c.r)), fma2(a.r, b.i, fma2(a.i, b.r, c.i))}; }
DI c2 cmul(c2 a, c2 b) { return {fma2(-a.i, b.i, a.r * b.r), fma2(a.r, b.i, a.i * b.r)}; }
DI c2 csq(c2 a) { return {fma2(a.r, a.r, -a.i * a.i), (a.r + a.r) * a.i}; }
DI c2 bc(c2 a, int ps) { return ps ? c2{a.r.yy, a.i.yy} : c2{a.r.xx, a.i.xx}; }
DI c2 csel(bool c, c2 a, c2 b) { return {c ? a.r : b.r, c ? a.i : b.i}; }
DI c2 asc(f32x4 v) { return {v.xy, v.zw}; }
DI void pinc(c2& a) { PIN(a.r); PIN(a.i); }
DI f32x2 spread16(float v) { const u32 u = __float_as_uint(v); const auto e = __builtin_amdgcn_permlane16_swap(u, u, false, false); return f32x2{__uint_as_float(e[0]), __uint_as_float(e[1])}; }
DI float sum16(float v) { const f32x2 e = spread16(v); return e.x + e.y; }
DI float sum32(float v) { const u32 u = __float_as_uint(v); const auto e = __builtin_amdgcn_permlane32_swap(u, u, false, false); return __uint_as_float(e[0]) + __uint_as_float(e[1]); }
template <int CTRL> DI float dppadd(float v) { return v + __int_as_float(__builtin_amdgcn_mov_dpp(__float_as_int(v), CTRL, 0xf, 0xf, true)); }

DI void hart_table(u32x4 (&tab)[2][4], int kap, bool sp, int g) {
#pragma unroll
  for (int sg = 0; sg < 2; ++sg)
#pragma unroll
    for (int r = 0; r < 4; ++r) {
      const float kk = (float)(sg == 0 ? kap : (sp ? (r == 1 ? 32 : -32) : -kap)) * (1.f / 256.f), a0 = fmaf(kk, (float)(32 * g + r), -0.125f);
#pragma unroll
      for (int j = 0; j < 4; ++j) tab[sg][r][j] = pk2(cosr(fmaf(kk, (float)(8 * j), a0)), cosr(fmaf(kk, (float)(8 * j + 4), a0)));
    }
}
DI void hart256(f32x4 (&o)[2][4], u32 ap, const u32x4 (&tab)[2][4], int kap, bool sp) {
  const float sgn = (kap & 1) ? -1.f : 1.f;
  f32x4 p[2][4];
#pragma unroll
  for (int r = 0; r < 4; ++r) {
    const bf16x8 a0 = cat(trr(ap + r * 8192), trr(ap + r * 8192 + 512)), a1 = cat(trr(ap + r * 8192 + 4096), trr(ap + r * 8192 + 4608));
#pragma unroll
    for (int sg = 0; sg < 2; ++sg) p[sg][r] = mma(a0, asf(tab[sg][r]), f32x4{}) + sgn * mma(a1, asf(tab[sg][r]), f32x4{});
  }
  const float al = sp ? 0.f : 1.f;
  const f32x4 s0 = p[0][1] + p[0][3], d0 = p[0][1] - p[0][3], s1 = al * p[1][1] + p[1][3], d1 = p[1][1] - al * p[1][3];
  const f32x4 x0 = sp ? d0 : d1, x1 = sp ? d1 : d0;
#pragma unroll
  for (int sg = 0; sg < 2; ++sg) {
    const f32x4 A = p[sg][0] + p[sg][2], B = p[sg][0] - p[sg][2], S = sg ? s1 : s0, X = sg ? x1 : x0;
    o[sg][0] = A + S; o[sg][2] = A - S; o[sg][1] = B + X; o[sg][3] = B - X;
#pragma unroll
    for (int qq = 0; qq < 4; ++qq) {
      const u32x4 v = __builtin_bit_cast(u32x4, o[sg][qq]);
      const auto e0 = __builtin_amdgcn_permlane32_swap(v[0], v[2], false, false), e1 = __builtin_amdgcn_permlane32_swap(v[1], v[3], false, false);
      const f32x2 a = {__uint_as_float(e0[0]), __uint_as_float(e1[0])}, b = {__uint_as_float(e0[1]), __uint_as_float(e1[1])};
      const f32x2 s = a + b, d = a - b;
      o[sg][qq] = f32x4{s.x, s.y, d.x, d.y};
    }
  }
}

struct TileD { int k, c, src, w; };
DI TileD tile_desc(int tau, int k1, int kd1) {
  TileD t; const int i = tau >> 1, ka = tau & 1, hi = i >= 5;
  t.c = kd1 + 2 * ka;
  t.src = k1 ? -1 : (kd1 ? ka : hi);
  t.k = 256 * i + (k1 ? k1 : (kd1 ? 128 * ka : -1152 * hi));
  t.w = k1 ? 1 : (kd1 ? 2 : (tau >= 18 ? 0 : ((i & 3) == 0 && i < 8 ? 1 : 2)));
  return t;
}
DI float m0val(const TileD& t, int ri, int dl, int s2, int rs) {
  float a = (float)(s2 * t.k) * (-1.f / 2048.f) - 0.25f * (float)(dl * t.c + ri);
  a += t.src < 0 ? (rs ? 0.125f : -0.125f) : 0.f;
  const float v = cosr(a);
  return (t.src >= 0 && (t.w == 0 || t.src != rs)) ? 0.f : v;
}
DI void m0_fwd(bf16x8& o, int n, int l15, int g, int k1, int kd1) {
  const TileD t = tile_desc(8 * n + (l15 >> 1), k1, kd1); f32x4 v0, v1;
#pragma unroll
  for (int j = 0; j < 4; ++j) { v0[j] = m0val(t, l15 & 1, 0, 2 * g + (j >> 1), j & 1); v1[j] = m0val(t, l15 & 1, 1, 2 * g + (j >> 1), j & 1); }
  o = pk8(v0, v1);
}
DI void m0_inv(bf16x8& o, int ks, int dl, int l15, int g, int k1, int kd1) {
  f32x4 v[2];
#pragma unroll
  for (int j = 0; j < 8; ++j) { const int pn = 32 * ks + 16 * (j >> 2) + 4 * g + (j & 3);
    const TileD t = tile_desc(pn >> 1, k1, kd1); v[j >> 2][j & 3] = (float)t.w * m0val(t, pn & 1, dl, l15 >> 1, l15 & 1); }
  o = pk8(v[0], v[1]);
}

template <int O> DI void ldhi(float& v, u32 a) { asm volatile("ds_read_u16_d16_hi %0, %1 offset:%2" : "+v"(v) : "v"(a), "n"(O) : "memory"); }
constexpr int g2_extra(bool isx, int u) { return u < 0 ? 0 : (u == 0 || ((u & 1) && u < 9) ? 4 : 0) + (u == 12 ? (isx ? 2 : 4) : 0) + (u == 4 - WIN ? 2 : 0) + (isx ? (u == 3 ? 1 : (u == 0 || u == 4 || u == 6) ? 4 : 0) : 0); }
constexpr int g2_wait(bool isx, int s) { int c = (isx && s < WIN - 1) ? 1 : 0; for (int u = s - WIN + 2; u < s; ++u) c += g2_extra(isx, u) + (u + WIN - 1 < 16 ? 4 : 0); return c; }
template <int N> struct IC { static constexpr int v = N; };
template <int I, int N, class F> DI void sfor(F&& f) { if constexpr (I < N) { f(IC<I>{}); sfor<I + 1, N>(f); } }

typedef __attribute__((ext_vector_type(4))) u64 u64x4;
typedef __attribute__((ext_vector_type(2))) u64 u64x2;
__global__ __launch_bounds__(512, 2) void k_all(const u16*, const u16*, const u16*, const u16*, const u16*, const u16*, const u16* __restrict__ wre, const u16* __restrict__ wim,
    const u16* __restrict__ c_re, const u16* __restrict__ c_im, const u16* __restrict__ dskip, const u16* __restrict__ gate_w, char* __restrict__ ws, float* __restrict__ out) {
  u64x4 ka; u64x2 kb_;
  asm volatile("s_load_dwordx8 %0, %2, 0x0\n\ts_load_dwordx4 %1, %2, 0x20" : "=&s"(ka), "=&s"(kb_) : "s"(__builtin_amdgcn_kernarg_segment_ptr()) : "memory");
  extern __shared__ __attribute__((aligned(16))) char lds_[];
  const u32 L = (u32)(uintptr_t)(LDSP char*)lds_;
  const int lane = threadIdx.x & 63, wv = __builtin_amdgcn_readfirstlane(threadIdx.x >> 6), w = wv & 3, rh = wv >> 2, l15 = lane & 15, g = lane >> 4;
  const int bid = blockIdx.x, xcd = bid & 7, jj = bid >> 3, ti = jj & 3, pj = jj >> 2;
  const int tt = (xcd >> 1) * 4 + ti, pt = (xcd & 1) * 8 + pj;
  const int s2 = tt >> 1, sh = tt & 1, ct = pt;
  const int lr8 = lane >> 3, lc8 = lane & 7, q = l15 >> 2, pq = lane & 3;
  int rot1 = ROT1, rot2 = ROT2;
  {
    u64 pc; u32 nb; asm volatile("s_getpc_b64 %0\n.Lcode_lo:\n\ts_mov_b32 %1, .Lcode_hi-.Lcode_lo" : "=s"(pc), "=s"(nb));
    if (wv == 7) { const rsrc_t pr = mkr((const void*)(pc & ~127ull), nb); asm volatile("s_mov_b32 m0, %2\n\ts_nop 0\n\tbuffer_load_dword %0, %1, 0 offen lds" :: "v"((lane * 32 + jj) * 128), "s"(pr), "s"(L + L_SYNC + 256) : "memory"); }
  }
  const int ii = 8 * (w & 1) + lr8, swz = 16 * (lc8 ^ ((ii >> 1) & 7)), ac = lc8 ^ (((lr8 >> 1) & 1) | ((w & 1) << 1)), r0 = 2 * sh + (ct & 1);
  u32 vB = (u32)(64 * pt + ii) * 2048u + swz, vA = (u32)(s2 + 32 * (8 * w + lr8)) * 2048u + 64 * ct + 16 * (ac & 3) + 1024 * (ac >> 2);
  u32 vP = (64 * pt + 32 * (w & 1) + (lane & 31)) * 2 + (rh << 12);
  PIN(vA); PIN(vB); PIN(vP);
  asm volatile("s_waitcnt lgkmcnt(0)" : "+s"(ka), "+s"(kb_) :: "memory");
  const u16 *x = (const u16*)ka[0], *b_re = (const u16*)ka[1], *b_im = (const u16*)ka[2], *log_dt = (const u16*)ka[3], *lam_re = (const u16*)kb_[0], *lam_im = (const u16*)kb_[1];
  const rsrc_t xr = mkr(x, 4u << 20), br = mkr((w & 2) ? b_im : b_re, 2u << 20);
  auto issue_a = [&](int rr, int hf) { const int r = (rr + r0) & 3; blds(xr, vA, (8 * r + 1024 * hf) * 2048, L + L_RA + r * 8192 + hf * 4096 + w * 1024); };
  auto issue_b1 = [&](int j, int i) { blds(br, vB, 16 * i * 2048 + ((j + rot1) & 15) * 128, L + (j % 3) * 32768 + 16384 + w * 1024 + i * 4096); };
  u32 q_dt = blh(mkr(log_dt, 2048), vP), q_lr = blh(mkr(lam_re, 2048), vP), q_li = blh(mkr(lam_im, 2048), vP);
  issue_a(0, rh); issue_b1(0, 2 * rh); issue_b1(0, 2 * rh + 1);
  SB();
  const rsrc_t wsr = mkr(ws, WS_END);
  const rsrc_t cr = mkr((w & 2) ? c_im : c_re, 2u << 20);
  auto c_issue = [&](int i, int half) {
    const u32 vC = (u32)(32 * ct + lr8) * 2048u + 1024u * (w & 1) + 16 * (lc8 ^ (lr8 >> 1));
    const u32 reg = L + ((i + 1) & 3) * 32768 + w * 8192 + half * 4096, so = (u32)half * (512 * 2048) + ((i + rot2) & 7) * 128;
#pragma unroll
    for (int e = 0; e < 4; ++e) blds(cr, vC ^ ((e & 1) * 64), so + e * 8 * 2048, reg + e * 1024);
  };
  f32x4 acc[4][4];
  const u32 vG = WS_G + (u32)(((2 * w + (lane & 1)) * 2 + (((bid >> 4) & 1) ^ ((bid & 32) && (bid & 15)))) * 128 + ((bid >> 5) & 1) * 64 + (bid & 15) * 4 + (bid >> 6)) * 4;
  u32 gq[2]; u32x4 gv[2], gw[2];
  auto gload = [&](bool isx) {
    const u32 vGo = WS_G + tt * 512 + (u32)((w & 1) * 64 + lr8 * 4 + 2 * rh + (w >> 1)) * 4;
    gq[0] = bl1c(wsr, vGo, 0); gq[1] = bl1c(wsr, vGo + 128, 0);
    if (!isx) { gv[0] = bl4c<0>(wsr, WS_G + tt * 512 + l15 * 16, 0); gv[1] = bl4c<256>(wsr, WS_G + tt * 512 + ((16 - l15) & 15) * 16, 0); }
  };
  auto gemm2 = [&](auto isx_, u64 tag) {
    constexpr bool ISX = decltype(isx_)::v;
    u32x4 hv[WIN][4], f0, f1, tv[8], gx[4]; u32 ta[8];
    const u32 hb = WS_H + (u32)(tt * 8 + 4 * rh) * 65536u + w * 16384, vl = lane * 16;
    auto hload1 = [&](int s, int m) { const u32 so = hb + ((m + w) & 3) * 65536 + (((s >> 1) + rot2) & 7) * 2048; hv[s % WIN][m] = (s & 1) ? bl4c<1024>(wsr, vl, so) : bl4c<0>(wsr, vl, so); };
    auto hok = [&](int s) { u32 mx = 0;
#pragma unroll
      for (int m = 0; m < 4; ++m) { PIN(hv[s % WIN][m]); mx = umax3(umax3(mx, hv[s % WIN][m].x, hv[s % WIN][m].y), hv[s % WIN][m].z, hv[s % WIN][m].w); }
      return __all(mx != SENT); };
    bf16x8 fc[2][4];
    u32 ca[2];
#pragma unroll
    for (int ks = 0; ks < 2; ++ks) ca[ks] = L + w * 8192 + l15 * 128 + 16 * ((4 * ks + g) ^ (l15 >> 1));
    auto crd = [&](int s, int n) { fc[s & 1][n] = lr<bf16x8>(ca[s & 1] + (((s >> 1) + 1) & 3) * 32768 + n * 2048); };
    if (ISX) {
#pragma unroll
      for (int i = 0; i < 8; ++i) {
        const int kb = 16 * i + 4 * w + g, k = (lane & 4) ? (kb ? 256 - kb : 128) : kb, rho = 64 * (k & 3) + (k >> 2);
        ta[i] = L + 32768 + rho * 128 + 16 * ((((lane >> 1) & 4) | pq) ^ (((rho >> 1) & 1) | ((rho >> 3) & 1) << 1)); PIN(ta[i]);
      }
    }
    WAITV(0); LBAR();
    if (!ISX) tag = lr<u64>(L + L_SYNC + 32);
    NOUNROLL for (u32 spn = 0; spn < SPIN_CAP; ++spn) {
      WAITV(0);
#pragma unroll
      for (int s = 0; s < WIN - 1; ++s)
#pragma unroll
        for (int m = 0; m < 4; ++m) hload1(s, m);
      if (ISX) st2c(tag, wsr, threadIdx.x == 256 && spn == 0 ? WS_FT + bid * 8 : WS_END);
      WAITV(g2_wait(ISX, 0));
      if (hok(0)) break;
    }
#pragma unroll
    for (int n = 0; n < 4; ++n) crd(0, n);
    sfor<0, 16>([&](auto s_) {
      constexpr int s = decltype(s_)::v;
      PINS(rot2);
      if (s > 0) {
        WAITV(g2_wait(ISX, s));
        if (s >= 4) {
#pragma unroll
          for (int m = 0; m < 4; ++m) PIN(hv[s % WIN][m]);
        } else if (__builtin_expect(!hok(s), 0)) {
          NOUNROLL for (u32 spn = 0; spn < SPIN_CAP; ++spn) {
#pragma unroll
            for (int m = 0; m < 4; ++m) hload1(s, m);
            WAITV(0);
            if (hok(s)) break;
          }
        }
      }
      if (s == 0) TS(12);
      if (s == 5 - WIN) { WAITV(4); PIN(f0); PIN(f1); if (__builtin_expect(!tagged(f0, f1, tag), 0)) pollflags(wsr, WS_FT, lane, tag); }
      if ((s & 1) && s < 10) LBAR();
      if (s == 0) c_issue(3, rh); else if ((s & 1) && s < 9) c_issue((s >> 1) + 4, rh);
      if (ISX && s == 0) { const u32 xo = (bid * 8 + 2 * w) * 2048; gx[0] = bl4<0>(xr, vl, xo); gx[1] = bl4<1024>(xr, vl, xo); gx[2] = bl4<2048>(xr, vl, xo); gx[3] = bl4<3072>(xr, vl, xo); }
      if (ISX && s == 3) {
        float sm[2] = {0.f, 0.f};
#pragma unroll
        for (int i = 0; i < 4; ++i) {
          PIN(gx[i]);
#pragma unroll
          for (int e = 0; e < 4; ++e) { const u32 a = gx[i][e], b = gw[i & 1][e]; sm[i >> 1] = __builtin_amdgcn_fdot2_f32_bf16(__builtin_bit_cast(bf16x2, a), __builtin_bit_cast(bf16x2, b), sm[i >> 1], false); }
        }
        sm[0] = dppadd<0xB1>(sm[0]); sm[1] = dppadd<0xB1>(sm[1]);
        float t = (lane & 1) ? sm[1] : sm[0];
        t = dppadd<0x4E>(t); t = dppadd<0x124>(t); t = dppadd<0x128>(t); t = sum32(sum16(t));
        __builtin_amdgcn_raw_buffer_store_b32(__float_as_uint(rbf(1.f / (1.f + expf(-rbf(t))))), wsr, lane < 2 ? vG : WS_END, 0, 16);
      }
      if (s == 12) gload(ISX);
      if (s == 4 - WIN) { f0 = bl4c<0>(wsr, vl, WS_FT); f1 = bl4c<1024>(wsr, vl, WS_FT); }
      if (ISX && (s == 4 || s == 6)) {
#pragma unroll
        for (int i = 0; i < 4; ++i) tv[2 * s - 8 + i] = bl4c<0>(wsr, vl, WS_T + (s2 * 16 + ct) * 32768 + (2 * s - 8 + i) * 4096 + w * 1024);
      }
      SB();
#pragma unroll
      for (int qq = 0; qq < 16; ++qq) {
        const int m = qq >> 2, n = (m & 1) ? 3 - (qq & 3) : (qq & 3);
        if (s) mmc(acc[m][n], fc[s & 1][n], asf(hv[s % WIN][m])); else mmz(acc[m][n], fc[s & 1][n], asf(hv[s % WIN][m]));
        SB();
        if (s < 15 && qq < 4) crd(s + 1, qq);
        if (s + WIN - 1 < 16 && (qq & 3) == 1) hload1(s + WIN - 1, qq >> 2);
        if (ISX && s == 9 && qq >= 8) { PIN(tv[qq - 8]); lw<u32x4>(ta[qq - 8], tv[qq - 8]); }
        SB();
      }
      if (ISX && s == 9) gpost(L + L_SYNC + 16, w, 4);
    });
    NOPS();
    NOUNROLL for (u32 spn = 0; spn < SPIN_CAP; ++spn) {
      PIN(gq[0]); PIN(gq[1]);
      u32 mx = max(gq[0], gq[1]);
      if (!ISX) { PIN(gv[0]); PIN(gv[1]); mx = umax3(umax3(gv[0].x, gv[0].y, gv[0].z), umax3(gv[0].w, gv[1].x, gv[1].y), umax3(gv[1].z, gv[1].w, mx)); }
      if (__all(mx != SENT)) break;
      gload(ISX); WAITV(0);
    }
  };

  if (rh) {
    const u32 vX = (u32)(128 * tt + 16 * (ii >> 2) + 8 * (ii & 1) + 2 * (w >> 1) + ((ii >> 1) & 1)) * 2048u + swz;
    auto issue_x = [&](int j) {
      const u32 l = L + (j % 3) * 32768 + w * 1024, kk = ((j + rot1) & 15) * 128;
#pragma unroll
      for (int i = 0; i < 4; ++i) blds(xr, vX, (64 * (i >> 1) + 4 * (i & 1)) * 2048 + kk, l + i * 4096);
    };
    auto issue_b = [&](int j) { issue_b1(j, 0); issue_b1(j, 1); issue_b1(j, 2); issue_b1(j, 3); };
    u64 tag; asm volatile("buffer_load_dwordx2 %0, off, %1, 0 sc1" : "=v"(tag) : "s"(wsr) : "memory");
    gw[0] = gl4<0>(gate_w + 8 * lane); gw[1] = gl4<1024>(gate_w + 8 * lane);
    TX(16);
    const int k1 = xcd * 16 + (jj >> 1), kd1 = jj & 1, cc = kd1 + 2 * (w & 1);
    const bool sp0 = (k1 == 0 && kd1 == 0);
    bf16x8 m0[2], mi[2], fa_r, fa_i, fb_r, fb_i, gb_r, gb_i, ga_r, ga_i; f32x4 t1c, t1s, t2c, t2s;
    const float fl = (float)l15, fc = (float)cc;
    u32 vW[5];
    SB();
    bf16x8 m0x, mix[2];
    u32 wa[5], wo[5]; float fr[4][4], fi[4][4], er[4], ei[4];
#pragma unroll
    for (int e = 0; e < 4; ++e) { const float a = (float)(4 * g + e) * (fc + 4.f * fl) * (-1.f / 64.f); t2c[e] = cosr(a); t2s[e] = sinr(a); t1c[e] = cosr(a * (1.f / 16.f)); t1s[e] = sinr(a * (1.f / 16.f)); }
    fa_r = pk8(t2c, -t2s); fa_i = pk8(t2s, t2c); PIN(fa_r); PIN(fa_i); PIN(t1c); PIN(t1s);
    {
      const float fm = (float)(l15 < 8 ? l15 : l15 ^ 3) * (-1.f / 16.f), sl = l15 < 8 ? 1.f : -1.f;
#pragma unroll
      for (int e = 0; e < 4; ++e) { const float a = (float)(4 * g + e) * fm; t2c[e] = cosr(a); t2s[e] = sinr(a); }
      fb_r = pk8(t2c, -t2s); fb_i = pk8(sl * t2s, sl * t2c); PIN(fb_r); PIN(fb_i);
    }
    if (__builtin_expect(k1 != 0, 1)) { m0_fwd(m0[0], 0, l15, g, k1, kd1); m0_fwd(m0[1], 1, l15, g, k1, kd1); }
    else { m0_fwd(m0[0], 0, l15, g, 0, kd1); m0_fwd(m0[1], 1, l15, g, 0, kd1); m0_fwd(m0x, 2, l15, g, 0, 0); }
    PIN(m0[0]); PIN(m0[1]);
#pragma unroll
    for (int i = 0; i < 5; ++i) {
      const int id = 64 * (w + 4 * i) + lane, slot = min((id * 993) >> 16, 15);
      const int row = k1 ? (slot < 8 ? k1 + 256 * slot : 4096 - k1 - 256 * slot) : 128 * slot;
      vW[i] = ((row * 1026) & ~15) + 16 * (id - 66 * slot); PIN(vW[i]);
    }
    SB();
    TX(17);
#pragma unroll
    for (int i = 2; i < 8; ++i) issue_a(i >> 1, i & 1);
    issue_x(0);
    TX(18);
    WAITV(4); BAR();
    issue_b(1); issue_x(1);
    PIN(tag); PIN(gw[0]); PIN(gw[1]); PIN(q_dt); PIN(q_lr); PIN(q_li); tag = mix64(tag);
    if (w == 0) lw<u64>(L + L_SYNC + 32, tag);
#pragma unroll
    for (int i = 0; i < 5; ++i) {
      const int k = tile_desc(w + 4 * i, k1, kd1).k, nk = (2048 - k) & 2047;
      const u32 wp = (k1 ? k >> 8 : k >> 7) * 1056 + ((k * 1026) & 15), wq = (k1 ? 8 + (k >> 8) : nk >> 7) * 1056 + ((nk * 1026) & 15);
      const int t = 2 * cc + 8 * l15 + 512 * g;
      wa[i] = L + L_W + (g < 2 ? wp + t : wq + 1664 - t); wo[i] = L + L_W + (g < 2 ? wq : wp + 1024); PIN(wa[i]); PIN(wo[i]);
    }
#pragma unroll
    for (int i = 0; i < 16; ++i) { fr[i >> 2][i & 3] = fi[i >> 2][i & 3] = 0.f; PIN(fr[i >> 2][i & 3]); PIN(fi[i >> 2][i & 3]); if (i < 4) { er[i] = ei[i] = 0.f; PIN(er[i]); PIN(ei[i]); } }
    u32x4 uq[4], f0, f1;
    const u32 ub = WS_U + (u32)(2 * k1 + kd1) * 16384u + w * 2048;
#pragma unroll
    for (int j = 0; j < 16; ++j) {
      if (j == 10) WAITV(10); else if (j == 13) WAITV(12); else WAITV(8);
      BAR();
      PINS(rot1);
      if (j == 3) TX(28); if (j == 6) TX(29); if (j == 9) TX(30); if (j == 12) TX(31);
      if (j == JF && w == 0) st2c(tag, wsr, lane == 0 ? WS_FU + bid * 8 : WS_END);
      if (j == 11) {
        PIN(f0); PIN(f1);
        if (w == 1 && __builtin_expect(!tagged(f0, f1, tag), 0)) pollflags(wsr, WS_FU, lane, tag);
        if (bid == 0 && w == 1) st2c(tag, wsr, lane == 0 ? WS_E : WS_END);
      }
      SB();
      if (j < 14) { issue_b(j + 2); issue_x(j + 2); }
      if (j == 9) { const u32 so = w == 1 ? WS_FU : WS_END; f0 = bl4c<0>(wsr, lane * 16, so); f1 = bl4c<1024>(wsr, lane * 16, so); }
      if (j == 12) { uq[0] = bl4c<0>(wsr, lane * 16, ub); uq[1] = bl4c<1024>(wsr, lane * 16, ub); uq[2] = bl4c<0>(wsr, lane * 16, ub + 8192); uq[3] = bl4c<1024>(wsr, lane * 16, ub + 8192); }
      if (j >= 14) {
        const rsrc_t w0 = mkr(wre, 2048 * 1026), w1 = mkr(wim, 2048 * 1026);
#pragma unroll
        for (int i = 0; i < 5; ++i) if (j == 14 ? i < 4 : (i == 4 && w == 0)) { blds(w0, vW[i], 0, L + L_W + (w + 4 * i) * 1024); blds(w1, vW[i], 0, L + L_W + L_WS + (w + 4 * i) * 1024); }
      }
      SB();
    }
    TX(8);
    const f32x4 z4 = {};
    const u32 vo = L + L_V + w * L_CH;
#pragma unroll
    for (int i = 0; i < 4; ++i) { PIN(uq[i]); lw<u32x4>(vo + (16 * (i >> 1) + (lane >> 2)) * L_PS + (i & 1) * 64 + pq * 16, uq[i]); }
    FENCE();
    {
      const u32 ap = vo + (4 * g + q) * L_PS + 16 * pq;
      bf16x8 a[2][2];
#pragma unroll
      for (int cb = 0; cb < 2; ++cb) { a[cb][0] = cat(trr(ap + 64 * cb), trr(ap + 64 * cb + 16 * L_PS)); a[cb][1] = cat(trr(ap + 64 * cb + 8), trr(ap + 64 * cb + 16 * L_PS + 8)); }
      FENCE();
#pragma unroll
      for (int n = 0; n < 3; ++n) if (n < 2 || sp0) {
        const bf16x8 mm = n < 2 ? m0[n & 1] : m0x;
#pragma unroll
        for (int cb = 0; cb < 2; ++cb) {
          const bf16x8 o = pk8(mma(a[cb][0], mm, z4), mma(a[cb][1], mm, z4));
          if (n < 2 || l15 < 4) lw<bf16x8>(vo + (16 * n + l15) * L_PS + 64 * cb + 16 * g, o);
        }
      }
    }
    TX(25);
    c_issue(0, 0); c_issue(0, 1);
    TX(26);
    SB();
    {
      const float sg = g < 2 ? 1.f : -1.f; f32x4 c, sn;
#pragma unroll
      for (int e = 0; e < 4; ++e) { const float a = (float)(4 * g + (g < 2 ? e : 3 - e)) * fl * (-1.f / 16.f); c[e] = cosr(a); sn[e] = sinr(a); }
      gb_r = pk8(c, sg * sn); gb_i = pk8(-sn, sg * c); PIN(gb_r); PIN(gb_i);
#pragma unroll
      for (int e = 0; e < 4; ++e) {
        const float a = fl * (fc + 4.f * (float)(4 * g + e)) * (1.f / 64.f); c[e] = cosr(a); sn[e] = sinr(a);
        t2c[e] = cosr(a * (1.f / 16.f)) * (1.f / 1048576.f); t2s[e] = sinr(a * (1.f / 16.f)) * (1.f / 1048576.f);
      }
      ga_r = pk8(c, -sn); ga_i = pk8(sn, c); PIN(ga_r); PIN(ga_i); PIN(t2c); PIN(t2s);
      if (__builtin_expect(k1 != 0, 1)) { m0_inv(mi[0], 0, 0, l15, g, k1, kd1); m0_inv(mi[1], 0, 1, l15, g, k1, kd1); }
      else { m0_inv(mi[0], 0, 0, l15, g, 0, kd1); m0_inv(mi[1], 0, 1, l15, g, 0, kd1); m0_inv(mix[0], 1, 0, l15, g, 0, 0); m0_inv(mix[1], 1, 1, l15, g, 0, 0); }
      PIN(mi[0]); PIN(mi[1]);
    }
    SB();
    WAITV(8);
    gbar(L + L_SYNC + 16, w, 1);
    TX(9);
    auto tiles = [&](auto nt_, auto i0_) {
      constexpr int NT = decltype(nt_)::v, i0 = decltype(i0_)::v;
      const u32 pr = L + L_V + (w + 4 * i0) * 2 * L_PS + g * L_CH + q * 32 + 8 * pq, pw_ = L + L_V + (w + 4 * i0) * 2 * L_PS + q * L_CH + pq * 32 + 8 * g;
      bf16x8 a[NT]; f32x4 yr[NT], yi[NT];
#pragma unroll
      for (int i = 0; i < NT; ++i) a[i] = cat(trr(pr + i * 8 * L_PS), trr(pr + i * 8 * L_PS + L_PS));
      SB();
#pragma unroll
      for (int i = 0; i < NT; ++i) { yr[i] = mma(a[i], fa_r, z4); yi[i] = mma(a[i], fa_i, z4); }
      SB();
#pragma unroll
      for (int i = 0; i < NT; ++i) {
        if (i0) {
#pragma unroll
          for (int r = 0; r < 4; ++r) fr[i][r] = fi[i][r] = er[i] = ei[i] = 0.f;
        }
        ldhi<0>(fr[i][0], wa[i0 + i]); ldhi<128>(fr[i][1], wa[i0 + i]); ldhi<256>(fr[i][2], wa[i0 + i]); ldhi<384>(fr[i][3], wa[i0 + i]);
        ldhi<L_WS>(fi[i][0], wa[i0 + i]); ldhi<L_WS + 128>(fi[i][1], wa[i0 + i]); ldhi<L_WS + 256>(fi[i][2], wa[i0 + i]); ldhi<L_WS + 384>(fi[i][3], wa[i0 + i]);
      }
#pragma unroll
      for (int i = 0; i < NT; ++i) { ldhi<0>(er[i], wo[i0 + i]); ldhi<L_WS>(ei[i], wo[i0 + i]); }
      SB();
#pragma unroll
      for (int i = 0; i < NT; ++i) { a[i] = pk8(yr[i] * t1c - yi[i] * t1s, yr[i] * t1s + yi[i] * t1c); yr[i] = mma(fb_r, a[i], z4); yi[i] = mma(fb_i, a[i], z4); }
      SB(); WAITL(0);
#pragma unroll
      for (int i = 0; i < NT; ++i)
#pragma unroll
        for (int r = 0; r < 4; ++r) { PIN(fr[i][r]); PIN(fi[i][r]); PIN(er[i]); PIN(ei[i]); }
      if (cc == 0) {
        const float b0 = lane == 0 ? 0.5f : 0.f, b3 = lane == 32 ? 0.5f : 0.f;
#pragma unroll
        for (int i = 0; i < NT; ++i) {
          float t;
          asm volatile("v_sub_f32 %4, %5, %0\n\tv_fmac_f32 %0, %7, %4\n\tv_add_f32 %4, %6, %1\n\tv_fma_f32 %1, -%7, %4, %1\n\t"
                       "v_sub_f32 %4, %5, %2\n\tv_fmac_f32 %2, %8, %4\n\tv_add_f32 %4, %6, %3\n\tv_fma_f32 %3, -%8, %4, %3"
                       : "+v"(fr[i][0]), "+v"(fi[i][0]), "+v"(fr[i][3]), "+v"(fi[i][3]), "=&v"(t) : "v"(er[i]), "v"(ei[i]), "v"(b0), "v"(b3));
        }
      }
#pragma unroll
      for (int i = 0; i < NT; ++i) {
        const f32x4 vr = {fr[i][0], fr[i][1], fr[i][2], fr[i][3]}, vi = {fi[i][0], fi[i][1], fi[i][2], fi[i][3]};
        a[i] = pk8(yr[i] * vr - yi[i] * vi, yr[i] * vi + yi[i] * vr); yr[i] = mma(a[i], gb_r, z4); yi[i] = mma(a[i], gb_i, z4);
      }
#pragma unroll
      for (int i = 0; i < NT; ++i) { a[i] = pk8(yr[i] * t2c - yi[i] * t2s, yr[i] * t2s + yi[i] * t2c); yr[i] = mma(a[i], ga_r, z4); yi[i] = mma(a[i], ga_i, z4); }
#pragma unroll
      for (int i = 0; i < NT; ++i) { lw<u32x2>(pw_ + i * 8 * L_PS, u32x2{pk2(yr[i].xy), pk2(yr[i].zw)}); lw<u32x2>(pw_ + i * 8 * L_PS + L_PS, u32x2{pk2(yi[i].xy), pk2(yi[i].zw)}); }
    };
    tiles(IC<4>{}, IC<0>{});
    if (__builtin_expect(sp0 && w < 2, 0)) tiles(IC<1>{}, IC<4>{});
    TX(23);
    gbar(L + L_SYNC + 16, w, 2);
    TX(24);
    {
      f32x4 o[2][2][2] = {};
#pragma unroll
      for (int ks = 0; ks < 2; ++ks) if (ks == 0 || sp0) {
        const u32 ap = vo + (ks ? 32 + q : 4 * g + q) * L_PS + 16 * pq, st = ks ? 0 : 16 * L_PS;
#pragma unroll
        for (int cb = 0; cb < 2; ++cb) {
          const bf16x8 a0 = cat(trr(ap + 64 * cb), trr(ap + 64 * cb + st)), a1 = cat(trr(ap + 64 * cb + 8), trr(ap + 64 * cb + st + 8));
#pragma unroll
          for (int dl = 0; dl < 2; ++dl) { const bf16x8 mm = ks ? mix[dl] : mi[dl]; o[dl][cb][0] = mma(a0, mm, o[dl][cb][0]); o[dl][cb][1] = mma(a1, mm, o[dl][cb][1]); }
        }
      }
      FENCE();
#pragma unroll
      for (int i = 0; i < 4; ++i) lw<bf16x8>(vo + i * 1280 + l15 * 80 + 16 * g, pk8(o[i >> 1][i & 1][0], o[i >> 1][i & 1][1]));
      FENCE();
      const u32 to = (u32)lr8 * (16 * 32768) + (u32)(2 * k1 + kd1) * 128 + lc8 * 16;
#pragma unroll
      for (int i = 0; i < 4; ++i) st4c(lr<u32x4>(vo + i * 1280 + (lane >> 2) * 80 + pq * 16), wsr, to, WS_T + (8 * (i >> 1) + 2 * w + (i & 1)) * 32768);
    }
    TX(10);
    c_issue(1, 0); c_issue(1, 1);
    c_issue(2, 0); c_issue(2, 1);
    TX(11);
    gemm2(IC<1>{}, tag);
    TX(27);
    BAR();
  } else {
    const int wn = w & 1, wm = w >> 1;
    const int hpos = 8 * wm + lc8, hsh = (tt & 2) ? (hpos ? (tt & 1) ^ 1 : tt & 1) : (tt & 1);
    const u32 vH = (u32)(lr8 & 1) * (1u << 20) + hsh * (1u << 19) + (tt >> 1) * 65536 + (2 * pt + wn) * 1024 + g * 256 + hpos * 16;
    const u32 vAg = WS_AGG + (u32)((pt * 2 + wn) * 16 + l15) * 16;
    if (w == 0 && lane < 8) lw<u32>(L + L_SYNC + 4 * lane, 0u);
    const int kap = 16 * sh + l15; const bool sp = kap == 0;
    const int ecol = 8 * w + 4 * (g & 1) + 2 * (g >> 1);
    u32 q_ds[2] = {gl1(dskip + 32 * ct + ecol), gl1(dskip + 512 + 32 * ct + ecol)};
    u32x4 tab[2][4];
    u32 xa[8], ua[8], hap = (8 * g + q) * 128 + 16 * ((4 * (pq >> 1) + w) ^ (((q >> 1) & 1) | ((g & 1) << 1))) + 8 * (pq & 1);
    SB();
    TS(19);
    {
      const u32x4 sv = {SENT, SENT, SENT, SENT};
      st4c(sv, wsr, vH, WS_H + ((pt & 7) >> 1) * (2u << 20)); st4c(sv, wsr, vH, WS_H + ((pt & 7) >> 1) * (2u << 20) + 32768);
      st4c(sv, wsr, lane < 16 ? vAg : WS_END, wm ? tt * 8192 : WS_END);
      __builtin_amdgcn_raw_buffer_store_b32(SENT, wsr, lane < 2 ? vG : WS_END, 0, 16);
    }
    SB();
    hart_table(tab, kap, sp, g);
#pragma unroll
    for (int i = 0; i < 8; ++i) PIN(tab[i >> 2][i & 3]);
#pragma unroll
    for (int i = 0; i < 8; ++i) {
      const int n = (i < 4 ? kap + 64 * i : 64 * (i & 3) - (sp ? 32 : kap)) & 255, rho = 64 * (n & 3) + (n >> 2), kb = n < 128 ? n : (256 - n) & 127, lid = ((kb >> 5) << 4) | (kb & 15);
      xa[i] = L + L_RA + rho * 128 + 16 * (w ^ (((rho >> 1) & 1) | ((rho >> 3) & 1) << 1)) + 2 * (ecol & 7); PIN(xa[i]);
      ua[i] = L + L_V + lid * 256 + (((n >> 7) * 64 + 2 * ecol) ^ ((lid & 7) << 4)); PIN(ua[i]);
    }
    PIN(hap);
    TS(20);
    c2 lam, coef, l8, l16;
    WAITV(9);
    {
      TS(21);
      PIN(q_dt); PIN(q_lr); PIN(q_li);
      const float lre = bf2f(q_lr), lim = bf2f(q_li);
      const float dt = rbf(expf(bf2f(q_dt))), mg = expf(dt * lre);
      float sn, cs; sincosf(dt * lim, &sn, &cs);
      const float nr = mg * cs - 1.f, ni = mg * sn;
      const bool big = fabsf(lre) >= fabsf(lim);
      const float u = big ? lre : lim, v = big ? lim : lre, t = v / u, dn = u + v * t;
      lam = {spread16(mg * cs), spread16(ni)};
      coef = {spread16(2.f * ((big ? nr + ni * t : nr * t + ni) / dn)), spread16(2.f * ((big ? ni - nr * t : ni * t - nr) / dn))};
      l8 = csq(csq(csq(lam))); l16 = csq(l8);
      pinc(lam); pinc(coef); pinc(l8); pinc(l16);
    }
    u32 sk[8][2];
    TS(22);
    WAITV(4); PIN(q_ds[0]); PIN(q_ds[1]);
    LBAR();
    TS(1);
    {
      f32x4 o[2][4];
      hart256(o, L + L_RA + hap, tab, kap, sp);
#pragma unroll
      for (int i = 0; i < 8; ++i) { sk[i][0] = lr<u32>(xa[i]); sk[i][1] = lr<u32>(xa[i] + 64); }
#pragma unroll
      for (int i = 0; i < 8; ++i) { lw<u32>(ua[i], pk2(o[i >> 2][i & 3].xy)); lw<u32>(ua[i] + 128, pk2(o[i >> 2][i & 3].zw)); }
    }
    {
      bf16x8 fa[2][4], fb[2][4];
      u32 aa[2], ab[2];
#pragma unroll
      for (int ks = 0; ks < 2; ++ks) { const int co = 16 * ((4 * ks + g) ^ (l15 >> 1)); aa[ks] = L + (64 * wm + l15) * 128 + co; ab[ks] = L + 16384 + (64 * wn + l15) * 128 + co; }
      auto rd1 = [&](int j, int ks, int i) {
        const u32 bs = (j % 3) * 32768 + (i >> 1) * 2048;
        if (i & 1) fb[ks][i >> 1] = lr<bf16x8>(ab[ks] + bs); else fa[ks][i >> 1] = lr<bf16x8>(aa[ks] + bs);
      };
      LBAR();
      TS(2);
#pragma unroll
      for (int i = 0; i < 8; ++i) { PIN(sk[i][0]); PIN(sk[i][1]); }
#pragma unroll
      for (int i = 0; i < 8; ++i) rd1(0, 0, i);
      u32x4 uv[4];
#pragma unroll
      for (int j = 0; j < 16; ++j) {
#pragma unroll
        for (int ks = 0; ks < 2; ++ks) {
          const bool nx = ks == 0 || j < 15;
          if (ks == 1 && j < 15) { if (j == JF - 1) WAITV(0); LBAR(); }
          SB();
#pragma unroll
          for (int qq = 0; qq < 16; ++qq) {
            const int m = qq >> 2, n = (m & 1) ? 3 - (qq & 3) : (qq & 3);
            if (j | ks) mmc(acc[m][n], fa[ks][m], fb[ks][n]); else mmz(acc[m][n], fa[ks][m], fb[ks][n]);
            SB();
            if (nx && qq < 8) rd1(j + ks, ks ^ 1, qq);
            if (j == 0 && ks == 0 && qq >= 12) { const int lid = 16 * w + 4 * (qq - 12) + g; uv[qq - 12] = lr<u32x4>(L + L_V + lid * 256 + (lr8 & 1) * 128 + 16 * (lc8 ^ (lid & 7))); }
            if (j == 0 && ks == 1 && qq >= 8 && (qq & 1)) {
              const int i = (qq - 8) >> 1, lo = 4 * i + g, kb = 64 * (w >> 1) + ((w & 1) ? 32 + lo + 16 * (lo ? 1 - sh : sh) : 16 * sh + lo);
              st4c(uv[i], wsr, (u32)(2 * kb + (lr8 & 1)) * 16384u + lc8 * 16, WS_U + ct * 1024 + s2 * 128);
            }
            SB();
          }
        }
      }
      NOPS();
    }
    TS(3);
    c2 h[2][8];
    const u32 sa = L + L_SEG + wn * 256 + l15 * 16;
#pragma unroll
    for (int s = 0; s < 8; ++s)
#pragma unroll
      for (int ps = 0; ps < 2; ++ps) {
        const f32x4 vr = acc[s >> 1][2 * ps], vi = acc[s >> 1][2 * ps + 1];
        h[ps][s] = (s & 1) ? c2{vr.zw, vi.zw} : c2{vr.xy, vi.xy};
        if (s) h[ps][s] = cfma(bc(lam, ps), h[ps][s - 1], h[ps][s]);
      }
    const c2 ea = {{h[0][7].r.x, h[1][7].r.x}, {h[0][7].i.x, h[1][7].i.x}};
    { const c2 e = cfma(l8, ea, c2{{h[0][7].r.y, h[1][7].r.y}, {h[0][7].i.y, h[1][7].i.y}});
      lw<f32x4>(sa + (4 * wm + g) * 512, f32x4{e.r.x, e.r.y, e.i.x, e.i.y}); }
    gbar(L + L_SYNC, w, 1);
    c2 cin;
    {
      c2 c[9];
      c[0] = c2{{0.f, 0.f}, {0.f, 0.f}}; c[1] = asc(lr<f32x4>(sa));
      if (wm) {
#pragma unroll
        for (int j = 1; j < 8; ++j) c[j + 1] = cfma(l16, c[j], asc(lr<f32x4>(sa + j * 512)));
        st4c(__builtin_bit_cast(u32x4, f32x4{c[8].r.x, c[8].r.y, c[8].i.x, c[8].i.y}), wsr, lane < 16 ? vAg : WS_END, tt * 8192);
        SB();
        cin = csel(g & 2, csel(g & 1, c[7], c[6]), csel(g & 1, c[5], c[4]));
      } else {
#pragma unroll
        for (int j = 1; j < 3; ++j) c[j + 1] = cfma(l16, c[j], asc(lr<f32x4>(sa + j * 512)));
        cin = csel(g & 2, csel(g & 1, c[3], c[2]), csel(g & 1, c[1], c[0]));
      }
    }
    TS(4);
    SB();
    c2 pw[8], wt[4], lsa, lsb, ca, cz;
    u32 wa[8], ra[8]; u32x4 v[4];
    {
      const c2 one = {{1.f, 1.f}, {0.f, 0.f}}, zero = {{0.f, 0.f}, {0.f, 0.f}};
      const c2 l32 = csq(l16), l64 = csq(l32), l128 = csq(l64), l256 = csq(l128), l512 = csq(l256);
      lsa = cmul(cmul(coef, csel(g & 1, l16, one)), cmul(csel(g & 2, l32, one), csel(wm, l64, one))); lsb = cmul(lsa, l8);
      wt[0] = cmul(csel(g & 1, l128, one), csel(g & 2, l256, one));
#pragma unroll
      for (int n = 1; n < 4; ++n) wt[n] = cmul(wt[n - 1], l512);
#pragma unroll
      for (int n = 0; n < 4; ++n) { wt[n] = csel(tt - 1 - g - 4 * n >= 0, wt[n], zero); pinc(wt[n]); }
      pw[0] = lam;
#pragma unroll
      for (int s = 1; s < 8; ++s) { pw[s] = s == 7 ? l8 : cmul(pw[s - 1], lam); pinc(pw[s]); }
      ca = cmul(coef, cin); cz = cmul(coef, cfma(l8, cin, ea));
      pinc(lsa); pinc(lsb); pinc(ca); pinc(cz);
      SB();
#pragma unroll
      for (int n = 0; n < 4; ++n) v[n] = bl4c<0>(wsr, vAg + max(tt - 1 - g - 4 * n, 0) * 8192, 0);
      SB();
#pragma unroll
      for (int s = 0; s < 8; ++s)
#pragma unroll
        for (int ps = 0; ps < 2; ++ps) { h[ps][s] = cmul(bc(coef, ps), h[ps][s]); pinc(h[ps][s]); }
      const u32 hs = L + w * 8192, z0 = (pt & 7) >> 1;
#pragma unroll
      for (int i = 0; i < 8; ++i) {
        wa[i] = hs + l15 * 128 + ((4 * g + 16 * i) ^ (8 * (l15 & 1) + 4 * (l15 & 4) + 16 * (l15 & 2) + 8 * (l15 & 8))); PIN(wa[i]);
        ra[i] = hs + (8 * g + q) * 128 + ((8 * pq) ^ (8 * (q & 1) + 16 * (q & 2) + 64 * (g & 1)) ^ (32 * ((z0 - (i >> 1)) & 3)) ^ (16 * (i & 1))); PIN(ra[i]);
      }
    }
    SB();
    TS(5);
    WAITV(0);
#pragma unroll
    for (int n = 0; n < 4; ++n) PIN(v[n]);
    if (tt > 0) {
      NOUNROLL for (u32 spn = 0; spn < SPIN_CAP; ++spn) {
        u32 mx = 0;
#pragma unroll
        for (int n = 0; n < 4; ++n) { PIN(v[n]); mx = umax3(umax3(mx, v[n].x, v[n].y), v[n].z, v[n].w); }
        if (__all(mx != SENT)) break;
#pragma unroll
        for (int n = 0; n < 4; ++n) v[n] = bl4c<0>(wsr, vAg + max(tt - 1 - g - 4 * n, 0) * 8192, 0);
        WAITV(0);
      }
      c2 cb = cfma(wt[0], asc(__builtin_bit_cast(f32x4, v[0])), cmul(wt[1], asc(__builtin_bit_cast(f32x4, v[1]))));
      const c2 cd = cfma(wt[2], asc(__builtin_bit_cast(f32x4, v[2])), cmul(wt[3], asc(__builtin_bit_cast(f32x4, v[3]))));
      cb.r += cd.r; cb.i += cd.i;
      cb = c2{{sum32(sum16(cb.r.x)), sum32(sum16(cb.r.y))}, {sum32(sum16(cb.i.x)), sum32(sum16(cb.i.y))}};
      ca = cfma(lsa, cb, ca); cz = cfma(lsb, cb, cz);
    }
    TS(6);
    {
      const c2 cc[2] = {{{ca.r.x, cz.r.x}, {ca.i.x, cz.i.x}}, {{ca.r.y, cz.r.y}, {ca.i.y, cz.i.y}}};
#pragma unroll
      for (int s = 0; s < 8; ++s)
#pragma unroll
        for (int ps = 0; ps < 2; ++ps) { const c2 v = cfma(bc(pw[s], ps), cc[ps], h[ps][s]); lw<u32>(wa[s] + ps * 2048, pk2(v.r)); lw<u32>(wa[s] + ps * 2048 + 4096, pk2(-v.i)); }
      FENCE();
      const u32 z0 = (pt & 7) >> 1;
#pragma unroll
      for (int i = 0; i < 8; ++i)
        st4c(__builtin_bit_cast(u32x4, cat(trr(ra[i & 6] + (i & 1) * 4096), trr(ra[i | 1] + (i & 1) * 4096 + 512))), wsr, vH, WS_H + ((z0 - (i >> 1)) & 3) * (2u << 20) + (i & 1) * 32768);
      WAITL(0);
    }
    TS(7);
#pragma unroll
    for (int i = 0; i < 16; ++i) { const u32 xv = sk[i >> 1][i & 1], dv = q_ds[i & 1]; sk[i >> 1][i & 1] = pk2(bf2f(xv & 0xffffu) * bf2f(dv & 0xffffu), bfhi(xv) * bfhi(dv)); PIN(sk[i >> 1][i & 1]); }
    gemm2(IC<0>{}, 0);
    TS(13);
    f32x4 o[2][4] = {};
    gwait(L + L_SYNC + 16, 4);
    hart256(o, L + 32768 + hap, tab, kap, sp);
#pragma unroll
    for (int i = 0; i < 8; ++i) {
      const float gg = __uint_as_float(gv[i >> 2][i < 4 ? i : (i + 3) & 3]), og = rbf(1.f - gg);
      const f32x4 sv = {bf2f(sk[i][0] & 0xffffu), bfhi(sk[i][0]), bf2f(sk[i][1] & 0xffffu), bfhi(sk[i][1])};
      o[i >> 2][i & 3] = gg * o[i >> 2][i & 3] + og * sv;
      PIN(o[i >> 2][i & 3]);
    }
    LBAR();
    const int ech = 2 * w + (g & 1), np = (16 - l15) & 15;
#pragma unroll
    for (int i = 0; i < 8; ++i) {
      const int pos = i < 4 ? l15 : np, rb = i < 4 ? 2 * i : 2 * ((i + 3) & 3) + 1;
#pragma unroll
      for (int hf = 0; hf < 2; ++hf) lw<f32x2>(L + 131072 + (rb * 16 + pos) * 256 + 16 * ((8 * hf + ech) ^ pos) + 8 * (g >> 1), hf ? o[i >> 2][i & 3].zw : o[i >> 2][i & 3].xy);
    }
  }
  TS(14);
#pragma unroll
  for (int m = 0; m < 4; ++m) {
    const u32 a = L + ((rh * 4 + ((m + w) & 3)) * 4 + m) * 4096 + g * 256 + (l15 ^ (4 * g)) * 16;
#pragma unroll
    for (int n = 0; n < 4; ++n) lw<f32x4>(a + n * 1024, acc[m][n]);
  }
  {
    const rsrc_t outr = mkr(out, 8u << 20);
    u32 pa[2], ba[4], oa[2]; float og[2];
#pragma unroll
    for (int hi = 0; hi < 2; ++hi) {
      const int row = lr8 + 8 * hi, s1 = 32 * wv + row + 16 * (sh ^ (w & 1 & (row != 0)));
      pa[hi] = L + wv * 16384 + lc8 * 256 + (row ^ (4 * pq)) * 16; oa[hi] = (u32)(s2 + 8 * s1) * 4096u + lc8 * 16; og[hi] = rbf(1.f - __uint_as_float(gq[hi]));
      ba[2 * hi] = L + 131072 + (wv * 16 + row) * 256 + 16 * (lc8 ^ row); ba[2 * hi + 1] = ba[2 * hi] ^ 128;
      PIN(pa[hi]); PIN(oa[hi]); PIN(og[hi]); PIN(ba[2 * hi]); PIN(ba[2 * hi + 1]);
    }
    LBAR();
    f32x4 v[4][5];
#pragma unroll
    for (int i = 0; i < 4; ++i) {
#pragma unroll
      for (int u = 0; u < 4; ++u) v[i][u] = lr<f32x4>(pa[i >> 1] + (i & 1) * 2048 + u * 4096);
      v[i][4] = lr<f32x4>(ba[i]);
    }
    SB();
#pragma unroll
    for (int i = 0; i < 4; ++i)
      st4(__builtin_bit_cast(u32x4, og[i >> 1] * ((v[i][0] + v[i][1]) + (v[i][2] + v[i][3])) + v[i][4]), outr, oa[i >> 1], 128 * ct + 2048 * (i & 1));
  }
  TS(15);
  asm volatile(".Lcode_hi:");
}

extern "C" void kernel_launch(void* const* d_in, const int* in_sizes, int n_in, void* d_out, int out_size, void* d_ws, size_t ws_size, hipStream_t stream) {
  (void)in_sizes; (void)n_in; (void)out_size; (void)ws_size;
  (void)hipFuncSetAttribute((const void*)k_all, hipFuncAttributeMaxDynamicSharedMemorySize, L_TOT);
  const u16* const* in = (const u16* const*)d_in;
  k_all<<<256, 512, L_TOT, stream>>>(in[0], in[6], in[7], in[10], in[4], in[5], in[1], in[2], in[8], in[9], in[11], in[12], (char*)d_ws, (float*)d_out);
}
```

```cpp
#include <hip/hip_runtime.h>
#include <hip/hip_bf16.h>
#include <stdint.h>

typedef unsigned short u16;
typedef unsigned int u32;
typedef unsigned long long u64;
typedef __attribute__((ext_vector_type(4))) short s16x4;
typedef __attribute__((ext_vector_type(8))) __bf16 bf16x8;
typedef __attribute__((ext_vector_type(2))) __bf16 bf16x2;
typedef __attribute__((ext_vector_type(2))) float f32x2;
typedef __attribute__((ext_vector_type(4))) float f32x4;
typedef __attribute__((ext_vector_type(4))) u32 u32x4;
typedef __attribute__((ext_vector_type(2))) u32 u32x2;
#define DI __device__ __forceinline__
#define LDSP __attribute__((address_space(3)))
#define RLX __ATOMIC_RELAXED, __HIP_MEMORY_SCOPE_AGENT

#define NREP 8
constexpr u32 WS_E = 0, WS_G = 16384, WS_AGG = 65536, WS_FU = 0x40000, WS_FH = 0x60000;
constexpr u32 WS_U = 1u << 20, WS_T = 5u << 20, WS_H = 9u << 20, WS_END = 17u << 20;
constexpr u32 L_RA = 65536, L_W = 98304, L_WS = 17408, L_V = 133120, L_CH = 5264, L_PS = 144, L_SYNC = 154176, L_SEG = 154688, L_TOT = 163840;
constexpr u32 SPIN_CAP = 1u << 18, SENT = 0xffffffffu;

DI float bf2f(u32 h) { return __builtin_bit_cast(float, h << 16); }
DI float bfhi(u32 h) { return __builtin_bit_cast(float, h & 0xffff0000u); }
DI u32 pk2(float a, float b) { f32x2 v = {a, b}; return __builtin_bit_cast(u32, __builtin_convertvector(v, bf16x2)); }
DI u32 pk2(f32x2 v) { return __builtin_bit_cast(u32, __builtin_convertvector(v, bf16x2)); }
DI float rbf(float a) { return bf2f(pk2(a, a) & 0xffffu); }
typedef __amdgpu_buffer_rsrc_t rsrc_t;
DI rsrc_t mkr(const void* p, u32 bytes) { return __builtin_amdgcn_make_buffer_rsrc((void*)p, 0, bytes, 0x00020000); }
DI void bldsc(rsrc_t r, u32 vo, u32 so, u32 l, u32 lo) { asm volatile("s_add_u32 m0, %3, %4\n\ts_nop 0\n\tbuffer_load_dwordx4 %0, %1, %2 offen sc1 lds" :: "v"(vo), "s"(r), "s"(so), "s"(l), "i"(lo) : "memory", "scc"); }
DI void blds(rsrc_t r, u32 vo, u32 so, u32 l, u32 lo) { asm volatile("s_add_u32 m0, %3, %4\n\ts_nop 0\n\tbuffer_load_dwordx4 %0, %1, %2 offen lds" :: "v"(vo), "s"(r), "s"(so), "s"(l), "i"(lo) : "memory", "scc"); }
template <class T> DI T lr(u32 a) { return *(const LDSP T*)(uintptr_t)a; }
template <class T> DI void lw(u32 a, T v) { *(LDSP T*)(uintptr_t)a = v; }
DI u32x2 trr(u32 a) { return __builtin_bit_cast(u32x2, __builtin_amdgcn_ds_read_tr16_b64_v4i16((LDSP s16x4*)(uintptr_t)a)); }
DI bf16x8 cat(u32x2 a, u32x2 b) { u32x4 v = {a.x, a.y, b.x, b.y}; return __builtin_bit_cast(bf16x8, v); }
DI bf16x8 pk8(f32x4 a, f32x4 b) { u32x4 v = {pk2(a[0], a[1]), pk2(a[2], a[3]), pk2(b[0], b[1]), pk2(b[2], b[3])}; return __builtin_bit_cast(bf16x8, v); }
DI f32x4 mma(bf16x8 a, bf16x8 b, f32x4 c) { return __builtin_amdgcn_mfma_f32_16x16x32_bf16(a, b, c, 0, 0, 0); }
DI void mmz(f32x4& c, bf16x8 a, bf16x8 b) { asm volatile("v_mfma_f32_16x16x32_bf16 %0, %1, %2, 0" : "=&v"(c) : "v"(a), "v"(b)); }
DI void mmc(f32x4& c, bf16x8 a, bf16x8 b) { asm volatile("v_mfma_f32_16x16x32_bf16 %0, %1, %2, %0" : "+v"(c) : "v"(a), "v"(b)); }
DI bf16x8 asf(u32x4 v) { return __builtin_bit_cast(bf16x8, v); }
DI float cosr(float rev) { return __builtin_amdgcn_cosf(rev); }
DI float sinr(float rev) { return __builtin_amdgcn_sinf(rev); }
template <int O> DI u32x4 bl4(rsrc_t r, u32 vo, u32 so) { u32x4 v; asm volatile("buffer_load_dwordx4 %0, %1, %2, %3 offen offset:%4" : "=v"(v) : "v"(vo), "s"(r), "s"(so), "n"(O) : "memory"); return v; }
template <int O> DI u32x4 bl4c(rsrc_t r, u32 vo, u32 so) { u32x4 v; asm volatile("buffer_load_dwordx4 %0, %1, %2, %3 offen offset:%4 sc1" : "=v"(v) : "v"(vo), "s"(r), "s"(so), "n"(O) : "memory"); return v; }
DI u32 bl1c(rsrc_t r, u32 vo, u32 so) { u32 v; asm volatile("buffer_load_dword %0, %1, %2, %3 offen sc1" : "=v"(v) : "v"(vo), "s"(r), "s"(so) : "memory"); return v; }
DI void blh(rsrc_t r, u32 vo, u32 l, u32 lo) { asm volatile("s_add_u32 m0, %2, %3\n\ts_nop 0\n\tbuffer_load_ushort %0, %1, 0 offen lds" :: "v"(vo), "s"(r), "s"(l), "i"(lo) : "memory", "scc"); }
DI u32 gl1(const void* p) { u32 v; asm volatile("global_load_dword %0, %1, off" : "=v"(v) : "v"(p) : "memory"); return v; }
DI u32 glh(const void* p) { u32 v; asm volatile("global_load_ushort %0, %1, off" : "=v"(v) : "v"(p) : "memory"); return v; }
template <int O> DI u32x4 gl4(const void* p) { u32x4 v; asm volatile("global_load_dwordx4 %0, %1, off offset:%2" : "=v"(v) : "v"(p), "n"(O) : "memory"); return v; }
DI void st4c(u32x4 v, rsrc_t r, u32 vo, u32 so) { asm volatile("buffer_store_dwordx4 %0, %1, %2, %3 offen sc1\n\ts_nop 1" :: "v"(v), "v"(vo), "s"(r), "s"(so) : "memory"); }
DI void st2c(u64 v, rsrc_t r, u32 vo, u32 so) { asm volatile("buffer_store_dwordx2 %0, %1, %2, %3 offen sc1" :: "v"(v), "v"(vo), "s"(r), "s"(so) : "memory"); }
DI void st4(u32x4 v, rsrc_t r, u32 vo, u32 so) { asm volatile("buffer_store_dwordx4 %0, %1, %2, %3 offen\n\ts_nop 1" :: "v"(v), "v"(vo), "s"(r), "s"(so) : "memory"); }
#define POLLSLP 2
#define NOUNROLL _Pragma("unroll 1")
#define SB() __builtin_amdgcn_sched_barrier(0)
template <class T> DI void PINS(T& v) { asm volatile("" : "+s"(v)); }
template <class T> DI void PIN(T& v) { asm volatile("" : "+v"(v)); }
#define NOPS() do { SB(); asm volatile("s_nop 7\n\ts_nop 1"); SB(); } while (0)
#define WAITV(n) asm volatile("s_waitcnt vmcnt(%0)" :: "n"(n) : "memory")
#define WAITL(n) asm volatile("s_waitcnt lgkmcnt(%0)" :: "n"(n) : "memory")
#define FENCE() asm volatile("" ::: "memory")
#define BAR() do { FENCE(); __builtin_amdgcn_s_barrier(); FENCE(); } while (0)
#define LBAR() do { WAITL(0); __builtin_amdgcn_s_barrier(); FENCE(); } while (0)
DI u32 umax3(u32 a, u32 b, u32 c) { return max(max(a, b), c); }
DI void gwait(u32 words, u32 phase) {
  NOUNROLL for (u32 s = 0; s < SPIN_CAP; ++s) {
    u32x4 v; asm volatile("ds_read_b128 %0, %1\n\ts_waitcnt lgkmcnt(0)" : "=v"(v) : "v"(words) : "memory");
    if (__builtin_amdgcn_readfirstlane(min(min(v.x, v.y), min(v.z, v.w))) >= phase) break;
  }
  FENCE();
}
DI void gpost(u32 words, int wq, u32 phase) { FENCE(); lw<u32>(words + 4 * wq, phase); FENCE(); }
DI void gbar(u32 words, int wq, u32 phase) { gpost(words, wq, phase); gwait(words, phase); }
DI bool tagged(u32x4 a, u32x4 b, u64 tag) { const u32 lo = (u32)tag, hi = (u32)(tag >> 32); return __all((((a.x ^ lo) | (a.y ^ hi) | (a.z ^ lo)) | ((a.w ^ hi) | (b.x ^ lo) | (b.y ^ hi)) | ((b.z ^ lo) | (b.w ^ hi))) == 0); }
DI void pollflags(rsrc_t wr, u32 off, int lane, u64 tag) {
  NOUNROLL for (u32 s = 0; s < SPIN_CAP; ++s) {
    u32x4 a = bl4c<0>(wr, lane * 16, off), b = bl4c<1024>(wr, lane * 16, off);
    WAITV(0); PIN(a); PIN(b);
    if (tagged(a, b, tag)) break;
    __builtin_amdgcn_s_sleep(2);
  }
}
DI void pollq(rsrc_t wr, u32 off, int lane, u64 tag) {
  NOUNROLL for (u32 s = 0; s < SPIN_CAP; ++s) {
    u64 a; asm volatile("buffer_load_dwordx2 %0, %1, %2, %3 offen sc1\n\ts_waitcnt vmcnt(0)" : "=&v"(a) : "v"(lane * 8), "s"(wr), "s"(off) : "memory");
    if (__all(a == tag)) break;
    __builtin_amdgcn_s_sleep(POLLSLP);
  }
}
#define TSW(n, t) do {} while (0)
#define CY0()
#define CY1(n)
#define ITS(p, j) do {} while (0)
#define TS(n) TSW(n, 0)
#define TX(n) TSW(n, 256)
#define ROT1 ((ti + pj + 8 * (xcd & 1) + 4 * (xcd >> 1)) & 15)
#define ROT2 tt
#define PRI 0
#define JF 6
struct c2 { f32x2 r, i; };
DI f32x2 fma2(f32x2 a, f32x2 b, f32x2 c) { return __builtin_elementwise_fma(a, b, c); }
DI c2 cfma(c2 a, c2 b, c2 c) { return {fma2(a.r, b.r, fma2(-a.i, b.i, c.r)), fma2(a.r, b.i, fma2(a.i, b.r, c.i))}; }
DI c2 cmul(c2 a, c2 b) { return {fma2(-a.i, b.i, a.r * b.r), fma2(a.r, b.i, a.i * b.r)}; }
DI c2 csq(c2 a) { return {fma2(a.r, a.r, -a.i * a.i), (a.r + a.r) * a.i}; }
DI c2 bc(c2 a, int ps) { return ps ? c2{a.r.yy, a.i.yy} : c2{a.r.xx, a.i.xx}; }
DI c2 csel(bool c, c2 a, c2 b) { return {c ? a.r : b.r, c ? a.i : b.i}; }
DI c2 asc(f32x4 v) { return {v.xy, v.zw}; }
DI void pinc(c2& a) { PIN(a.r); PIN(a.i); }
DI f32x2 spread16(float v) { const u32 u = __float_as_uint(v); const auto e = __builtin_amdgcn_permlane16_swap(u, u, false, false); return f32x2{__uint_as_float(e[0]), __uint_as_float(e[1])}; }
DI float sum16(float v) { const f32x2 e = spread16(v); return e.x + e.y; }
DI float sum32(float v) { const u32 u = __float_as_uint(v); const auto e = __builtin_amdgcn_permlane32_swap(u, u, false, false); return __uint_as_float(e[0]) + __uint_as_float(e[1]); }
template <int CTRL> DI float dppadd(float v) { return v + __int_as_float(__builtin_amdgcn_mov_dpp(__float_as_int(v), CTRL, 0xf, 0xf, true)); }

template <bool INV> DI void hart_table(u32x4 (&tab)[2][4], int kap, bool sp, int g) {
#pragma unroll
  for (int sg = 0; sg < 2; ++sg)
#pragma unroll
    for (int r = 0; r < 4; ++r) {
      const float kk = INV ? (float)(2 * kap + 1) * (sg ? -1.f / 512.f : 1.f / 512.f) : (float)(sg == 0 ? kap : (sp ? (r == 1 ? 32 : -32) : -kap)) * (1.f / 256.f), a0 = fmaf(kk, (float)(32 * g + r), -0.125f);
#pragma unroll
      for (int j = 0; j < 4; ++j) tab[sg][r][j] = pk2(cosr(fmaf(kk, (float)(8 * j), a0)), cosr(fmaf(kk, (float)(8 * j + 4), a0)));
    }
}
template <bool INV> DI void hart256(f32x2 (&o)[8][2], u32 ap, const u32x4 (&tab)[2][4], int kap, bool sp) {
  const float sg1 = (kap & 1) ? -1.f : 1.f, al1 = sp ? 0.f : 1.f;
  const f32x2 sgn = {sg1, sg1}, al = {al1, al1};
  bf16x8 a[4][2]; f32x4 m[2][4][2];
#pragma unroll
  for (int r = 0; r < 4; ++r) { a[r][0] = cat(trr(ap + r * 8192), trr(ap + r * 8192 + 512)); a[r][1] = cat(trr(ap + r * 8192 + 4096), trr(ap + r * 8192 + 4608)); }
  SB();
#pragma unroll
  for (int r = 0; r < 4; ++r)
#pragma unroll
    for (int i = 0; i < 4; ++i) m[i >> 1][r][i & 1] = mma(a[r][i & 1], asf(tab[(i >> 1) ^ (INV & i & 1)][r]), f32x4{});
  SB();
#pragma unroll
  for (int hh = 0; hh < 2; ++hh) {
    f32x2 p[2][4];
#pragma unroll
    for (int i = 0; i < 8; ++i) { const f32x2 sg_ = INV && i >= 4 ? -sgn : sgn; p[i >> 2][i & 3] = hh ? fma2(sg_, m[i >> 2][i & 3][1].zw, m[i >> 2][i & 3][0].zw) : fma2(sg_, m[i >> 2][i & 3][1].xy, m[i >> 2][i & 3][0].xy); }
    const f32x2 s0 = p[0][1] + p[0][3], d0 = p[0][1] - p[0][3], s1 = INV ? p[1][1] + p[1][3] : fma2(al, p[1][1], p[1][3]), d1 = INV ? p[1][1] - p[1][3] : fma2(-al, p[1][3], p[1][1]);
    const f32x2 x0 = sp ? d0 : d1, x1 = sp ? d1 : d0;
#pragma unroll
    for (int sg = 0; sg < 2; ++sg) {
      const f32x2 A = p[sg][0] + p[sg][2], B = p[sg][0] - p[sg][2], S = sg ? s1 : s0, X = sg ? x1 : x0;
      o[4 * sg][hh] = A + S; o[4 * sg + 2][hh] = A - S; o[4 * sg + 1][hh] = B + X; o[4 * sg + 3][hh] = B - X;
    }
  }
  SB();
#pragma unroll
  for (int i = 0; i < 8; ++i) {
    const auto e0 = __builtin_amdgcn_permlane32_swap(__float_as_uint(o[i][0].x), __float_as_uint(o[i][1].x), false, false);
    const auto e1 = __builtin_amdgcn_permlane32_swap(__float_as_uint(o[i][0].y), __float_as_uint(o[i][1].y), false, false);
    o[i][0] = f32x2{__uint_as_float(e0[0]), __uint_as_float(e1[0])}; o[i][1] = f32x2{__uint_as_float(e0[1]), __uint_as_float(e1[1])};
  }
  SB();
#pragma unroll
  for (int i = 0; i < 8; ++i) { const f32x2 a_ = o[i][0], b_ = o[i][1]; o[i][0] = a_ + b_; o[i][1] = a_ - b_; }
}

struct TileD { int k, c, src, w; };
DI TileD tile_desc(int tau, int k1, int kd1) {
  TileD t; const int i = tau >> 1, ka = tau & 1, hi = i >= 5;
  t.c = kd1 + 2 * ka;
  t.src = k1 ? -1 : (kd1 ? ka : hi);
  t.k = 256 * i + (k1 ? k1 : (kd1 ? 128 * ka : -1152 * hi));
  t.w = k1 ? 1 : (kd1 ? 2 : (tau >= 18 ? 0 : ((i & 3) == 0 && i < 8 ? 1 : 2)));
  return t;
}
DI void m0_fwd(bf16x8& o, int n, int l15, int g, int k1, int kd1, bool inv = false) {
  const TileD t = tile_desc(8 * n + (l15 >> 1), k1, kd1); f32x4 v[2];
  const float ak = (float)t.k * (-1.f / 2048.f), a0 = fmaf(ak, (float)(2 * g), -0.25f * (float)(l15 & 1)) + (inv ? (float)k1 * (1.f / 512.f) : 0.f), ac = -0.25f * (float)t.c;
#pragma unroll
  for (int j = 0; j < 8; ++j) {
    const int rs = j & 1;
    const float c = cosr(a0 + ((j & 2) ? ak : 0.f) + ((j & 4) ? ac : 0.f) + (t.src < 0 ? (rs ? (inv ? 0.625f : 0.125f) : -0.125f) : 0.f));
    v[j >> 2][j & 3] = (t.src >= 0 && (t.w == 0 || t.src != rs)) ? 0.f : c;
  }
  o = pk8(v[0], v[1]);
}
DI void tr2(bf16x8& X, bf16x8& Y, bf16x8 A, bf16x8 B, u32 wr, u32 rd) {
  const u32x4 a = __builtin_bit_cast(u32x4, A), b = __builtin_bit_cast(u32x4, B);
  FENCE(); lw<u32x2>(wr, a.xy); lw<u32x2>(wr + 512, b.xy); lw<u32x2>(wr + 1024, a.zw); lw<u32x2>(wr + 1536, b.zw); FENCE();
  X = cat(trr(rd), trr(rd + 512)); Y = cat(trr(rd + 1024), trr(rd + 1536)); FENCE();
}

template <int O> DI void ldhi(float& v, u32 a) { asm volatile("ds_read_u16_d16_hi %0, %1 offset:%2" : "+v"(v) : "v"(a), "n"(O) : "memory"); }
constexpr int g1_post(int j) { return j == 1 || j == 2 ? 2 : j == 12 ? 4 : j == 13 ? 8 : 0; }
constexpr int g1_wait(int j) { int c = j >= 3 ? g1_post(j - 3) : 0; for (int u = j - 2; u < j; ++u) c += (u + 3 < 16 ? 8 : 0) + (u >= 0 ? g1_post(u) : 0); return j == 14 ? 8 : c; }
constexpr int g2_post(int u) { return (u >= 4 && u <= 12 ? 4 : 0) + (u <= 2 ? 4 : 0); }
constexpr int g2_wait(int u) { return (u >= 2 ? g2_post(u - 2) : 0) + (u + 1 < 16 ? 8 : 0) + (u >= 1 ? g2_post(u - 1) : 0); }
template <int N> struct IC { static constexpr int v = N; };
template <int I, int N, class F> DI void sfor(F&& f) { if constexpr (I < N) { f(IC<I>{}); sfor<I + 1, N>(f); } }

typedef __attribute__((ext_vector_type(4))) u64 u64x4;
typedef __attribute__((ext_vector_type(2))) u64 u64x2;
__global__ __launch_bounds__(512, 2) void k_all(const u16*, const u16*, const u16*, const u16*, const u16*, const u16*, const u16* __restrict__ wre, const u16* __restrict__ wim,
    const u16* __restrict__ c_re, const u16* __restrict__ c_im, const u16* __restrict__ dskip, const u16* __restrict__ gate_w, char* __restrict__ ws, float* __restrict__ out) {
  u64x4 ka; u64x2 kb_;
  asm volatile("s_load_dwordx8 %0, %2, 0x0\n\ts_load_dwordx4 %1, %2, 0x20" : "=&s"(ka), "=&s"(kb_) : "s"(__builtin_amdgcn_kernarg_segment_ptr()) : "memory");
  extern __shared__ __attribute__((aligned(16))) char lds_[];
  const u32 L = (u32)(uintptr_t)(LDSP char*)lds_;
  const int lane = threadIdx.x & 63, wv = __builtin_amdgcn_readfirstlane(threadIdx.x >> 6), w = wv & 3, rh = wv >> 2, l15 = lane & 15, g = lane >> 4;
  const int bid = blockIdx.x, xcd = bid & 7, jj = bid >> 3, ti = jj & 3, pj = jj >> 2;
  const int tt = (xcd >> 1) * 4 + ti, pt = (xcd & 1) * 8 + pj;
  const int s2 = tt >> 1, sh = tt & 1, ct = pt;
  const int lr8 = lane >> 3, lc8 = lane & 7, q = l15 >> 2, pq = lane & 3;
  int rot1 = ROT1, rot2 = ROT2;
  {
    u64 pc; u32 nb; asm volatile("s_getpc_b64 %0\n.Lcode_lo:\n\ts_mov_b32 %1, .Lcode_hi-.Lcode_lo" : "=s"(pc), "=s"(nb));
    if (wv == 7) { const rsrc_t pr = mkr((const void*)(pc & ~127ull), nb); asm volatile("s_mov_b32 m0, %2\n\ts_nop 0\n\tbuffer_load_dword %0, %1, 0 offen lds" :: "v"((lane * 32 + jj) * 128), "s"(pr), "s"(L + L_SYNC + 256) : "memory"); }
  }
  const int ii = 8 * (w & 1) + lr8, swz = 16 * (lc8 ^ ((ii >> 1) & 7)), ac = lc8 ^ (((lr8 >> 1) & 1) | ((w & 1) << 1)), r0 = 2 * sh + (ct & 1);
  u32 vB = (u32)(64 * pt + ii) * 2048u + swz, vA = (u32)(s2 + 32 * (8 * w + lr8)) * 2048u + 64 * ct + 16 * (ac & 3) + 1024 * (ac >> 2);
  u32 vP = (64 * pt + 32 * (w & 1) + (lane & 31)) * 2;
  PIN(vA); PIN(vB); PIN(vP);
  asm volatile("s_waitcnt lgkmcnt(0)" : "+s"(ka), "+s"(kb_) :: "memory");
  const u16 *x = (const u16*)ka[0], *b_re = (const u16*)ka[1], *b_im = (const u16*)ka[2], *log_dt = (const u16*)ka[3], *lam_re = (const u16*)kb_[0], *lam_im = (const u16*)kb_[1];
  const rsrc_t xr = mkr(x, 4u << 20), br = mkr((w & 2) ? b_im : b_re, 2u << 20);
  auto issue_a = [&](int rr, int hf) { const int r = (rr + r0) & 3; blds(xr, vA, (8 * r + 1024 * hf) * 2048, L + r * 8192 + hf * 4096 + w * 1024, L_RA); };
  auto issue_b1 = [&](int j, int i) { blds(br, vB, 16 * i * 2048 + ((j + rot1) & 15) * 128, L + w * 1024, ((j + 3) & 3) * 32768 + 16384 + i * 4096); };
  if (!rh) { blh(mkr(log_dt, 2048), vP, L + w * 1024, L_V + 16384); blh(mkr(lam_re, 2048), vP, L + w * 1024, L_V + 16640); blh(mkr(lam_im, 2048), vP, L + w * 1024, L_V + 16896); }
  issue_a(0, rh); blds(br, vB, rh * 65536 + rot1 * 128, L + w * 1024 + rh * 8192, 114688); blds(br, vB, rh * 65536 + 32768 + rot1 * 128, L + w * 1024 + rh * 8192, 118784);
  SB();
  const rsrc_t wsr = mkr(ws, WS_END);
  const u32 vX = (u32)(128 * tt + 16 * (ii >> 2) + 8 * (ii & 1) + 2 * (w >> 1) + ((ii >> 1) & 1)) * 2048u + swz;
  auto issue_x1 = [&](int j, int i) { blds(xr, vX, (64 * (i >> 1) + 4 * (i & 1)) * 2048 + ((j + rot1) & 15) * 128, L + w * 1024, ((j + 3) & 3) * 32768 + i * 4096); };
  const rsrc_t cr = mkr((w & 2) ? c_im : c_re, 2u << 20);
  const u32 vGo = WS_G + tt * 512 + (u32)((w & 1) * 64 + lr8 * 4 + 2 * rh + (w >> 1)) * 4;
  u32 gq[2];

  if (rh) {
    const u32 vl = lane * 16;
    u32 vC[2]; vC[0] = (u32)(32 * ct + 512 * (w & 1) + lr8) * 2048u + 16 * (lc8 ^ (lr8 >> 1)); vC[1] = vC[0] ^ 64;
    auto c_piece = [&](int i, int e) { blds(cr, vC[e & 1], e * 8 * 2048 + ((i + rot2) & 15) * 128, L + w * 4096, (i & 3) * 16384 + e * 1024); };
    u64 tag; asm volatile("buffer_load_dwordx2 %0, off, %1, 0 sc1" : "=v"(tag) : "s"(wsr) : "memory");
    if (w == 2) {
      const u32 o = lane < (2 * NREP + 15) / 16 ? WS_FU + ((bid + 256 * lane) / (16 * NREP)) * 0x20000 + ((bid + 256 * lane) % (16 * NREP)) * 128 : (lane == 63 && bid < 64 ? WS_G + bid * 128 : WS_END);
      asm volatile("s_mov_b32 m0, %2\n\ts_nop 0\n\tbuffer_load_dword %0, %1, 0 offen lds" :: "v"(o), "s"(wsr), "s"(L + L_SYNC + 256) : "memory");
    }
    const int kap = 16 * sh + l15, ecol = 8 * w + 4 * (g & 1) + 2 * (g >> 1);
    u32 q_ds[2] = {gl1(dskip + 32 * ct + ecol), gl1(dskip + 512 + 32 * ct + ecol)};
    const rsrc_t w0 = mkr(wre, 2048 * 1026), w1 = mkr(wim, 2048 * 1026);
    u32x4 gw[2], gx[4];
    { const rsrc_t gr = mkr(gate_w, 2048); gw[0] = bl4<0>(gr, vl, 0); gw[1] = bl4<1024>(gr, vl, 0); }
    const u32 rep = (bid & (NREP - 1)) * 2048;
    TX(16);
    const int k1 = xcd * 16 + (jj >> 1), kd1 = jj & 1, cc = kd1 + 2 * (w & 1);
    const bool sp0 = (k1 == 0 && kd1 == 0);
    bf16x8 m0[2], mi[2], m0x, mix[2], fa_r, fa_i, fb_r, fb_i, gb_r, gb_i, ga_r, ga_i; f32x4 t1c, t1s, t2c, t2s;
    u32 vW[5], wa[5], wo[5]; float fr[4][4], fi[4][4], er[4], ei[4];
    const float fl = (float)l15, fg = (float)(4 * g), fc = (float)cc;
    const u32 tw = L + L_SEG + w * 2048 + l15 * 32 + 8 * g, tr = L + L_SEG + w * 2048 + (4 * g + q) * 32 + 8 * pq;
    SB();
    {
      const float st = fmaf(fl, -4.f / 64.f, fc * (-1.f / 64.f));
#pragma unroll
      for (int e = 0; e < 4; ++e) { const float a = (fg + (float)e) * st; t2c[e] = cosr(a); t2s[e] = sinr(a); t1c[e] = cosr(a * (1.f / 16.f)); t1s[e] = sinr(a * (1.f / 16.f)); }
      fa_r = pk8(t2c, -t2s); fa_i = pk8(t2s, t2c); PIN(t1c); PIN(t1s);
      tr2(ga_r, ga_i, fa_r, fa_i, tw, tr);
      const float fm = (float)(l15 < 8 ? l15 : l15 ^ 3) * (-1.f / 16.f); const u32 sl = l15 < 8 ? 0u : 0x80008000u;
#pragma unroll
      for (int e = 0; e < 4; ++e) { const float a = (fg + (float)e) * fm; t2c[e] = cosr(a); t2s[e] = sinr(a); }
      fb_r = pk8(t2c, -t2s); fb_i = asf(__builtin_bit_cast(u32x4, pk8(t2s, t2c)) ^ sl);
      tr2(gb_r, gb_i, fb_r, fb_i, tw, tr);
    }
    u32 xa[2], ua, us, us0, fo, ap0, wp0, tr_, tw_;
    const u32 vo = L + L_V + w * L_CH;
    {
      if (__builtin_expect(k1 != 0, 1)) {
        const float mc = -0.25f * (float)(kd1 + (l15 & 2)), t = fmaf((float)(l15 & 1), -0.25f, -0.125f), sft = (float)k1 * (1.f / 512.f);
#pragma unroll
        for (int n = 0; n < 2; ++n) {
          const float mk = (float)(k1 + 256 * (l15 >> 2)) * (-1.f / 2048.f) - 0.5f * (float)n, ma = fmaf(mk, 0.5f * fg, t);
          u32x4 f, b;
#pragma unroll
          for (int e = 0; e < 4; ++e) { const float a = ma + ((e & 1) ? mk : 0.f) + ((e & 2) ? mc : 0.f); f[e] = pk2(cosr(a), -sinr(a)); b[e] = pk2(cosr(a + sft), sinr(a + sft)); }
          m0[n] = asf(f); mi[n] = asf(b);
        }
      } else { m0_fwd(m0[0], 0, l15, g, 0, kd1); m0_fwd(m0[1], 1, l15, g, 0, kd1); m0_fwd(m0x, 2, l15, g, 0, 0); tr2(mix[0], mix[1], m0x, bf16x8{}, tw, tr); mi[0] = m0[0]; mi[1] = m0[1]; }
      tr2(mi[0], mi[1], mi[0], mi[1], tw, tr);
#pragma unroll
      for (int e = 0; e < 4; ++e) { const float a = fl * fmaf(fg + (float)e, 4.f / 1024.f, fc * (1.f / 1024.f)); t2c[e] = cosr(a) * (1.f / 1048576.f); t2s[e] = sinr(a) * (1.f / 1048576.f); }
      PIN(t2c); PIN(t2s);
#pragma unroll
      for (int i = 0; i < 2; ++i) { const int n = i ? 63 - kap : kap, rho = 64 * (n & 3) + (n >> 2); xa[i] = L + L_RA + rho * 128 + 16 * (w ^ (((rho >> 1) & 1) | ((rho >> 3) & 1) << 1)) + 2 * (ecol & 7); PIN(xa[i]); }
      ua = L + L_V + (16 * w + g) * 256 + (lr8 & 1) * 128 + 16 * (lc8 ^ g);
      us = (u32)(64 * w + 2 * g + (lr8 & 1) + 32 * (sh ^ (w & 1))) * 16384u + lc8 * 16; us0 = (w & 1) && g == 0 ? us ^ (32 * 16384) : us;
      fo = lane < NREP ? lane * 2048 + bid * 8 : WS_END;
      PIN(ua); PIN(us); PIN(us0); PIN(fo);
#pragma unroll
      for (int i = 0; i < 5; ++i) {
        const int id = 64 * (w + 4 * i) + lane, sl = min((id * 993) >> 16, 15), r0_ = (k1 ? 256 : 128) * sl, row = r0_ < 2048 ? k1 + r0_ : 4096 - k1 - r0_;
        vW[i] = ((row * 1026) & ~15) + 16 * id - 1056 * sl; PIN(vW[i]);
      }
    }
    TX(17);
    WAITV(0); LBAR();
    u32 sk[8][2];
#pragma unroll
    for (int i = 0; i < 8; ++i) { const u32 a = xa[i >> 2] + 2048 * (i < 4 ? i : (i + 3) & 3); sk[i][0] = lr<u32>(a); sk[i][1] = lr<u32>(a + 64); }
    auto issue_t = [&](int j) {
#pragma unroll
      for (int i = 0; i < 4; ++i) issue_b1(j, i);
#pragma unroll
      for (int i = 0; i < 4; ++i) issue_x1(j, i);
    };
    issue_t(1); issue_t(2);
    PIN(tag); PIN(gw[0]); PIN(gw[1]); PIN(q_ds[0]); PIN(q_ds[1]);
    PIN(fa_r); PIN(fa_i); PIN(fb_r); PIN(fb_i); PIN(ga_r); PIN(ga_i); PIN(gb_r); PIN(gb_i); PIN(m0[0]); PIN(m0[1]); PIN(mi[0]); PIN(mi[1]);
    tag += 0x9e3779b97f4a7c15ull;
    {
      const int t = 2 * cc + 8 * l15 + 512 * g;
      const u32 wp = L + L_W + (w >> 1) * 1056 + ((k1 * 1026) & 15), wq = L + L_W + (8 + (w >> 1)) * 1056 + ((-k1 * 1026) & 15);
#pragma unroll
      for (int i = 0; i < 4; ++i) { wa[i] = (g < 2 ? wp + t : wq + 1664 - t) + 2112 * i; wo[i] = (g < 2 ? wq : wp + 1024) + 2112 * i; }
      if (__builtin_expect(k1 == 0, 0)) {
#pragma unroll
        for (int i = 0; i < 5; ++i) {
          const int kk = tile_desc(w + 4 * i, 0, kd1).k;
          const u32 wp_ = L + L_W + (kk >> 7) * 1056, wq_ = L + L_W + (((2048 - kk) & 2047) >> 7) * 1056;
          wa[i] = g < 2 ? wp_ + t : wq_ + 1664 - t; wo[i] = g < 2 ? wq_ : wp_ + 1024;
        }
      }
#pragma unroll
      for (int i = 0; i < 4; ++i) { PIN(wa[i]); PIN(wo[i]); }
      ap0 = vo + (4 * g + q) * 64 + 16 * pq; wp0 = vo + l15 * L_PS + 16 * g; tr_ = L + L_V + w * 2 * L_PS + g * L_CH + q * 32 + 8 * pq; tw_ = L + L_V + w * 2 * L_PS + q * L_CH + pq * 32 + 8 * g;
      PIN(ap0); PIN(wp0); PIN(tr_); PIN(tw_); PIN(vC[0]); PIN(vC[1]);
    }
    TX(18);
    u32x4 uq[4];
    const f32x4 z4 = {};
    bf16x8 a0[2][2];
    sfor<0, 15>([&](auto j_) {
      constexpr int j = decltype(j_)::v;
      WAITV(g1_wait(j));
      if (j == 0) LBAR(); else BAR();
      ITS(1, j);
      PINS(rot1);
      if (j == 0) {
#pragma unroll
        for (int i = 0; i < 4; ++i) uq[i] = lr<u32x4>((ua ^ (64 * (i & 1))) + 1024 * i);
        if (w == 3) st4c(u32x4{SENT, SENT, SENT, SENT}, wsr, lane < 32 ? vl : WS_END, WS_AGG + tt * 8192 + pt * 512);
      }
      if (j == JF && w == 0) st2c(tag, wsr, fo, WS_FU);
      if (j == 9 && w == 1) { bldsc(wsr, vl, WS_FU + rep, L, L_SEG + 4096); bldsc(wsr, vl, WS_FU + rep + 1024, L, L_SEG + 5120); }
      SB();
      if (j < 13) issue_t(j + 3);
      if constexpr (j >= 3 && j < 8) {
#pragma unroll
        for (int r = 0; r < 4; ++r) { float& y = j < 7 ? fr[j - 3][r] : er[r]; float& z = j < 7 ? fi[j - 3][r] : ei[r]; y = z = 0.f; PIN(y); PIN(z); }
      }
      SB();
      if constexpr (j == 1 || j == 2) { st4c(uq[2 * j - 2], wsr, j == 1 ? us0 : us, WS_U + ct * 1024 + s2 * 128 + (2 * j - 2) * 8 * 16384); st4c(uq[2 * j - 1], wsr, us, WS_U + ct * 1024 + s2 * 128 + (2 * j - 1) * 8 * 16384); }
      if (j == 11 && w == 1) {
        WAITV(16);
        if (__builtin_expect(!tagged(lr<u32x4>(L + L_SEG + 4096 + vl), lr<u32x4>(L + L_SEG + 5120 + vl), tag), 0)) pollflags(wsr, WS_FU + rep, lane, tag);
        if (bid == 0) st2c(tag, wsr, lane == 0 ? WS_E : WS_END, 0);
      }
      if (j == 12) {
#pragma unroll
        for (int i = 0; i < 4; ++i) bldsc(wsr, vl, WS_U + (u32)(2 * k1 + kd1) * 16384u + w * 2048 + (i >> 1) * 8192 + (i & 1) * 1024, L + w * L_CH, L_V + i * 1024);
      }
      if (j == 13) {
#pragma unroll
        for (int i = 0; i < 4; ++i) { blds(w0, vW[i], 0, L + w * 1024, L_W + i * 4096); blds(w1, vW[i], 0, L + w * 1024, L_W + L_WS + i * 4096); }
      }
      SB();
    });
    if (w == 0) { blds(w0, vW[4], 0, L, L_W + 16384); blds(w1, vW[4], 0, L, L_W + L_WS + 16384); }
    {
#pragma unroll
      for (int cb = 0; cb < 2; ++cb) { a0[cb][0] = cat(trr(ap0 + 1024 * cb), trr(ap0 + 1024 * cb + 2048)); a0[cb][1] = cat(trr(ap0 + 1024 * cb + 8), trr(ap0 + 1024 * cb + 2056)); }
      FENCE();
#pragma unroll
      for (int e = 0; e < 4; ++e) c_piece(0, e);
      f32x4 d[2][2][2];
#pragma unroll
      for (int i = 0; i < 8; ++i) d[i >> 2][(i >> 1) & 1][i & 1] = mma(a0[(i >> 1) & 1][i & 1], m0[i >> 2], z4);
#pragma unroll
      for (int i = 0; i < 4; ++i) lw<bf16x8>(wp0 + 16 * (i >> 1) * L_PS + 64 * (i & 1), pk8(d[i >> 1][i & 1][0], d[i >> 1][i & 1][1]));
      if (__builtin_expect(sp0, 0)) {
#pragma unroll
        for (int cb = 0; cb < 2; ++cb) { const bf16x8 o = pk8(mma(a0[cb][0], m0x, z4), mma(a0[cb][1], m0x, z4)); if (l15 < 4) lw<bf16x8>(wp0 + 32 * L_PS + 64 * cb, o); }
      }
      WAITV(4);
      gbar(L + L_SYNC + 16, w, 1);
    }
    TX(8);
    auto tiles = [&](auto nt_, auto i0_) {
      constexpr int NT = decltype(nt_)::v, i0 = decltype(i0_)::v;
      const u32 pr = tr_ + i0 * 8 * L_PS, pw_ = tw_ + i0 * 8 * L_PS;
      bf16x8 a[NT]; f32x4 yr[NT], yi[NT];
#pragma unroll
      for (int i = 0; i < NT; ++i) a[i] = cat(trr(pr + i * 8 * L_PS), trr(pr + i * 8 * L_PS + L_PS));
      SB();
#pragma unroll
      for (int i = 0; i < NT; ++i) { yr[i] = mma(a[i], fa_r, z4); yi[i] = mma(a[i], fa_i, z4); }
      SB();
      TX(9);
#pragma unroll
      for (int i = 0; i < NT; ++i) {
        if (i0) {
#pragma unroll
          for (int r = 0; r < 4; ++r) fr[i][r] = fi[i][r] = er[i] = ei[i] = 0.f;
        }
        ldhi<0>(fr[i][0], wa[i0 + i]); ldhi<128>(fr[i][1], wa[i0 + i]); ldhi<256>(fr[i][2], wa[i0 + i]); ldhi<384>(fr[i][3], wa[i0 + i]);
        ldhi<L_WS>(fi[i][0], wa[i0 + i]); ldhi<L_WS + 128>(fi[i][1], wa[i0 + i]); ldhi<L_WS + 256>(fi[i][2], wa[i0 + i]); ldhi<L_WS + 384>(fi[i][3], wa[i0 + i]);
        ldhi<0>(er[i], wo[i0 + i]); ldhi<L_WS>(ei[i], wo[i0 + i]);
        SB();
        a[i] = pk8(yr[i] * t1c - yi[i] * t1s, yr[i] * t1s + yi[i] * t1c); yr[i] = mma(fb_r, a[i], z4); yi[i] = mma(fb_i, a[i], z4);
        SB(); if (!i0) c_piece(1, i); SB();
      }
      if (!i0) gwait(L + L_SYNC, 1); else WAITL(0);
#pragma unroll
      for (int i = 0; i < NT; ++i)
#pragma unroll
        for (int r = 0; r < 4; ++r) { PIN(fr[i][r]); PIN(fi[i][r]); PIN(er[i]); PIN(ei[i]); }
      if (cc == 0) {
        const float b0 = lane == 0 ? 0.5f : 0.f, b3 = lane == 32 ? 0.5f : 0.f;
#pragma unroll
        for (int i = 0; i < NT; ++i) {
          float t;
          asm volatile("v_sub_f32 %4, %5, %0\n\tv_fmac_f32 %0, %7, %4\n\tv_add_f32 %4, %6, %1\n\tv_fma_f32 %1, -%7, %4, %1\n\t"
                       "v_sub_f32 %4, %5, %2\n\tv_fmac_f32 %2, %8, %4\n\tv_add_f32 %4, %6, %3\n\tv_fma_f32 %3, -%8, %4, %3"
                       : "+v"(fr[i][0]), "+v"(fi[i][0]), "+v"(fr[i][3]), "+v"(fi[i][3]), "=&v"(t) : "v"(er[i]), "v"(ei[i]), "v"(b0), "v"(b3));
        }
      }
      if (__builtin_expect(k1 == 0, 0)) {
#pragma unroll
        for (int i = 0; i < NT; ++i) { const float wt_ = (float)tile_desc(w + 4 * (i0 + i), 0, kd1).w; yr[i] *= wt_; yi[i] *= wt_; }
      }
      SB();
#pragma unroll
      for (int i = 0; i < NT; ++i) {
        const f32x4 vr = {fr[i][0], fr[i][1], fr[i][2], fr[i][3]}, vi = {fi[i][0], fi[i][1], fi[i][2], fi[i][3]};
        a[i] = pk8(yr[i] * vr - yi[i] * vi, yr[i] * vi + yi[i] * vr); yr[i] = mma(a[i], gb_r, z4); yi[i] = mma(a[i], gb_i, z4);
        SB(); if (!i0) c_piece(2, i); SB();
      }
#pragma unroll
      for (int i = 0; i < NT; ++i) {
        a[i] = pk8(yr[i] * t2c - yi[i] * t2s, yr[i] * t2s + yi[i] * t2c); yr[i] = mma(a[i], ga_r, z4); yi[i] = mma(a[i], ga_i, z4);
        SB(); if (!i0) c_piece(3, i); SB();
      }
#pragma unroll
      for (int i = 0; i < NT; ++i) { lw<u32x2>(pw_ + i * 8 * L_PS, u32x2{pk2(yr[i].xy), pk2(yr[i].zw)}); lw<u32x2>(pw_ + i * 8 * L_PS + L_PS, u32x2{pk2(yi[i].xy), pk2(yi[i].zw)}); }
    };
    tiles(IC<4>{}, IC<0>{});
    if (__builtin_expect(sp0 && w < 2, 0)) tiles(IC<1>{}, IC<4>{});
    TX(23);
    gpost(L + L_SYNC + 16, w, 2);
    { const u32 xo = (bid * 8 + 2 * w) * 2048; gx[0] = bl4<0>(xr, vl, xo); gx[1] = bl4<1024>(xr, vl, xo); gx[2] = bl4<2048>(xr, vl, xo); gx[3] = bl4<3072>(xr, vl, xo); }
    gwait(L + L_SYNC + 16, 2);
    TX(24);
    {
      f32x4 o[2][2][2];
      const u32 ap = vo + (4 * g + q) * L_PS + 16 * pq;
#pragma unroll
      for (int cb = 0; cb < 2; ++cb) { a0[cb][0] = cat(trr(ap + 64 * cb), trr(ap + 64 * cb + 16 * L_PS)); a0[cb][1] = cat(trr(ap + 64 * cb + 8), trr(ap + 64 * cb + 16 * L_PS + 8)); }
#pragma unroll
      for (int i = 0; i < 8; ++i) o[i >> 2][(i >> 1) & 1][i & 1] = mma(a0[(i >> 1) & 1][i & 1], mi[i >> 2], z4);
      if (__builtin_expect(sp0, 0)) {
        const u32 ax = vo + (32 + q) * L_PS + 16 * pq;
#pragma unroll
        for (int i = 0; i < 4; ++i) { const u32x2 t = trr(ax + 64 * (i >> 1) + 8 * (i & 1)); const bf16x8 a = cat(t, t); o[0][i >> 1][i & 1] = mma(a, mix[0], o[0][i >> 1][i & 1]); o[1][i >> 1][i & 1] = mma(a, mix[1], o[1][i >> 1][i & 1]); }
      }
      FENCE();
#pragma unroll
      for (int i = 0; i < 4; ++i) lw<bf16x8>(vo + i * 1280 + l15 * 80 + 16 * g, pk8(o[i >> 1][i & 1][0], o[i >> 1][i & 1][1]));
      FENCE();
      const u32 to = (u32)lr8 * (16 * 32768) + (u32)(2 * k1 + kd1) * 128 + lc8 * 16;
      u32x4 tq[4];
#pragma unroll
      for (int i = 0; i < 4; ++i) tq[i] = lr<u32x4>(vo + i * 1280 + (lane >> 2) * 80 + pq * 16);
#pragma unroll
      for (int i = 0; i < 4; ++i) st4c(tq[i], wsr, to, WS_T + (8 * (i >> 1) + 2 * w + (i & 1)) * 32768);
    }
    TX(10);
    {
      const u32 vG = WS_G + (u32)(((2 * w + (lane & 1)) * 2 + (((bid >> 4) ^ (bid >> 5)) & 1)) * 128 + ((bid >> 5) & 1) * 64 + (bid & 15) * 4 + (bid >> 6)) * 4;
      float sm[2] = {0.f, 0.f};
      WAITV(4);
#pragma unroll
      for (int i = 0; i < 4; ++i) {
        PIN(gx[i]);
#pragma unroll
        for (int e = 0; e < 4; ++e) { const u32 a = gx[i][e], b = gw[i & 1][e]; sm[i >> 1] = __builtin_amdgcn_fdot2_f32_bf16(__builtin_bit_cast(bf16x2, a), __builtin_bit_cast(bf16x2, b), sm[i >> 1], false); }
      }
      sm[0] = dppadd<0xB1>(sm[0]); sm[1] = dppadd<0xB1>(sm[1]);
      float t = (lane & 1) ? sm[1] : sm[0];
      t = dppadd<0x4E>(t); t = dppadd<0x124>(t); t = dppadd<0x128>(t); t = sum32(sum16(t));
      __builtin_amdgcn_raw_buffer_store_b32(__float_as_uint(rbf(1.f / (1.f + expf(-rbf(t))))), wsr, lane < 2 ? vG : WS_END, 0, 16);
    }
    u32x4 tab[2][4];
    TX(11);
    hart_table<true>(tab, kap, false, g);
#pragma unroll
    for (int i = 0; i < 8; ++i) PIN(tab[i >> 2][i & 3]);
#pragma unroll
    for (int i = 0; i < 16; ++i) { const u32 xv = sk[i >> 1][i & 1], dv = q_ds[i & 1]; sk[i >> 1][i & 1] = pk2(bf2f(xv & 0xffffu) * bf2f(dv & 0xffffu), bfhi(xv) * bfhi(dv)); PIN(sk[i >> 1][i & 1]); }
    WAITV(0); BAR();
    if (w == 0) st2c(tag, wsr, fo, WS_FH);
    TX(25);
    BAR();
#pragma unroll
    for (int e = 0; e < 12; ++e) c_piece(4 + (e >> 2), e & 3);
    u32 ta[8];
#pragma unroll
    for (int i = 0; i < 8; ++i) {
      const int kb = 16 * i + 4 * w + g, k = (lane & 4) ? (kb ? 256 - kb : 128) : kb, rho = 64 * (k & 3) + (k >> 2);
      ta[i] = L + 98304 + rho * 128 + 16 * ((((lane >> 1) & 4) | pq) ^ (((rho >> 1) & 1) | ((rho >> 3) & 1) << 1)); PIN(ta[i]);
    }
    TX(26);
    pollq(wsr, WS_FH + rep + w * 512, lane, tag);
    BAR();
    TX(27);
    const u32 hb = WS_H + (u32)(tt * 8 + 4 * (w & 1)) * 65536u + (w >> 1) * 32768;
    auto h_issue = [&](int u) {
      const u32 so = hb + ((u + rot2) & 15) * 2048;
#pragma unroll
      for (int m = 0; m < 8; ++m) bldsc(wsr, vl, so + (m >> 1) * 65536 + (m & 1) * 1024, L + w * 8192, 65536 + (u % 3) * 32768 + m * 1024);
    };
    h_issue(0); h_issue(1);
    u32x4 tv[8], gv[2];
    f32x2 o[8][2];
    sfor<0, 16>([&](auto u_) {
      constexpr int u = decltype(u_)::v;
      WAITV(g2_wait(u));
      if (u == 15) LBAR(); else BAR();
      ITS(2, u);
      PINS(rot2);
      if (u + 2 < 16) h_issue(u + 2);
      if (u >= 4 && u <= 12) {
#pragma unroll
        for (int e = 0; e < 4; ++e) c_piece(u + 3, e);
      }
      if constexpr (u == 1 || u == 2) {
#pragma unroll
        for (int i = 0; i < 4; ++i) tv[4 * u - 4 + i] = bl4c<0>(wsr, vl, WS_T + (s2 * 16 + ct) * 32768 + (4 * u - 4 + i) * 4096 + w * 1024);
      }
      if (u == 0) { gq[0] = bl1c(wsr, vGo, 0); gq[1] = bl1c(wsr, vGo + 128, 0); gv[0] = bl4c<0>(wsr, WS_G + tt * 512 + l15 * 16, 0); gv[1] = bl4c<256>(wsr, WS_G + tt * 512 + (15 - l15) * 16, 0); }
      if (u == 14) {
#pragma unroll
        for (int i = 0; i < 8; ++i) { PIN(tv[i]); lw<u32x4>(ta[i], tv[i]); }
      }
    });
    hart256<true>(o, L + 98304 + (8 * g + q) * 128 + 16 * ((4 * (pq >> 1) + w) ^ (((q >> 1) & 1) | ((g & 1) << 1))) + 8 * (pq & 1), tab, kap, false);
    PIN(gq[0]); PIN(gq[1]); PIN(gv[0]); PIN(gv[1]);
#pragma unroll
    for (int i = 0; i < 8; ++i) {
      const float gg = __uint_as_float(gv[i >> 2][i < 4 ? i : (i + 3) & 3]), og = rbf(1.f - gg);
      const int pos = i < 4 ? l15 : 15 - l15;
#pragma unroll
      for (int hf = 0; hf < 2; ++hf)
        lw<f32x2>(L + 131072 + pos * 256 + 16 * ((8 * hf + 2 * w + (g & 1)) ^ pos) + 8 * (g >> 1) + 4096 * (i < 4 ? 2 * i : 2 * ((i + 3) & 3) + 1), fma2(f32x2{og, og}, f32x2{bf2f(sk[i][hf] & 0xffffu), bfhi(sk[i][hf])}, gg * o[i][hf]));
    }
    TX(30);
  } else {
    const int wn = w & 1, wm = w >> 1;
    const int hpos = 8 * wm + lc8, hsh = (tt ^ (tt >> 1)) & 1;
    const u32 vH = (u32)(lr8 & 1) * (1u << 20) + hsh * (1u << 19) + (tt >> 1) * 65536 + (2 * pt + wn) * 1024 + g * 256 + hpos * 16;
    const u32 vAg = WS_AGG + (u32)((pt * 2 + wn) * 16 + l15) * 16;
    if (w == 0 && lane < 8) lw<u32>(L + L_SYNC + 4 * lane, 0u);
    const int kap = 16 * sh + l15, np = (16 - l15) & 15; const bool sp = kap == 0;
    const int ecol = 8 * w + 4 * (g & 1) + 2 * (g >> 1);
    TS(19);
    u32x4 tab[2][4];
    hart_table<false>(tab, kap, sp, g);
#pragma unroll
    for (int i = 0; i < 8; ++i) PIN(tab[i >> 2][i & 3]);
    u32 ua[6], hap = (8 * g + q) * 128 + 16 * ((4 * (pq >> 1) + w) ^ (((q >> 1) & 1) | ((g & 1) << 1))) + 8 * (pq & 1);
#pragma unroll
    for (int i = 0; i < 2; ++i) {
      const int lo = i ? np : l15;
      ua[i] = L + L_V + lo * 256 + ((2 * ecol) ^ ((lo & 7) << 4)); PIN(ua[i]);
    }
    ua[2] = (ua[1] ^ 64) + (sp ? 0 : 48 * 256); ua[3] = (ua[1] ^ 64) + (sp ? 32 * 256 : 16 * 256); ua[4] = (ua[0] ^ 64) + (sp ? 16 * 256 : 0);
    PIN(ua[2]); PIN(ua[3]); PIN(ua[4]); PIN(hap);
    c2 lam, coef, l8, l16;
    WAITV(3);
    {
      const u32 qa = L + L_V + 16384 + w * 1024 + lane * 4, q_dt = lr<u16>(qa), q_lr = lr<u16>(qa + 256), q_li = lr<u16>(qa + 512);
      const float lre = bf2f(q_lr), lim = bf2f(q_li);
      const float dt = rbf(expf(bf2f(q_dt))), mg = expf(dt * lre);
      float sn, cs; sincosf(dt * lim, &sn, &cs);
      const float nr = mg * cs - 1.f, ni = mg * sn;
      const bool big = fabsf(lre) >= fabsf(lim);
      const float u = big ? lre : lim, v = big ? lim : lre, t = v * __builtin_amdgcn_rcpf(u), rd = 2.f * __builtin_amdgcn_rcpf(u + v * t);
      lam = {spread16(mg * cs), spread16(ni)};
      coef = {spread16(rd * (big ? nr + ni * t : nr * t + ni)), spread16(rd * (big ? ni - nr * t : ni * t - nr))};
      l8 = csq(csq(csq(lam))); l16 = csq(l8);
      pinc(lam); pinc(coef); pinc(l8); pinc(l16);
    }
    TS(22);
#pragma unroll
    for (int i = 2; i < 8; ++i) issue_a(i >> 1, i & 1);
#pragma unroll
    for (int i = 0; i < 4; ++i) issue_x1(0, i);
    WAITV(4);
    LBAR();
    TS(1);
    {
      f32x2 o[8][2];
      hart256<false>(o, L + L_RA + hap, tab, kap, sp);
#pragma unroll
      for (int i = 0; i < 8; ++i) {
        const u32 a = i == 0 ? ua[0] : i == 1 ? ua[0] + 32 * 256 : i == 2 ? ua[2] : i == 3 ? ua[3] : i == 4 ? ua[4] : i == 5 ? ua[1] + 16 * 256 : i == 6 ? ua[1] + 48 * 256 : ua[4] + 32 * 256;
        lw<u32>(a, pk2(o[i][0])); lw<u32>(a + 128, pk2(o[i][1]));
      }
    }
    f32x4 acc[4][4];
    {
      bf16x8 fa[2][4], fb[2][4];
      u32 aa[2][2], ab[2][2];
#pragma unroll
      for (int i = 0; i < 4; ++i) {
        const int co = 16 * ((4 * (i & 1) + g) ^ (l15 >> 1)) + (i >> 1) * 65536;
        aa[i & 1][i >> 1] = L + (64 * wm + l15) * 128 + co; ab[i & 1][i >> 1] = L + 16384 + (64 * wn + l15) * 128 + co; PIN(aa[i & 1][i >> 1]); PIN(ab[i & 1][i >> 1]);
      }
      auto rd1 = [&](int j, int ks, int i) {
        const int sl = (j + 3) & 3; const u32 bs = (sl & 1) * 32768 + (i >> 1) * 2048;
        if (i & 1) fb[ks][i >> 1] = lr<bf16x8>(ab[ks][sl >> 1] + bs); else fa[ks][i >> 1] = lr<bf16x8>(aa[ks][sl >> 1] + bs);
      };
      WAITV(0); LBAR();
      TS(2);
      u64 cy0 = 0; CY0();
#pragma unroll
      for (int i = 0; i < 8; ++i) rd1(0, 0, i);
#pragma unroll
      for (int j = 0; j < 16; ++j) {
#pragma unroll
        for (int ks = 0; ks < 2; ++ks) {
          const bool nx = ks == 0 || j < 15;
          if (ks == 1 && j < 14) LBAR();
          SB();
#pragma unroll
          for (int qq = 0; qq < 16; ++qq) {
            const int m = qq >> 2, n = (m & 1) ? 3 - (qq & 3) : (qq & 3);
            if (j | ks) mmc(acc[m][n], fa[ks][m], fb[ks][n]); else mmz(acc[m][n], fa[ks][m], fb[ks][n]);
            SB();
            if (nx && qq < 8) rd1(j + ks, ks ^ 1, qq);
            SB();
          }
        }
      }
      NOPS();
      CY1(20);
    }
    TS(3);
    c2 h[2][8];
    const u32 sa = L + L_SEG + wn * 256 + l15 * 16;
#pragma unroll
    for (int s = 0; s < 8; ++s) {
#pragma unroll
      for (int ps = 0; ps < 2; ++ps) {
        const f32x4 vr = acc[s >> 1][2 * ps], vi = acc[s >> 1][2 * ps + 1];
        h[ps][s] = (s & 1) ? c2{vr.zw, vi.zw} : c2{vr.xy, vi.xy};
        if (s) { const c2 a = bc(lam, ps); h[ps][s].r = fma2(-a.i, h[ps][s - 1].i, h[ps][s].r); h[ps][s].i = fma2(a.i, h[ps][s - 1].r, h[ps][s].i); }
      }
      SB();
#pragma unroll
      for (int ps = 0; ps < 2; ++ps) if (s) { const c2 a = bc(lam, ps); h[ps][s].r = fma2(a.r, h[ps][s - 1].r, h[ps][s].r); h[ps][s].i = fma2(a.r, h[ps][s - 1].i, h[ps][s].i); }
      SB();
    }
    const c2 ea = {{h[0][7].r.x, h[1][7].r.x}, {h[0][7].i.x, h[1][7].i.x}};
    { const c2 e = cfma(l8, ea, c2{{h[0][7].r.y, h[1][7].r.y}, {h[0][7].i.y, h[1][7].i.y}});
      lw<f32x4>(sa + (4 * wm + g) * 512, f32x4{e.r.x, e.r.y, e.i.x, e.i.y}); }
    gpost(L + L_SYNC, w, 1);
    const c2 l32 = csq(l16), l64 = csq(l32);
    gwait(L + L_SYNC, 1);
    c2 cin;
    {
      c2 e[8];
#pragma unroll
      for (int j = 0; j < 8; ++j) e[j] = asc(lr<f32x4>(sa + j * 512));
      SB();
      const c2 e01 = cfma(l16, e[0], e[1]), e23 = cfma(l16, e[2], e[3]), e45 = cfma(l16, e[4], e[5]), e67 = cfma(l16, e[6], e[7]);
      const c2 e03 = cfma(l32, e01, e23), e47 = cfma(l32, e45, e67), tot = cfma(l64, e03, e47);
      st4c(__builtin_bit_cast(u32x4, f32x4{tot.r.x, tot.r.y, tot.i.x, tot.i.y}), wsr, lane < 16 ? vAg : WS_END, wm ? tt * 8192 : WS_END);
      SB();
      const c2 zero = {{0.f, 0.f}, {0.f, 0.f}};
      const c2 c0 = csel(wm, e03, zero), c2_ = cfma(l32, c0, csel(wm, e45, e01)), cb = csel(g & 2, c2_, c0);
      cin = csel(g & 1, cfma(l16, cb, csel(wm, csel(g & 2, e[6], e[4]), csel(g & 2, e[2], e[0]))), cb);
    }
    TS(4);
    SB();
    c2 pw[8], wt[4], lsa, lsb, ca, cz;
    u32 wa[8], ra[8]; u32x4 v[4];
    {
      const c2 one = {{1.f, 1.f}, {0.f, 0.f}}, zero = {{0.f, 0.f}, {0.f, 0.f}};
#pragma unroll
      for (int s = 0; s < 8; ++s) {
        f32x2 t[4];
#pragma unroll
        for (int ps = 0; ps < 2; ++ps) { const c2 a = bc(coef, ps); t[2 * ps] = a.r * h[ps][s].r; t[2 * ps + 1] = a.i * h[ps][s].r; }
        SB();
#pragma unroll
        for (int ps = 0; ps < 2; ++ps) { const c2 a = bc(coef, ps); h[ps][s].r = fma2(-a.i, h[ps][s].i, t[2 * ps]); h[ps][s].i = fma2(-a.r, h[ps][s].i, -t[2 * ps + 1]); }
        SB();
      }
#pragma unroll
      for (int n = 0; n < 4; ++n) v[n] = bl4c<0>(wsr, vAg + max(tt - 1 - g - 4 * n, 0) * 8192, 0);
      SB();
      const c2 l128 = csq(l64), l256 = csq(l128), l512 = csq(l256);
      lsa = cmul(cmul(coef, csel(g & 1, l16, one)), cmul(csel(g & 2, l32, one), csel(wm, l64, one))); lsb = cmul(lsa, l8);
      wt[0] = cmul(csel(g & 1, l128, one), csel(g & 2, l256, one));
#pragma unroll
      for (int n = 1; n < 4; ++n) wt[n] = cmul(wt[n - 1], l512);
#pragma unroll
      for (int n = 0; n < 4; ++n) { wt[n] = csel(tt - 1 - g - 4 * n >= 0, wt[n], zero); pinc(wt[n]); }
      pw[0] = lam; pw[1] = csq(lam); pw[2] = cmul(pw[1], lam); pw[3] = csq(pw[1]); pw[4] = cmul(pw[3], lam); pw[5] = csq(pw[2]); pw[6] = cmul(pw[3], pw[2]); pw[7] = l8;
#pragma unroll
      for (int s = 1; s < 7; ++s) pinc(pw[s]);
      ca = cmul(coef, cin); cz = cmul(coef, cfma(l8, cin, ea));
      pinc(lsa); pinc(lsb); pinc(ca); pinc(cz);
      const u32 hs = L + 65536 + w * 8192;
#pragma unroll
      for (int i = 0; i < 8; ++i) {
        wa[i] = hs + l15 * 128 + ((4 * g + 16 * i) ^ (8 * (l15 & 1) + 4 * (l15 & 4) + 16 * (l15 & 2) + 8 * (l15 & 8))); PIN(wa[i]);
        ra[i] = hs + (8 * g + q) * 128 + ((8 * pq) ^ (8 * (q & 1) + 16 * (q & 2) + 64 * (g & 1)) ^ (16 * i)); PIN(ra[i]);
      }
    }
    SB();
    TS(5);
    WAITV(0);
    if (tt > 0) {
      auto vok = [&]() { u32 mx = 0;
#pragma unroll
        for (int n = 0; n < 4; ++n) { PIN(v[n]); mx = umax3(umax3(mx, v[n].x, v[n].y), v[n].z, v[n].w); }
        return __all(mx != SENT); };
      if (!vok()) {
        int gp = tt - 1 - g; PIN(gp);
        NOUNROLL for (u32 spn = 0; spn < SPIN_CAP; ++spn) {
#pragma unroll
          for (int n = 0; n < 4; ++n) v[n] = bl4c<0>(wsr, vAg + max(gp - 4 * n, 0) * 8192, 0);
          WAITV(0);
          if (vok()) break;
        }
      }
      c2 cb = cfma(wt[0], asc(__builtin_bit_cast(f32x4, v[0])), cmul(wt[1], asc(__builtin_bit_cast(f32x4, v[1]))));
      const c2 cd = cfma(wt[2], asc(__builtin_bit_cast(f32x4, v[2])), cmul(wt[3], asc(__builtin_bit_cast(f32x4, v[3]))));
      cb.r += cd.r; cb.i += cd.i;
      cb = c2{{sum32(sum16(cb.r.x)), sum32(sum16(cb.r.y))}, {sum32(sum16(cb.i.x)), sum32(sum16(cb.i.y))}};
      ca = cfma(lsa, cb, ca); cz = cfma(lsb, cb, cz);
    } else {
#pragma unroll
      for (int n = 0; n < 4; ++n) PIN(v[n]);
    }
    TS(6);
    {
      const c2 cc[2] = {{{ca.r.x, cz.r.x}, {ca.i.x, cz.i.x}}, {{ca.r.y, cz.r.y}, {ca.i.y, cz.i.y}}};
      u32x4 hq[8];
#pragma unroll
      for (int s = 0; s < 8; ++s) {
#pragma unroll
        for (int ps = 0; ps < 2; ++ps) { const c2 a = bc(pw[s], ps); h[ps][s].r = fma2(-a.i, cc[ps].i, h[ps][s].r); h[ps][s].i = fma2(-a.i, cc[ps].r, h[ps][s].i); }
        SB();
#pragma unroll
        for (int ps = 0; ps < 2; ++ps) { const c2 a = bc(pw[s], ps); h[ps][s].r = fma2(a.r, cc[ps].r, h[ps][s].r); h[ps][s].i = fma2(-a.r, cc[ps].i, h[ps][s].i); }
        SB();
#pragma unroll
        for (int ps = 0; ps < 2; ++ps) { lw<u32>(wa[s] + ps * 2048, pk2(h[ps][s].r)); lw<u32>(wa[s] + ps * 2048 + 4096, pk2(h[ps][s].i)); }
        FENCE();
        if (s & 1) {
          hq[s - 1] = __builtin_bit_cast(u32x4, cat(trr(ra[s - 1]), trr(ra[s] + 512))); hq[s] = __builtin_bit_cast(u32x4, cat(trr(ra[s - 1] + 4096), trr(ra[s] + 4608)));
          if (s > 1) { st4c(hq[s - 3], wsr, vH, WS_H + ((s - 3) >> 1) * (2u << 20)); st4c(hq[s - 2], wsr, vH, WS_H + ((s - 3) >> 1) * (2u << 20) + 32768); }
        }
        SB();
      }
      st4c(hq[6], wsr, vH, WS_H + 3 * (2u << 20)); st4c(hq[7], wsr, vH, WS_H + 3 * (2u << 20) + 32768);
    }
    TS(7);
    {
      bf16x8 fh[2][4], fc[2][4], cp[6][4];
      u32 ha[2] = {L + 65536 + w * 8192 + lane * 16, L + 131072 + w * 8192 + lane * 16}, ca[2];
#pragma unroll
      for (int i = 0; i < 2; ++i) { ca[i] = L + wm * 8192 + l15 * 128 + 16 * ((4 * i + g) ^ (l15 >> 1)); PIN(ca[i]); PIN(ha[i]); }
      auto rd2 = [&](int u, int ks, int i) {
        if (i & 1) { if (u >= 3) fc[ks][i >> 1] = lr<bf16x8>(ca[ks] + (u & 3) * 16384 + (i >> 1) * 2048); } else fh[ks][i >> 1] = lr<bf16x8>(ha[(u % 3) >> 1] + ((u % 3) & 1) * 32768 + (i >> 1) * 2048 + ks * 1024);
      };
      WAITV(0); BAR();
      TS(28);
#pragma unroll
      for (int i = 0; i < 24; ++i) cp[i >> 2][i & 3] = lr<bf16x8>(ca[(i >> 2) & 1] + (i >> 3) * 16384 + (i & 3) * 2048);
      LBAR();
#pragma unroll
      for (int i = 0; i < 24; ++i) PIN(cp[i >> 2][i & 3]);
      BAR();
      gq[0] = bl1c(wsr, vGo, 0); gq[1] = bl1c(wsr, vGo + 128, 0);
      BAR();
      TS(12);
      u64 cy0 = 0; CY0();
#pragma unroll
      for (int i = 0; i < 8; ++i) rd2(0, 0, i);
#pragma unroll
      for (int u = 0; u < 16; ++u) {
#pragma unroll
        for (int ks = 0; ks < 2; ++ks) {
          if (ks == 1 && u < 15) LBAR();
          SB();
#pragma unroll
          for (int qq = 0; qq < 16; ++qq) {
            const int m = qq >> 2, n = (m & 1) ? 3 - (qq & 3) : (qq & 3);
            if (u | ks) mmc(acc[m][n], u < 3 ? cp[(2 * u + ks) % 6][n] : fc[ks][n], fh[ks][m]); else mmz(acc[m][n], cp[0][n], fh[ks][m]);
            SB();
            if ((ks == 0 || u < 15) && qq < 8) rd2(u + ks, ks ^ 1, qq);
            SB();
          }
        }
      }
      NOPS();
      CY1(21);
      WAITV(0); PIN(gq[0]); PIN(gq[1]);
    }
    TS(13);
    const u32 pa = L + g * 256 + (l15 ^ (4 * g)) * 16;
#pragma unroll
    for (int i = 0; i < 16; ++i) lw<f32x4>(pa + (i < 12 ? w * 12288 + i * 1024 : 65536 + w * 8192 + (i & 3) * 1024), acc[i >> 2][i & 3]);
  }
  TS(14);
  {
    const rsrc_t outr = mkr(out, 8u << 20);
    u32 pa[2], ba[4], oa[2]; float og[2];
#pragma unroll
    for (int hi = 0; hi < 2; ++hi) {
      const int row = lr8 + 8 * hi, s1 = 32 * wv + row + 16 * (sh ^ (w & 1));
      pa[hi] = L + (w < 3 ? rh * 12288 + w * 4096 : 65536 + rh * 8192) + lc8 * 256 + (row ^ (4 * pq)) * 16; oa[hi] = (u32)(s2 + 8 * s1) * 4096u + lc8 * 16; og[hi] = rbf(1.f - __uint_as_float(gq[hi]));
      ba[2 * hi] = L + 131072 + (wv * 16 + row) * 256 + 16 * (lc8 ^ row); ba[2 * hi + 1] = ba[2 * hi] ^ 128;
      PIN(pa[hi]); PIN(oa[hi]); PIN(og[hi]); PIN(ba[2 * hi]); PIN(ba[2 * hi + 1]);
    }
    LBAR();
    f32x4 v[4][3];
#pragma unroll
    for (int i = 0; i < 4; ++i) {
#pragma unroll
      for (int u = 0; u < 2; ++u) v[i][u] = lr<f32x4>(pa[i >> 1] + (i & 1) * 2048 + u * (w < 3 ? 24576 : 16384));
      v[i][2] = lr<f32x4>(ba[i]);
    }
    SB();
#pragma unroll
    for (int i = 0; i < 4; ++i)
      st4(__builtin_bit_cast(u32x4, og[i >> 1] * (v[i][0] + v[i][1]) + v[i][2]), outr, oa[i >> 1], 128 * ct + 2048 * (i & 1));
  }
  TS(15);
  asm volatile(".Lcode_hi:");
}

extern "C" void kernel_launch(void* const* d_in, const int* in_sizes, int n_in, void* d_out, int out_size, void* d_ws, size_t ws_size, hipStream_t stream) {
  (void)in_sizes; (void)n_in; (void)out_size; (void)ws_size;
  (void)hipFuncSetAttribute((const void*)k_all, hipFuncAttributeMaxDynamicSharedMemorySize, L_TOT);
  const u16* const* in = (const u16* const*)d_in;
  k_all<<<256, 512, L_TOT, stream>>>(in[0], in[6], in[7], in[10], in[4], in[5], in[1], in[2], in[8], in[9], in[11], in[12], (char*)d_ws, (float*)d_out);
}
```

```cpp
#include <hip/hip_runtime.h>
#include <hip/hip_bf16.h>
#include <stdint.h>

typedef unsigned short u16;
typedef unsigned int u32;
typedef unsigned long long u64;
typedef __attribute__((ext_vector_type(4))) short s16x4;
typedef __attribute__((ext_vector_type(8))) __bf16 bf16x8;
typedef __attribute__((ext_vector_type(2))) __bf16 bf16x2;
typedef __attribute__((ext_vector_type(2))) float f32x2;
typedef __attribute__((ext_vector_type(4))) float f32x4;
typedef __attribute__((ext_vector_type(4))) u32 u32x4;
typedef __attribute__((ext_vector_type(2))) u32 u32x2;
#define DI __device__ __forceinline__
#define LDSP __attribute__((address_space(3)))
#define RLX __ATOMIC_RELAXED, __HIP_MEMORY_SCOPE_AGENT

#define NREP 8
constexpr u32 WS_E = 0, WS_AGG = 65536, WS_FP = 0x40000, WS_FU = 0x60000, WS_FH = WS_FU + NREP * 2048, WS_FT = WS_FH + NREP * 2048, WS_G = WS_FT + NREP * 2048;
constexpr u32 WS_U = 1u << 20, WS_T = 5u << 20, WS_H = 9u << 20, WS_X = 17u << 20, WS_END = 21u << 20;
constexpr u32 L_RA = 65536, L_W = 98304, L_WS = 17408, L_V = 133120, L_CH = 5264, L_PS = 144, L_SYNC = 154176, L_SEG = 154688, L_TOT = 163840;
constexpr u32 SPIN_CAP = 1u << 18, SENT = 0xffffffffu;

DI float bf2f(u32 h) { return __builtin_bit_cast(float, h << 16); }
DI float bfhi(u32 h) { return __builtin_bit_cast(float, h & 0xffff0000u); }
DI u32 pk2(float a, float b) { f32x2 v = {a, b}; return __builtin_bit_cast(u32, __builtin_convertvector(v, bf16x2)); }
DI u32 pk2(f32x2 v) { return __builtin_bit_cast(u32, __builtin_convertvector(v, bf16x2)); }
DI float rbf(float a) { return bf2f(pk2(a, a) & 0xffffu); }
typedef __amdgpu_buffer_rsrc_t rsrc_t;
DI rsrc_t mkr(const void* p, u32 bytes) { return __builtin_amdgcn_make_buffer_rsrc((void*)p, 0, bytes, 0x00020000); }
DI void bldsc(rsrc_t r, u32 vo, u32 so, u32 l, u32 lo) { asm volatile("s_add_u32 m0, %3, %4\n\ts_nop 0\n\tbuffer_load_dwordx4 %0, %1, %2 offen sc1 lds" :: "v"(vo), "s"(r), "s"(so), "s"(l), "i"(lo) : "memory", "scc"); }
DI void blds(rsrc_t r, u32 vo, u32 so, u32 l, u32 lo) { asm volatile("s_add_u32 m0, %3, %4\n\ts_nop 0\n\tbuffer_load_dwordx4 %0, %1, %2 offen lds" :: "v"(vo), "s"(r), "s"(so), "s"(l), "i"(lo) : "memory", "scc"); }
template <class T> DI T lr(u32 a) { return *(const LDSP T*)(uintptr_t)a; }
template <class T> DI void lw(u32 a, T v) { *(LDSP T*)(uintptr_t)a = v; }
DI u32x2 trr(u32 a) { return __builtin_bit_cast(u32x2, __builtin_amdgcn_ds_read_tr16_b64_v4i16((LDSP s16x4*)(uintptr_t)a)); }
DI bf16x8 cat(u32x2 a, u32x2 b) { u32x4 v = {a.x, a.y, b.x, b.y}; return __builtin_bit_cast(bf16x8, v); }
DI bf16x8 pk8(f32x4 a, f32x4 b) { u32x4 v = {pk2(a[0], a[1]), pk2(a[2], a[3]), pk2(b[0], b[1]), pk2(b[2], b[3])}; return __builtin_bit_cast(bf16x8, v); }
DI f32x4 mma(bf16x8 a, bf16x8 b, f32x4 c) { return __builtin_amdgcn_mfma_f32_16x16x32_bf16(a, b, c, 0, 0, 0); }
DI void mmz(f32x4& c, bf16x8 a, bf16x8 b) { asm volatile("v_mfma_f32_16x16x32_bf16 %0, %1, %2, 0" : "=&v"(c) : "v"(a), "v"(b)); }
DI void mmc(f32x4& c, bf16x8 a, bf16x8 b) { asm volatile("v_mfma_f32_16x16x32_bf16 %0, %1, %2, %0" : "+v"(c) : "v"(a), "v"(b)); }
DI bf16x8 asf(u32x4 v) { return __builtin_bit_cast(bf16x8, v); }
DI float cosr(float rev) { return __builtin_amdgcn_cosf(rev); }
DI float sinr(float rev) { return __builtin_amdgcn_sinf(rev); }
template <int O> DI u32x4 bl4(rsrc_t r, u32 vo, u32 so) { u32x4 v; asm volatile("buffer_load_dwordx4 %0, %1, %2, %3 offen offset:%4" : "=v"(v) : "v"(vo), "s"(r), "s"(so), "n"(O) : "memory"); return v; }
template <int O> DI u32x4 bl4c(rsrc_t r, u32 vo, u32 so) { u32x4 v; asm volatile("buffer_load_dwordx4 %0, %1, %2, %3 offen offset:%4 sc1" : "=v"(v) : "v"(vo), "s"(r), "s"(so), "n"(O) : "memory"); return v; }
DI u32 bl1c(rsrc_t r, u32 vo, u32 so) { u32 v; asm volatile("buffer_load_dword %0, %1, %2, %3 offen sc1" : "=v"(v) : "v"(vo), "s"(r), "s"(so) : "memory"); return v; }
DI void touch(rsrc_t r, u32 vo, u32 l) { asm volatile("s_add_u32 m0, %2, %3\n\ts_nop 0\n\tbuffer_load_dword %0, %1, 0 offen lds" :: "v"(vo), "s"(r), "s"(l), "i"(L_SYNC + 256) : "memory", "scc"); }
DI u32 bl1(rsrc_t r, u32 vo, u32 so) { u32 v; asm volatile("buffer_load_dword %0, %1, %2, %3 offen" : "=v"(v) : "v"(vo), "s"(r), "s"(so) : "memory"); return v; }
DI void blh(rsrc_t r, u32 vo, u32 l, u32 lo) { asm volatile("s_add_u32 m0, %2, %3\n\ts_nop 0\n\tbuffer_load_ushort %0, %1, 0 offen lds" :: "v"(vo), "s"(r), "s"(l), "i"(lo) : "memory", "scc"); }
DI u32 gl1(const void* p) { u32 v; asm volatile("global_load_dword %0, %1, off" : "=v"(v) : "v"(p) : "memory"); return v; }
DI u32 glh(const void* p) { u32 v; asm volatile("global_load_ushort %0, %1, off" : "=v"(v) : "v"(p) : "memory"); return v; }
template <int O> DI u32x4 gl4(const void* p) { u32x4 v; asm volatile("global_load_dwordx4 %0, %1, off offset:%2" : "=v"(v) : "v"(p), "n"(O) : "memory"); return v; }
DI void st4c(u32x4 v, rsrc_t r, u32 vo, u32 so) { asm volatile("buffer_store_dwordx4 %0, %1, %2, %3 offen sc1\n\ts_nop 1" :: "v"(v), "v"(vo), "s"(r), "s"(so) : "memory"); }
DI void st2c(u64 v, rsrc_t r, u32 vo, u32 so) { asm volatile("buffer_store_dwordx2 %0, %1, %2, %3 offen sc1" :: "v"(v), "v"(vo), "s"(r), "s"(so) : "memory"); }
DI void st4(u32x4 v, rsrc_t r, u32 vo, u32 so) { asm volatile("buffer_store_dwordx4 %0, %1, %2, %3 offen\n\ts_nop 1" :: "v"(v), "v"(vo), "s"(r), "s"(so) : "memory"); }
#define POLLSLP 2
#define NOUNROLL _Pragma("unroll 1")
#define SB() __builtin_amdgcn_sched_barrier(0)
template <class T> DI void PINS(T& v) { asm volatile("" : "+s"(v)); }
template <class T> DI void PIN(T& v) { asm volatile("" : "+v"(v)); }
#define NOPS() do { SB(); asm volatile("s_nop 7\n\ts_nop 1"); SB(); } while (0)
#define WAITV(n) asm volatile("s_waitcnt vmcnt(%0)" :: "n"(n) : "memory")
#define WAITL(n) asm volatile("s_waitcnt lgkmcnt(%0)" :: "n"(n) : "memory")
#define FENCE() asm volatile("" ::: "memory")
#define BAR() do { FENCE(); __builtin_amdgcn_s_barrier(); FENCE(); } while (0)
#define LBAR() do { WAITL(0); __builtin_amdgcn_s_barrier(); FENCE(); } while (0)
DI u32 umax3(u32 a, u32 b, u32 c) { return max(max(a, b), c); }
DI void gwait(u32 words, u32 phase) {
  NOUNROLL for (u32 s = 0; s < SPIN_CAP; ++s) {
    u32x4 v; asm volatile("ds_read_b128 %0, %1\n\ts_waitcnt lgkmcnt(0)" : "=v"(v) : "v"(words) : "memory");
    if (__builtin_amdgcn_readfirstlane(min(min(v.x, v.y), min(v.z, v.w))) >= phase) break;
  }
  FENCE();
}
DI void gpost(u32 words, int wq, u32 phase) { FENCE(); lw<u32>(words + 4 * wq, phase); FENCE(); }
DI void gbar(u32 words, int wq, u32 phase) { gpost(words, wq, phase); gwait(words, phase); }
DI bool tagged(u32x4 a, u32x4 b, u64 tag) { const u32 lo = (u32)tag, hi = (u32)(tag >> 32); return __all((((a.x ^ lo) | (a.y ^ hi) | (a.z ^ lo)) | ((a.w ^ hi) | (b.x ^ lo) | (b.y ^ hi)) | ((b.z ^ lo) | (b.w ^ hi))) == 0); }
DI void pollflags(rsrc_t wr, u32 off, int lane, u64 tag) {
  NOUNROLL for (u32 s = 0; s < SPIN_CAP; ++s) {
    u32x4 a = bl4c<0>(wr, lane * 16, off), b = bl4c<1024>(wr, lane * 16, off);
    WAITV(0); PIN(a); PIN(b);
    if (tagged(a, b, tag)) break;
    __builtin_amdgcn_s_sleep(2);
  }
}
DI void pollq(rsrc_t wr, u32 off, int lane, u64 tag) {
  NOUNROLL for (u32 s = 0; s < SPIN_CAP; ++s) {
    u64 a; asm volatile("buffer_load_dwordx2 %0, %1, %2, %3 offen sc1\n\ts_waitcnt vmcnt(0)" : "=&v"(a) : "v"(lane * 8), "s"(wr), "s"(off) : "memory");
    if (__all(a == tag)) break;
    __builtin_amdgcn_s_sleep(POLLSLP);
  }
}
#define TSW(n, t) do {} while (0)
#define CY0()
#define CY1(n)
#define ITS(p, j) do {} while (0)
#define TS(n) TSW(n, 0)
#define TX(n) TSW(n, 256)
#define ROT1 ((ti + pj + 8 * (xcd & 1) + 4 * (xcd >> 1)) & 15)
#define ROT2 tt
#define PRI 0
#define WPOS 0
#define NC0 1
#define NW13 4
#define TRP 1
#define JF 5
struct c2 { f32x2 r, i; };
DI f32x2 fma2(f32x2 a, f32x2 b, f32x2 c) { return __builtin_elementwise_fma(a, b, c); }
DI c2 cfma(c2 a, c2 b, c2 c) { return {fma2(a.r, b.r, fma2(-a.i, b.i, c.r)), fma2(a.r, b.i, fma2(a.i, b.r, c.i))}; }
DI c2 cmul(c2 a, c2 b) { return {fma2(-a.i, b.i, a.r * b.r), fma2(a.r, b.i, a.i * b.r)}; }
DI c2 csq(c2 a) { return {fma2(a.r, a.r, -a.i * a.i), (a.r + a.r) * a.i}; }
DI c2 bc(c2 a, int ps) { return ps ? c2{a.r.yy, a.i.yy} : c2{a.r.xx, a.i.xx}; }
DI c2 csel(bool c, c2 a, c2 b) { return {c ? a.r : b.r, c ? a.i : b.i}; }
DI c2 asc(f32x4 v) { return {v.xy, v.zw}; }
DI void pinc(c2& a) { PIN(a.r); PIN(a.i); }
DI f32x2 spread16(float v) { const u32 u = __float_as_uint(v); const auto e = __builtin_amdgcn_permlane16_swap(u, u, false, false); return f32x2{__uint_as_float(e[0]), __uint_as_float(e[1])}; }
DI float sum16(float v) { const f32x2 e = spread16(v); return e.x + e.y; }
DI float sum32(float v) { const u32 u = __float_as_uint(v); const auto e = __builtin_amdgcn_permlane32_swap(u, u, false, false); return __uint_as_float(e[0]) + __uint_as_float(e[1]); }
template <int CTRL> DI float dppadd(float v) { return v + __int_as_float(__builtin_amdgcn_mov_dpp(__float_as_int(v), CTRL, 0xf, 0xf, true)); }

DI u32x4 cos8(float a0, float kk, float c2d) {
  float c[8]; c[0] = cosr(a0); c[1] = cosr(fmaf(kk, 4.f, a0));
#pragma unroll
  for (int j = 2; j < 8; ++j) c[j] = fmaf(c2d, c[j - 1], -c[j - 2]);
  return u32x4{pk2(c[0], c[1]), pk2(c[2], c[3]), pk2(c[4], c[5]), pk2(c[6], c[7])};
}
DI void hart_table(u32x4 (&tab)[2][4], int kap, bool sp, int g) {
  const float kp = (float)kap * (1.f / 256.f), km = sp ? 0.125f : kp, cp = 2.f * cosr(4.f * kp), cm = sp ? -2.f : cp;
#pragma unroll
  for (int r = 0; r < 4; ++r) {
    const float kk = r == 1 && sp ? km : -km, fr_ = (float)(32 * g + r);
    tab[0][r] = cos8(fmaf(kp, fr_, -0.125f), kp, cp); tab[1][r] = cos8(fmaf(kk, fr_, -0.125f), kk, cm);
  }
}
template <bool INV> DI void hart256(f32x2 (&o)[8][2], u32 ap, const u32x4 (&tab)[2][4], int kap, bool sp) {
  const float sg1 = (kap & 1) ? -1.f : 1.f, al1 = sp ? 0.f : 1.f;
  const f32x2 sgn = {sg1, sg1}, al = {al1, al1};
  bf16x8 a[4][2]; f32x4 m[2][4][2];
#pragma unroll
  for (int r = 0; r < 4; ++r) { a[r][0] = cat(trr(ap + r * 8192), trr(ap + r * 8192 + 512)); a[r][1] = cat(trr(ap + r * 8192 + 4096), trr(ap + r * 8192 + 4608)); }
  SB();
#pragma unroll
  for (int r = 0; r < 4; ++r)
#pragma unroll
    for (int i = 0; i < 4; ++i) m[i >> 1][r][i & 1] = mma(a[r][i & 1], asf(tab[(i >> 1) ^ (INV & i & 1)][r]), f32x4{});
  SB();
#pragma unroll
  for (int hh = 0; hh < 2; ++hh) {
    f32x2 p[2][4];
#pragma unroll
    for (int i = 0; i < 8; ++i) { const f32x2 sg_ = INV && i >= 4 ? -sgn : sgn; p[i >> 2][i & 3] = hh ? fma2(sg_, m[i >> 2][i & 3][1].zw, m[i >> 2][i & 3][0].zw) : fma2(sg_, m[i >> 2][i & 3][1].xy, m[i >> 2][i & 3][0].xy); }
    const f32x2 s0 = p[0][1] + p[0][3], d0 = p[0][1] - p[0][3], s1 = INV ? p[1][1] + p[1][3] : fma2(al, p[1][1], p[1][3]), d1 = INV ? p[1][1] - p[1][3] : fma2(-al, p[1][3], p[1][1]);
    const f32x2 x0 = sp ? d0 : d1, x1 = sp ? d1 : d0;
#pragma unroll
    for (int sg = 0; sg < 2; ++sg) {
      const f32x2 A = p[sg][0] + p[sg][2], B = p[sg][0] - p[sg][2], S = sg ? s1 : s0, X = sg ? x1 : x0;
      o[4 * sg][hh] = A + S; o[4 * sg + 2][hh] = A - S; o[4 * sg + 1][hh] = B + X; o[4 * sg + 3][hh] = B - X;
    }
  }
  SB();
#pragma unroll
  for (int i = 0; i < 8; ++i) {
    const auto e0 = __builtin_amdgcn_permlane32_swap(__float_as_uint(o[i][0].x), __float_as_uint(o[i][1].x), false, false);
    const auto e1 = __builtin_amdgcn_permlane32_swap(__float_as_uint(o[i][0].y), __float_as_uint(o[i][1].y), false, false);
    o[i][0] = f32x2{__uint_as_float(e0[0]), __uint_as_float(e1[0])}; o[i][1] = f32x2{__uint_as_float(e0[1]), __uint_as_float(e1[1])};
  }
  SB();
#pragma unroll
  for (int i = 0; i < 8; ++i) { const f32x2 a_ = o[i][0], b_ = o[i][1]; o[i][0] = a_ + b_; o[i][1] = a_ - b_; }
}

struct TileD { int k, c, src, w; };
DI TileD tile_desc(int tau, int k1, int kd1) {
  TileD t; const int i = tau >> 1, ka = tau & 1, hi = i >= 5;
  t.c = kd1 + 2 * ka;
  t.src = k1 ? -1 : (kd1 ? ka : hi);
  t.k = 256 * i + (k1 ? k1 : (kd1 ? 128 * ka : -1152 * hi));
  t.w = k1 ? 1 : (kd1 ? 2 : (tau >= 18 ? 0 : ((i & 3) == 0 && i < 8 ? 1 : 2)));
  return t;
}
DI void m0_fwd(bf16x8& o, int n, int l15, int g, int k1, int kd1) {
  const TileD t = tile_desc(8 * n + (l15 >> 1), k1, kd1); f32x4 v[2];
  const float ak = (float)t.k * (-1.f / 2048.f), a0 = fmaf(ak, (float)(2 * g), -0.25f * (float)(l15 & 1)), ac = -0.25f * (float)t.c;
#pragma unroll
  for (int j = 0; j < 8; ++j) {
    const int rs = j & 1;
    const float c = cosr(a0 + ((j & 2) ? ak : 0.f) + ((j & 4) ? ac : 0.f) + (t.src < 0 ? (rs ? 0.125f : -0.125f) : 0.f));
    v[j >> 2][j & 3] = (t.src >= 0 && (t.w == 0 || t.src != rs)) ? 0.f : c;
  }
  o = pk8(v[0], v[1]);
}
DI void tr2(bf16x8& X, bf16x8& Y, bf16x8 A, bf16x8 B, u32 wr, u32 rd) {
  const u32x4 a = __builtin_bit_cast(u32x4, A), b = __builtin_bit_cast(u32x4, B);
  FENCE(); lw<u32x2>(wr, a.xy); lw<u32x2>(wr + 512, b.xy); lw<u32x2>(wr + 1024, a.zw); lw<u32x2>(wr + 1536, b.zw); FENCE();
  X = cat(trr(rd), trr(rd + 512)); Y = cat(trr(rd + 1024), trr(rd + 1536)); FENCE();
}

template <int O> DI void ldhi(float& v, u32 a) { asm volatile("ds_read_u16_d16_hi %0, %1 offset:%2" : "+v"(v) : "v"(a), "n"(O) : "memory"); }
constexpr int g1_post(int j) { return j <= 1 ? 2 : 0; }
constexpr int g1_wait(int j) { int c = j >= 3 ? g1_post(j - 3) : 0; for (int u = j - 2; u < j; ++u) c += (u + 3 < 16 ? 8 : 0) + (u >= 0 ? g1_post(u) : 0); return c; }
#define TU 12
constexpr int g2_c0(int u) { return u >= 13 ? 36 : TU ? u * 36 / 13 : u < 4 ? 2 * u : 8 + (u - 4) * 28 / 9; }
constexpr int g2_post(int u) { return g2_c0(u + 1) - g2_c0(u) + (u >= TU && u < TU + 4 ? 3 : 0) + (u == TU - 4 ? 1 : 0); }
constexpr int g2_wait(int u) { return (u >= 2 ? g2_post(u - 2) : 0) + (u + 1 < 16 ? 4 : 0) + (u >= 1 ? g2_post(u - 1) : 0); }
template <int N> struct IC { static constexpr int v = N; };
template <int I, int N, class F> DI void sfor(F&& f) { if constexpr (I < N) { f(IC<I>{}); sfor<I + 1, N>(f); } }

typedef __attribute__((ext_vector_type(8))) u64 u64x8;
typedef __attribute__((ext_vector_type(4))) u64 u64x4;
typedef __attribute__((ext_vector_type(2))) u64 u64x2;
__global__ __launch_bounds__(512, 2) void k_all(const u16*, const u16*, const u16*, const u16*, const u16*, const u16*, const u16*, const u16*,
    const u16*, const u16*, const u16*, const u16*, char*, float*) {
#define GETARGS() u64x8 ka; u64x4 kb_; u64x2 kc; \
  asm volatile("s_load_dwordx16 %0, %3, 0x0\n\ts_load_dwordx8 %1, %3, 0x40\n\ts_load_dwordx4 %2, %3, 0x60" : "=&s"(ka), "=&s"(kb_), "=&s"(kc) : "s"(__builtin_amdgcn_kernarg_segment_ptr()) : "memory")
  extern __shared__ __attribute__((aligned(16))) char lds_[];
  const u32 L = (u32)(uintptr_t)(LDSP char*)lds_;
  const int lane = threadIdx.x & 63, wv = __builtin_amdgcn_readfirstlane(threadIdx.x >> 6), w = wv & 3, rh = wv >> 2, l15 = lane & 15, g = lane >> 4;
  const int bid = blockIdx.x, xcd = bid & 7, jj = bid >> 3, ti = jj & 3, pj = jj >> 2;
  const int tt = (xcd >> 1) * 4 + ti, pt = (xcd & 1) * 8 + pj;
  const int s2 = tt >> 1, sh = tt & 1, ct = pt;
  const int lr8 = lane >> 3, lc8 = lane & 7, q = l15 >> 2, pq = lane & 3;
  int rot1 = ROT1, rot2 = ROT2;
  u32 gq[2]; u64 outp, wsp;
#define ARGS() asm volatile("s_waitcnt lgkmcnt(0)" : "+s"(ka), "+s"(kb_), "+s"(kc) :: "memory"); \
  char* ws = (char*)kc[0]; outp = kc[1]; wsp = kc[0]; \
  const rsrc_t xr = mkr((const void*)ka[0], 4u << 20), wsr = mkr(ws, WS_END), cr = mkr((const void*)((ct & 1) ? kb_[1] : kb_[0]), 2u << 20); \
  auto issue_a = [&](int rr, int hf) { const int r = (rr + r0) & 3; blds(xr, vA, (8 * r + 1024 * hf) * 2048, L + r * 8192 + hf * 4096 + w * 1024, L_RA); }; \
  u32 vC[2]; vC[0] = (u32)(32 * (ct ^ (w & 1)) + 512 * (w >> 1) + lr8) * 2048u + 16 * (lc8 ^ (lr8 >> 1)); vC[1] = vC[0] ^ 64; \
  auto c_piece = [&](int i, int e) { blds(cr, vC[e & 1], e * 8 * 2048 + ((i + rot2) & 15) * 128, L + w * 4096, (i % 7) * 16384 + e * 1024); }; \
  const u32 vGo = WS_G + tt * 512 + (u32)((w & 1) * 64 + lr8 * 4 + 2 * rh + (w >> 1)) * 4

  if (rh) {
    GETARGS();
    {
      u64 pc; u32 nb; asm volatile("s_getpc_b64 %0\n.Lcode_lo:\n\ts_mov_b32 %1, .Lcode_hi-.Lcode_lo\n\t.subsection 1\n.Lcode_hi:\n\t.subsection 0" : "=s"(pc), "=s"(nb));
      if (w == 3) touch(mkr((const void*)(pc & ~127ull), min(nb, 61440u)), (lane * 32 + jj) * 128, L);
    }
    const int ii = 8 * (w & 1) + lr8, swz = 16 * (lc8 ^ ((ii >> 1) & 7)), ac = lc8 ^ (((lr8 >> 1) & 1) | ((w & 1) << 1)), r0 = 2 * sh + (ct & 1);
    u32 vB = (u32)(64 * pt + ii) * 2048u + swz, vA = (u32)(s2 + 32 * (8 * w + lr8)) * 2048u + 64 * ct + 16 * (ac & 3) + 1024 * (ac >> 2);
    u32 vP = (64 * pt + 32 * (w & 1) + (lane & 31)) * 2;
    PIN(vA); PIN(vB); PIN(vP);
    lw<u32>(L + L_SYNC + 4 * wv, 0u);
    ARGS();
    const rsrc_t br = mkr((const void*)((w & 2) ? ka[2] : ka[1]), 2u << 20);
    auto issue_b1 = [&](int j, int i) { blds(br, vB, 16 * i * 2048 + ((j + rot1) & 15) * 128, L + w * 1024, ((j + 3) & 3) * 32768 + 16384 + i * 4096); };
    blh(mkr((const void*)ka[3], 2048), vP, L + w * 1024, L_V + 16384); blh(mkr((const void*)ka[4], 2048), vP, L + w * 1024, L_V + 16640); blh(mkr((const void*)ka[5], 2048), vP, L + w * 1024, L_V + 16896);
    issue_a(0, 0); issue_a(0, 1); issue_b1(0, 0); issue_b1(0, 1); issue_b1(0, 2); issue_b1(0, 3);
    BAR();
    SB();
    const u32 vX = (u32)(128 * tt + 16 * (ii >> 2) + 8 * (ii & 1) + 2 * (w >> 1) + ((ii >> 1) & 1)) * 2048u + swz, vl = lane * 16;
    auto issue_x1 = [&](int j, int i) { blds(xr, vX, (64 * (i >> 1) + 4 * (i & 1)) * 2048 + ((j + rot1) & 15) * 128, L + w * 1024, ((j + 3) & 3) * 32768 + i * 4096); };
    u64 tag; asm volatile("buffer_load_dwordx2 %0, off, %1, 0 sc1" : "=v"(tag) : "s"(wsr) : "memory");
    if (w == 2) touch(mkr(ws + WS_FP, WS_G + 8192 - WS_FP), (lane * 256 + bid) * 128, L);
    if (w == 1) touch(mkr((const void*)kb_[3], 2048), ((jj & 15) + 16 * lane) * 128, L);
    const int kap = 16 * sh + l15, ecol = 8 * w + 4 * (g & 1) + 2 * (g >> 1);
    u32 q_ds[2] = {bl1(mkr((const void*)kb_[2], 2048), 2 * ecol, 64 * ct), bl1(mkr((const void*)kb_[2], 2048), 2 * ecol, 64 * ct + 1024)};
    const rsrc_t w0 = mkr((const void*)ka[6], 2048 * 1026), w1 = mkr((const void*)ka[7], 2048 * 1026);
    const u32 rep = (bid & (NREP - 1)) * 2048;
    TX(16);
    const int k1 = xcd * 16 + (jj >> 1), kd1 = jj & 1, cc = kd1 + 2 * (w & 1);
    const bool sp0 = (k1 == 0 && kd1 == 0);
    bf16x8 m0[2], mi[2], m0x, mix[2], fa_r, fa_i, fb_r, fb_i, gb_r, gb_i, ga_r, ga_i; f32x4 t1c, t1s, t2c, t2s;
    u32 vW[5], wa[5], wo[5]; float fr[4][4], fi[4][4], er[4], ei[4];
    const float fl = (float)l15, fg = (float)(4 * g), fc = (float)cc;
    const u32 tw = L + L_SEG + w * 2048 + l15 * 32 + 8 * g, tr = L + L_SEG + w * 2048 + (4 * g + q) * 32 + 8 * pq;
    SB();
    {
      const float st = fmaf(fl, -4.f / 64.f, fc * (-1.f / 64.f));
#pragma unroll
      for (int e = 0; e < 4; ++e) { const float a = (fg + (float)e) * st; t2c[e] = cosr(a); t2s[e] = sinr(a); t1c[e] = cosr(a * (1.f / 16.f)); t1s[e] = sinr(a * (1.f / 16.f)); }
      fa_r = pk8(t2c, -t2s); fa_i = pk8(t2s, t2c); PIN(t1c); PIN(t1s);
      PIN(fa_r); PIN(fa_i);
      WAITV(9); gpost(L + L_SYNC + 16, w, 1);
      const float fm = (float)(l15 < 8 ? l15 : l15 ^ 3) * (-1.f / 16.f); const u32 sl = l15 < 8 ? 0u : 0x80008000u;
#pragma unroll
      for (int e = 0; e < 4; ++e) { const float a = (fg + (float)e) * fm; t2c[e] = cosr(a); t2s[e] = sinr(a); }
      fb_r = pk8(t2c, -t2s); fb_i = asf(__builtin_bit_cast(u32x4, pk8(t2s, t2c)) ^ sl);
      PIN(fb_r); PIN(fb_i);
    }
    u32 xa[2], ua, us, us0, fo, ap0, wp0, tr_, tw_;
    const u32 vo = L + L_V + w * L_CH;
    int wid, wsl; f32x4 uc, us_;
    auto fill = [&](auto k_) {
      constexpr int k = decltype(k_)::v;
      SB();
      if constexpr (k == -4) {
        const float mk = (float)(k1 + 256 * (l15 >> 2)) * (-1.f / 2048.f), mc = -0.25f * (float)(kd1 + (l15 & 2)), ma = fmaf(mk, 0.5f * fg, fmaf((float)(l15 & 1), -0.25f, -0.125f));
        u32x4 f;
#pragma unroll
        for (int e = 0; e < 4; ++e) { const float a = ma + ((e & 1) ? mk : 0.f) + ((e & 2) ? mc : 0.f); f[e] = pk2(cosr(a), -sinr(a)); }
        m0[0] = asf(f); m0[1] = asf(f ^ u32x4{0u, 0x80008000u, 0u, 0x80008000u});
        const float b = fmaf((float)(l15 >> 1), fmaf(fg, -1.f / 32.f, (float)k1 * (-1.f / 2048.f)), fmaf((float)(l15 & 1), 0.75f, fmaf((float)k1, 1.f / 512.f, -0.125f))), b1 = b - 0.25f * (float)kd1;
        const u32 p0 = pk2(cosr(b), sinr(b)), p1 = pk2(cosr(b1), sinr(b1)), sgn = (l15 & 2) ? 0x80008000u : 0u;
        mi[0] = asf(u32x4{p0, p0, p0 ^ sgn, p0 ^ sgn}); mi[1] = asf(u32x4{p1, p1 ^ 0x80008000u, p1 ^ sgn, p1 ^ sgn ^ 0x80008000u});
        if (__builtin_expect(k1 == 0, 0)) { m0_fwd(m0[0], 0, l15, g, 0, kd1); m0_fwd(m0[1], 1, l15, g, 0, kd1); m0_fwd(m0x, 2, l15, g, 0, 0); tr2(mix[0], mix[1], m0x, bf16x8{}, tw, tr); tr2(mi[0], mi[1], m0[0], m0[1], tw, tr); }
        PIN(m0[0]); PIN(m0[1]); PIN(mi[0]); PIN(mi[1]);
      }
      if constexpr (k == -2 || k == -1) { constexpr int i = k + 2; const int n = i ? 63 - kap : kap, rho = 64 * (n & 3) + (n >> 2); xa[i] = L + L_RA + rho * 128 + 16 * (w ^ (((rho >> 1) & 1) | ((rho >> 3) & 1) << 1)) + 2 * (ecol & 7); PIN(xa[i]); }
      if constexpr (k >= 0 && k < 4) {
        const float a = fl * fmaf(fg + (float)k, 4.f / 1024.f, fc * (1.f / 1024.f));
        t2c[k] = cosr(a) * (1.f / 1048576.f); t2s[k] = sinr(a) * (1.f / 1048576.f); uc[k] = cosr(16.f * a); us_[k] = sinr(16.f * a); PIN(t2c); PIN(t2s); PIN(uc); PIN(us_);
      }
      if constexpr (k == 4) { ga_r = pk8(uc, -us_); ga_i = pk8(us_, uc); PIN(ga_r); PIN(ga_i); }
      if constexpr (k >= 5 && k < 9) { const float a = (fg + (g < 2 ? (float)(k - 5) : (float)(8 - k))) * fl * (-1.f / 16.f); uc[k - 5] = cosr(a); us_[k - 5] = sinr(a); PIN(uc); PIN(us_); }
      if constexpr (k == 9) {
        const u32 sg = g < 2 ? 0u : 0x80008000u; const u32x4 t = __builtin_bit_cast(u32x4, pk8(uc, us_));
        gb_r = asf(u32x4{t.x, t.y, t.z ^ sg, t.w ^ sg}); gb_i = asf(u32x4{t.z ^ 0x80008000u, t.w ^ 0x80008000u, t.x ^ sg, t.y ^ sg}); PIN(gb_r); PIN(gb_i);
      }
      if constexpr (k == 12) { ua = L + L_V + (16 * w + g) * 256 + (lr8 & 1) * 128 + 16 * (lc8 ^ g); PIN(ua); }
      if constexpr (k == 13) { us = (u32)(64 * w + 2 * g + (lr8 & 1) + 32 * (sh ^ (w & 1))) * 16384u + lc8 * 16; PIN(us); }
      if constexpr (k == 14) { us0 = (w & 1) && g == 0 ? us ^ (32 * 16384) : us; PIN(us0); }
      if constexpr (k == 32) { fo = lane < NREP ? lane * 2048 + bid * 8 : WS_END; PIN(fo); }
      if constexpr (k == 33) { ap0 = vo + (4 * g + q) * 64 + 16 * pq; PIN(ap0); }
      if constexpr (k == 34) { wp0 = vo + l15 * L_PS + 16 * g; PIN(wp0); }
      if constexpr (k == 35) { tr_ = L + L_V + w * 2 * L_PS + g * L_CH + q * 32 + 8 * pq; PIN(tr_); }
      if constexpr (k == 36) { tw_ = L + L_V + w * 2 * L_PS + q * L_CH + pq * 32 + 8 * g; PIN(tw_); }
      if constexpr (k == 37) { PIN(vC[0]); PIN(vC[1]); }
      if constexpr (k == 40) { wid = 2 * cc + 8 * l15 + 512 * g; PIN(wid); }
      if constexpr (k == 41) { wa[0] = g < 2 ? L + L_W + (w >> 1) * 1056 + ((k1 * 1026) & 15) + wid : L + L_W + (8 + (w >> 1)) * 1056 + ((-k1 * 1026) & 15) + 1664 - wid; PIN(wa[0]); }
      if constexpr (k == 42) { wo[0] = g < 2 ? L + L_W + (8 + (w >> 1)) * 1056 + ((-k1 * 1026) & 15) : L + L_W + (w >> 1) * 1056 + ((k1 * 1026) & 15) + 1024; PIN(wo[0]); }
      if constexpr (k >= 43 && k < 46) { wa[k - 42] = wa[0] + 2112 * (k - 42); wo[k - 42] = wo[0] + 2112 * (k - 42); PIN(wa[k - 42]); PIN(wo[k - 42]); }
      if constexpr (k == 46) {
        if (__builtin_expect(k1 == 0, 0)) {
#pragma unroll
          for (int i = 0; i < 5; ++i) {
            const int kk = tile_desc(w + 4 * i, 0, kd1).k;
            const u32 wp = L + L_W + (kk >> 7) * 1056, wq = L + L_W + (((2048 - kk) & 2047) >> 7) * 1056;
            wa[i] = g < 2 ? wp + wid : wq + 1664 - wid; wo[i] = g < 2 ? wq : wp + 1024;
          }
        }
      }
      if constexpr (k >= 48 && k < 73) {
        constexpr int i = (k - 48) / 5, ph = (k - 48) % 5;
        if (ph == 0) { wid = 64 * (w + 4 * i) + lane; wsl = (wid * 993) >> 16; PIN(wid); PIN(wsl); }
        if (ph == 1) { wsl = min(wsl, 15); wid = 16 * wid - 1056 * wsl; PIN(wid); PIN(wsl); }
        if (ph == 2) { wsl = k1 + (k1 ? 256 : 128) * wsl; PIN(wsl); }
        if (ph == 3) { wsl = wsl < 2048 ? wsl : 4096 - wsl; PIN(wsl); }
        if (ph == 4) { vW[i] = ((wsl * 1026) & ~15) + wid; PIN(vW[i]); }
      }
      if constexpr (k >= 76 && k < 96) {
#pragma unroll
        for (int r = 2 * (k & 1); r < 2 * (k & 1) + 2; ++r) { constexpr int z_ = (k - 76) >> 1; float& z = z_ < 4 ? fr[z_ & 3][r] : z_ < 8 ? fi[z_ & 3][r] : z_ == 8 ? er[r] : ei[r]; z = 0.f; PIN(z); }
      }
      SB();
    };
    auto issue_t = [&](auto j_) {
      constexpr int j = decltype(j_)::v;
      sfor<0, 8>([&](auto i_) { constexpr int i = decltype(i_)::v; if (i < 4) issue_b1(j, i); else issue_x1(j, i - 4); if constexpr (j > 2) fill(IC<8 * j - 8 + i>{}); });
    };
    sfor<-4, 15>(fill);
    TX(17);
    WAITV(0); gpost(L + L_SYNC + 16, w, 2);
#pragma unroll
    for (int i = 0; i < 4; ++i) issue_x1(0, i);
    issue_t(IC<1>{}); issue_t(IC<2>{});
    gwait(L + L_SYNC, 1);
    u32 sk[8][2];
#pragma unroll
    for (int i = 0; i < 8; ++i) { const u32 a = xa[i >> 2] + 2048 * (i < 4 ? i : (i + 3) & 3); sk[i][0] = lr<u32>(a); sk[i][1] = lr<u32>(a + 64); }
    PIN(tag); PIN(q_ds[0]); PIN(q_ds[1]);
    tag += 0x9e3779b97f4a7c15ull;
    if (w == 0) lw<u64>(L + L_SYNC + 32, tag);
    TX(18);
    u32x4 uq[4];
    const f32x4 z4 = {};
    bf16x8 a0[2][2];
    sfor<0, 14>([&](auto j_) {
      constexpr int j = decltype(j_)::v;
      WAITV(g1_wait(j));
      if (j == 0) LBAR(); else BAR();
      ITS(1, j);
      PINS(rot1);
      if (j == 0) {
#pragma unroll
        for (int i = 0; i < 4; ++i) uq[i] = lr<u32x4>((ua ^ (64 * (i & 1))) + 1024 * i);
        if (w == 3) st4c(u32x4{SENT, SENT, SENT, SENT}, wsr, lane < 32 ? vl : WS_END, WS_AGG + tt * 8192 + pt * 512);
      }
      if (j == JF && w == 0) st2c(tag, wsr, fo, WS_FU);
      if (j == 9 && w == 1) { bldsc(wsr, vl, WS_FU + rep, L, L_SEG + 4096); bldsc(wsr, vl, WS_FU + rep + 1024, L, L_SEG + 5120); }
      if (j == 12) { uq[0] = lr<u32x4>(L + L_SEG + 4096 + vl); uq[1] = lr<u32x4>(L + L_SEG + 5120 + vl); }
      SB();
      if constexpr (j < 13) issue_t(IC<j + 3>{});
      SB();
      if constexpr (j <= 1) { st4c(uq[2 * j], wsr, j == 0 ? us0 : us, WS_U + ct * 1024 + s2 * 128 + 2 * j * 8 * 16384); st4c(uq[2 * j + 1], wsr, us, WS_U + ct * 1024 + s2 * 128 + (2 * j + 1) * 8 * 16384); }
      if (j == 12 && __builtin_expect(!tagged(uq[0], uq[1], tag), 0)) pollflags(wsr, WS_FU + rep, lane, tag);
      SB();
    });
#pragma unroll
    for (int i = 0; i < 4; ++i) bldsc(wsr, vl, WS_U + (u32)(2 * k1 + kd1) * 16384u + w * 2048 + (i >> 1) * 8192 + (i & 1) * 1024, L + w * L_CH, L_V + i * 1024);
#pragma unroll
    for (int i = 0; i < NW13; ++i) blds(w0, vW[i], 0, L + w * 1024, L_W + i * 4096);
    WAITV(4 + NW13); BAR();
    ITS(1, 14);
    auto wissue = [&]() {
#pragma unroll
      for (int i = 0; i < 4; ++i) { if (i >= NW13) blds(w0, vW[i], 0, L + w * 1024, L_W + i * 4096); blds(w1, vW[i], 0, L + w * 1024, L_W + L_WS + i * 4096); }
      if (w == 0) { blds(w0, vW[4], 0, L, L_W + 16384); blds(w1, vW[4], 0, L, L_W + L_WS + 16384); }
#pragma unroll
      for (int e = 0; e < 4 * NC0; ++e) c_piece(e >> 2, e & 3);
    };
    {
      WAITV(NW13);
#pragma unroll
      for (int cb = 0; cb < 2; ++cb) { a0[cb][0] = cat(trr(ap0 + 1024 * cb), trr(ap0 + 1024 * cb + 2048)); a0[cb][1] = cat(trr(ap0 + 1024 * cb + 8), trr(ap0 + 1024 * cb + 2056)); }
      FENCE();
      if (WPOS == 0) wissue();
      f32x4 d[2][2][2];
#pragma unroll
      for (int i = 0; i < 8; ++i) d[i >> 2][(i >> 1) & 1][i & 1] = mma(a0[(i >> 1) & 1][i & 1], m0[i >> 2], z4);
#pragma unroll
      for (int i = 0; i < 4; ++i) lw<bf16x8>(wp0 + 16 * (i >> 1) * L_PS + 64 * (i & 1), pk8(d[i >> 1][i & 1][0], d[i >> 1][i & 1][1]));
      if (__builtin_expect(sp0, 0)) {
#pragma unroll
        for (int cb = 0; cb < 2; ++cb) { const bf16x8 o = pk8(mma(a0[cb][0], m0x, z4), mma(a0[cb][1], m0x, z4)); if (l15 < 4) lw<bf16x8>(wp0 + 32 * L_PS + 64 * cb, o); }
      }
      gbar(L + L_SYNC + 16, w, 3);
    }
    TX(8);
    auto tiles = [&](auto nt_, auto i0_) {
      constexpr int NT = decltype(nt_)::v, i0 = decltype(i0_)::v;
      const u32 pr = tr_ + i0 * 8 * L_PS, pw_ = tw_ + i0 * 8 * L_PS;
      bf16x8 a[NT]; f32x4 yr[NT], yi[NT];
#pragma unroll
      for (int i = 0; i < NT; ++i) a[i] = cat(trr(pr + i * 8 * L_PS), trr(pr + i * 8 * L_PS + L_PS));
      SB();
      if (!i0 && WPOS == 1) wissue();
#pragma unroll
      for (int i = 0; i < NT; ++i) { yr[i] = mma(a[i], fa_r, z4); yi[i] = mma(a[i], fa_i, z4); }
      SB();
#pragma unroll
      for (int i = 0; i < NT; ++i) { a[i] = pk8(yr[i] * t1c - yi[i] * t1s, yr[i] * t1s + yi[i] * t1c); yr[i] = mma(fb_r, a[i], z4); yi[i] = mma(fb_i, a[i], z4); SB(); }
      if (!i0) { WAITV(4 * NC0); gbar(L + L_SYNC + 16, w, 4); }
      TX(9);
#pragma unroll
      for (int i = 0; i < NT; ++i) {
        if (i0) {
#pragma unroll
          for (int r = 0; r < 4; ++r) fr[i][r] = fi[i][r] = er[i] = ei[i] = 0.f;
        }
        ldhi<0>(fr[i][0], wa[i0 + i]); ldhi<128>(fr[i][1], wa[i0 + i]); ldhi<256>(fr[i][2], wa[i0 + i]); ldhi<384>(fr[i][3], wa[i0 + i]);
        ldhi<L_WS>(fi[i][0], wa[i0 + i]); ldhi<L_WS + 128>(fi[i][1], wa[i0 + i]); ldhi<L_WS + 256>(fi[i][2], wa[i0 + i]); ldhi<L_WS + 384>(fi[i][3], wa[i0 + i]);
        ldhi<0>(er[i], wo[i0 + i]); ldhi<L_WS>(ei[i], wo[i0 + i]);
      }
      WAITL(0);
#pragma unroll
      for (int i = 0; i < NT; ++i)
#pragma unroll
        for (int r = 0; r < 4; ++r) { PIN(fr[i][r]); PIN(fi[i][r]); PIN(er[i]); PIN(ei[i]); }
      if (cc == 0) {
        const float b0 = lane == 0 ? 0.5f : 0.f, b3 = lane == 32 ? 0.5f : 0.f;
#pragma unroll
        for (int i = 0; i < NT; ++i) {
          float t;
          asm volatile("v_sub_f32 %4, %5, %0\n\tv_fmac_f32 %0, %7, %4\n\tv_add_f32 %4, %6, %1\n\tv_fma_f32 %1, -%7, %4, %1\n\t"
                       "v_sub_f32 %4, %5, %2\n\tv_fmac_f32 %2, %8, %4\n\tv_add_f32 %4, %6, %3\n\tv_fma_f32 %3, -%8, %4, %3"
                       : "+v"(fr[i][0]), "+v"(fi[i][0]), "+v"(fr[i][3]), "+v"(fi[i][3]), "=&v"(t) : "v"(er[i]), "v"(ei[i]), "v"(b0), "v"(b3));
        }
      }
      if (__builtin_expect(k1 == 0, 0)) {
#pragma unroll
        for (int i = 0; i < NT; ++i) { const float wt_ = (float)tile_desc(w + 4 * (i0 + i), 0, kd1).w; yr[i] *= wt_; yi[i] *= wt_; }
      }
      SB();
#pragma unroll
      for (int i = 0; i < NT; ++i) {
        const f32x4 vr = {fr[i][0], fr[i][1], fr[i][2], fr[i][3]}, vi = {fi[i][0], fi[i][1], fi[i][2], fi[i][3]};
        a[i] = pk8(yr[i] * vr - yi[i] * vi, yr[i] * vi + yi[i] * vr); yr[i] = mma(a[i], gb_r, z4); yi[i] = mma(a[i], gb_i, z4);
        SB();
      }
#pragma unroll
      for (int i = 0; i < NT; ++i) {
        a[i] = pk8(yr[i] * t2c - yi[i] * t2s, yr[i] * t2s + yi[i] * t2c); yr[i] = mma(a[i], ga_r, z4); yi[i] = mma(a[i], ga_i, z4);
        SB();
      }
#pragma unroll
      for (int i = 0; i < NT; ++i) { lw<u32x2>(pw_ + i * 8 * L_PS, u32x2{pk2(yr[i].xy), pk2(yr[i].zw)}); lw<u32x2>(pw_ + i * 8 * L_PS + L_PS, u32x2{pk2(yi[i].xy), pk2(yi[i].zw)}); }
    };
    tiles(IC<4>{}, IC<0>{});
    if (__builtin_expect(sp0 && w < 2, 0)) tiles(IC<1>{}, IC<4>{});
    TX(23);
    gbar(L + L_SYNC + 16, w, 5);
    TX(24);
    u32x4 tq[4];
    {
      f32x4 o[2][2][2];
      const u32 ap = vo + (4 * g + q) * L_PS + 16 * pq;
#pragma unroll
      for (int cb = 0; cb < 2; ++cb) { a0[cb][0] = cat(trr(ap + 64 * cb), trr(ap + 64 * cb + 16 * L_PS)); a0[cb][1] = cat(trr(ap + 64 * cb + 8), trr(ap + 64 * cb + 16 * L_PS + 8)); }
#pragma unroll
      for (int i = 0; i < 8; ++i) o[i >> 2][(i >> 1) & 1][i & 1] = mma(a0[(i >> 1) & 1][i & 1], mi[i >> 2], z4);
      if (__builtin_expect(sp0, 0)) {
        const u32 ax = vo + (32 + q) * L_PS + 16 * pq;
#pragma unroll
        for (int i = 0; i < 4; ++i) { const u32x2 t = trr(ax + 64 * (i >> 1) + 8 * (i & 1)); const bf16x8 a = cat(t, t); o[0][i >> 1][i & 1] = mma(a, mix[0], o[0][i >> 1][i & 1]); o[1][i >> 1][i & 1] = mma(a, mix[1], o[1][i >> 1][i & 1]); }
      }
      FENCE();
#pragma unroll
      for (int i = 0; i < 4; ++i) lw<bf16x8>(vo + i * 1280 + l15 * 80 + 16 * g, pk8(o[i >> 1][i & 1][0], o[i >> 1][i & 1][1]));
      FENCE();
#pragma unroll
      for (int i = 0; i < 4; ++i) { tq[i] = lr<u32x4>(vo + i * 1280 + (lane >> 2) * 80 + pq * 16); PIN(tq[i]); }
    }
    TX(10);
    u32x4 tab[2][4];
    if (__builtin_expect(bid == 0 && w == 0, 0)) st2c(tag, wsr, lane == 0 ? WS_E : WS_END, 0);
#pragma unroll
    for (int i = 0; i < 16; ++i) { const u32 xv = sk[i >> 1][i & 1], dv = q_ds[i & 1]; sk[i >> 1][i & 1] = pk2(bf2f(xv & 0xffffu) * bf2f(dv & 0xffffu), bfhi(xv) * bfhi(dv)); PIN(sk[i >> 1][i & 1]); }
    TX(11);
    WAITV(0); BAR();
    if (w == 0) st2c(tag, wsr, fo, WS_FH);
    TX(25);
    {
      const u32 to = (u32)lr8 * (16 * 32768) + (u32)(2 * k1 + kd1) * 128 + lc8 * 16;
#pragma unroll
      for (int i = 0; i < 4; ++i) st4c(tq[i], wsr, to, WS_T + (8 * (i >> 1) + 2 * w + (i & 1)) * 32768);
    }
#pragma unroll
    for (int e = 0; e < 12; ++e) c_piece(4 + (e >> 2), e & 3);
    TX(26);
    pollq(wsr, WS_FH + rep + w * 512, lane, tag);
    BAR();
    TX(27);
    const u32 hb = WS_H + (u32)(tt * 8 + 2 * w) * 65536u + (ct & 1) * 32768;
    auto h_issue = [&](int u) {
      const u32 so = hb + ((u + rot2) & 15) * 2048;
#pragma unroll
      for (int m = 0; m < 4; ++m) bldsc(wsr, vl, so + (m >> 1) * 65536 + (m & 1) * 1024, L + w * 4096, 114688 + (u % 3) * 16384 + m * 1024);
    };
    h_issue(0); h_issue(1);
    u64 ft;
    u32x4 tv[8], gv[2]; u32 ta[8]; float tk, tc;
    f32x2 o[8][2];
    sfor<0, 16>([&](auto u_) {
      constexpr int u = decltype(u_)::v;
      WAITV(g2_wait(u));
      BAR();
      ITS(2, u);
      PINS(rot2);
      if (u == 0 && w == 0) st2c(tag, wsr, fo, WS_FT);
      if (u + 2 < 16) h_issue(u + 2);
#pragma unroll
      for (int e = g2_c0(u); e < g2_c0(u + 1); ++e) c_piece(7 + (e >> 2), e & 3);
      if constexpr (u >= TU && u < TU + 4) {
#pragma unroll
        for (int i = 2 * (u - TU); i < 2 * (u - TU) + 2; ++i) tv[i] = bl4c<0>(wsr, vl, WS_T + (s2 * 16 + ct) * 32768 + i * 4096 + w * 1024);
        if (u < TU + 2) gq[(u - TU) & 1] = bl1c(wsr, vGo + 128 * (u - TU), 0); else if (u == TU + 2) gv[0] = bl4c<0>(wsr, WS_G + tt * 512 + l15 * 16, 0); else gv[1] = bl4c<256>(wsr, WS_G + tt * 512 + (15 - l15) * 16, 0);
      }
      if (u == TU - 4) asm volatile("buffer_load_dwordx2 %0, %1, %2, %3 offen sc1" : "=v"(ft) : "v"(lane * 8), "s"(wsr), "s"(WS_FT + rep + w * 512) : "memory");
      if (u == TU - 1) { PIN(ft); if (__builtin_expect(!__all(ft == tag), 0)) pollq(wsr, WS_FT + rep + w * 512, lane, tag); }
      SB();
      if constexpr (u < 8) {
        if (u == 0) { tk = (float)(2 * kap + 1) * (1.f / 512.f); tc = 2.f * cosr(4.f * tk); PIN(tk); PIN(tc); }
        const float kk = (u & 4) ? -tk : tk;
        tab[u >> 2][u & 3] = cos8(fmaf(kk, 8.f * fg + (float)(u & 3), -0.125f), kk, tc);
        PIN(tab[u >> 2][u & 3]);
      }
      if constexpr (u == 8 || u == 9 || u == 13 || u == 14) {
#pragma unroll
        for (int i = (u < 10 ? 2 * u - 16 : 2 * u - 22); i < (u < 10 ? 2 * u - 14 : 2 * u - 20); ++i) {
          const int kb = 16 * i + 4 * w + g, k = (lane & 4) ? (kb ? 256 - kb : 128) : kb, rho = 64 * (k & 3) + (k >> 2);
          ta[i] = L + 49152 + rho * 128 + 16 * ((((lane >> 1) & 4) | pq) ^ (((rho >> 1) & 1) | ((rho >> 3) & 1) << 1)); PIN(ta[i]);
        }
      }
      SB();
    });
    WAITV(0);
#pragma unroll
    for (int i = 0; i < 8; ++i) { PIN(tv[i]); lw<u32x4>(ta[i], tv[i]); }
    LBAR();
    hart256<true>(o, L + 49152 + (8 * g + q) * 128 + 16 * ((4 * (pq >> 1) + w) ^ (((q >> 1) & 1) | ((g & 1) << 1))) + 8 * (pq & 1), tab, kap, false);
    PIN(gq[0]); PIN(gq[1]); PIN(gv[0]); PIN(gv[1]);
#pragma unroll
    for (int i = 0; i < 8; ++i) {
      const float gg = __uint_as_float(gv[i >> 2][i < 4 ? i : (i + 3) & 3]), og = rbf(1.f - gg);
      const int pos = i < 4 ? l15 : 15 - l15;
#pragma unroll
      for (int hf = 0; hf < 2; ++hf)
        lw<f32x2>(L + 81920 + pos * 256 + 16 * ((8 * hf + 2 * w + (g & 1)) ^ pos) + 8 * (g >> 1) + 4096 * (i < 4 ? 2 * i : 2 * ((i + 3) & 3) + 1), fma2(f32x2{og, og}, f32x2{bf2f(sk[i][hf] & 0xffffu), bfhi(sk[i][hf])}, gg * o[i][hf]));
    }
    TX(30);
  } else {
    GETARGS();
    lw<u32>(L + L_SYNC + 4 * wv, 0u);
    const int ac = lc8 ^ (((lr8 >> 1) & 1) | ((w & 1) << 1)), r0 = 2 * sh + (ct & 1);
    const u32 vA = (u32)(s2 + 32 * (8 * w + lr8)) * 2048u + 64 * ct + 16 * (ac & 3) + 1024 * (ac >> 2);
    ARGS();
    BAR();
    const int wn = w & 1, wm = w >> 1;
    const int hpos = 8 * wm + lc8, hsh = (tt ^ (tt >> 1)) & 1;
    u32 vH = (u32)(lr8 & 1) * (1u << 20) + hsh * (1u << 19) + (tt >> 1) * 65536 + (2 * pt + wn) * 1024 + g * 256 + hpos * 16;
    const u32 vAg = WS_AGG + (u32)((pt * 2 + wn) * 16 + l15) * 16;
    const int kap = 16 * sh + l15, np = (16 - l15) & 15; const bool sp = kap == 0;
    const int ecol = 8 * w + 4 * (g & 1) + 2 * (g >> 1);
    TS(19);
    u32x4 tab[2][4];
    hart_table(tab, kap, sp, g);
#pragma unroll
    for (int i = 0; i < 8; ++i) PIN(tab[i >> 2][i & 3]);
    u32 ua[6], hap = L + L_RA + (8 * g + q) * 128 + 16 * ((4 * (pq >> 1) + w) ^ (((q >> 1) & 1) | ((g & 1) << 1))) + 8 * (pq & 1);
#pragma unroll
    for (int i = 0; i < 2; ++i) {
      const int lo = i ? np : l15;
      ua[i] = L + L_V + lo * 256 + ((2 * ecol) ^ ((lo & 7) << 4)); PIN(ua[i]);
    }
    ua[2] = (ua[1] ^ 64) + (sp ? 0 : 48 * 256); ua[3] = (ua[1] ^ 64) + (sp ? 32 * 256 : 16 * 256); ua[4] = (ua[0] ^ 64) + (sp ? 16 * 256 : 0);
    PIN(ua[2]); PIN(ua[3]); PIN(ua[4]); PIN(hap);
    c2 lam, coef, l8, l16;
    gwait(L + L_SYNC + 16, 1);
    {
      const u32 qa = L + L_V + 16384 + w * 1024 + lane * 4, q_dt = lr<u16>(qa), q_lr = lr<u16>(qa + 256), q_li = lr<u16>(qa + 512);
      const float lre = bf2f(q_lr), lim = bf2f(q_li);
      const float dt = rbf(expf(bf2f(q_dt))), mg = expf(dt * lre);
      float sn, cs; sincosf(dt * lim, &sn, &cs);
      const float nr = mg * cs - 1.f, ni = mg * sn;
      const bool big = fabsf(lre) >= fabsf(lim);
      const float u = big ? lre : lim, v = big ? lim : lre, t = v * __builtin_amdgcn_rcpf(u), rd = 2.f * __builtin_amdgcn_rcpf(u + v * t);
      lam = {spread16(mg * cs), spread16(ni)};
      coef = {spread16(rd * (big ? nr + ni * t : nr * t + ni)), spread16(rd * (big ? ni - nr * t : ni * t - nr))};
      l8 = csq(csq(csq(lam))); l16 = csq(l8);
      pinc(lam); pinc(coef); pinc(l8); pinc(l16);
    }
    TS(22);
#pragma unroll
    for (int i = 2; i < 8; ++i) issue_a(i >> 1, i & 1);
    SB();
    SB();
    c2 pw[8], wt[4], lsa, lsb, l32 = csq(l16), l64 = csq(l32);
    u32 wa[8], ra[8], pa[4], vAs = lane < 16 ? vAg : WS_END, sa = L + L_SEG + wn * 256 + l15 * 16;
    {
      const c2 one = {{1.f, 1.f}, {0.f, 0.f}}, zero = {{0.f, 0.f}, {0.f, 0.f}};
      const c2 l128 = csq(l64), l256 = csq(l128), l512 = csq(l256);
      lsa = cmul(cmul(coef, csel(g & 1, l16, one)), cmul(csel(g & 2, l32, one), csel(wm, l64, one))); lsb = cmul(lsa, l8);
      wt[0] = cmul(csel(g & 1, l128, one), csel(g & 2, l256, one));
#pragma unroll
      for (int n = 1; n < 4; ++n) wt[n] = cmul(wt[n - 1], l512);
#pragma unroll
      for (int n = 0; n < 4; ++n) { wt[n] = csel(tt - 1 - g - 4 * n >= 0, wt[n], zero); pinc(wt[n]); pa[n] = vAg + max(tt - 1 - g - 4 * n, 0) * 8192; PIN(pa[n]); }
      pw[0] = lam; pw[1] = csq(lam); pw[2] = cmul(pw[1], lam); pw[3] = csq(pw[1]); pw[4] = cmul(pw[3], lam); pw[5] = csq(pw[2]); pw[6] = cmul(pw[3], pw[2]); pw[7] = l8;
#pragma unroll
      for (int s = 1; s < 7; ++s) pinc(pw[s]);
      pinc(lsa); pinc(lsb); pinc(l32); pinc(l64); PIN(vH); PIN(vAs); PIN(sa);
    }
      u32 aa[2][2], ab[2][2];
#pragma unroll
      for (int i = 0; i < 4; ++i) {
        const int co = 16 * ((4 * (i & 1) + g) ^ (l15 >> 1)) + (i >> 1) * 65536;
        aa[i & 1][i >> 1] = L + (64 * wm + l15) * 128 + co; ab[i & 1][i >> 1] = L + 16384 + (64 * wn + l15) * 128 + co; PIN(aa[i & 1][i >> 1]); PIN(ab[i & 1][i >> 1]);
      }
    WAITV(0);
    gwait(L + L_SYNC + 16, 2);
    gbar(L + L_SYNC, w, 1);
    TS(1);
    {
      f32x2 o[8][2];
      hart256<false>(o, hap, tab, kap, sp);
#pragma unroll
      for (int i = 0; i < 8; ++i) {
        const u32 a = i == 0 ? ua[0] : i == 1 ? ua[0] + 32 * 256 : i == 2 ? ua[2] : i == 3 ? ua[3] : i == 4 ? ua[4] : i == 5 ? ua[1] + 16 * 256 : i == 6 ? ua[1] + 48 * 256 : ua[4] + 32 * 256;
        lw<u32>(a, pk2(o[i][0])); lw<u32>(a + 128, pk2(o[i][1]));
      }
    }
    u64 tag;
    f32x4 acc[4][4];
    {
      bf16x8 fa[2][4], fb[2][4];
      auto rd1 = [&](int j, int ks, int i) {
        const int sl = (j + 3) & 3; const u32 bs = (sl & 1) * 32768 + (i >> 1) * 2048;
        if (i & 1) fb[ks][i >> 1] = lr<bf16x8>(ab[ks][sl >> 1] + bs); else fa[ks][i >> 1] = lr<bf16x8>(aa[ks][sl >> 1] + bs);
      };
      LBAR();
      TS(2);
      u64 cy0 = 0; CY0();
#pragma unroll
      for (int i = 0; i < 8; ++i) rd1(0, 0, i);
#pragma unroll
      for (int j = 0; j < 16; ++j) {
#pragma unroll
        for (int ks = 0; ks < 2; ++ks) {
          const bool nx = ks == 0 || j < 15;
          if (ks == 1 && j < 14) LBAR();
          SB();
#pragma unroll
          for (int qq = 0; qq < 16; ++qq) {
            const int m = qq >> 2, n = (m & 1) ? 3 - (qq & 3) : (qq & 3);
            if (j | ks) mmc(acc[m][n], fa[ks][m], fb[ks][n]); else mmz(acc[m][n], fa[ks][m], fb[ks][n]);
            SB();
            if (nx && qq < 8) rd1(j + ks, ks ^ 1, qq);
            SB();
          }
        }
      }
      NOPS();
      CY1(20);
    }
    TS(3);
    c2 h[2][8];
#pragma unroll
    for (int s = 0; s < 8; ++s) {
#pragma unroll
      for (int ps = 0; ps < 2; ++ps) {
        const f32x4 vr = acc[s >> 1][2 * ps], vi = acc[s >> 1][2 * ps + 1];
        h[ps][s] = (s & 1) ? c2{vr.zw, vi.zw} : c2{vr.xy, vi.xy};
        if (s) { const c2 a = bc(lam, ps); h[ps][s].r = fma2(-a.i, h[ps][s - 1].i, h[ps][s].r); h[ps][s].i = fma2(a.i, h[ps][s - 1].r, h[ps][s].i); }
      }
      SB();
#pragma unroll
      for (int ps = 0; ps < 2; ++ps) if (s) { const c2 a = bc(lam, ps); h[ps][s].r = fma2(a.r, h[ps][s - 1].r, h[ps][s].r); h[ps][s].i = fma2(a.r, h[ps][s - 1].i, h[ps][s].i); }
      SB();
    }
    const c2 ea = {{h[0][7].r.x, h[1][7].r.x}, {h[0][7].i.x, h[1][7].i.x}};
    { const c2 e = cfma(l8, ea, c2{{h[0][7].r.y, h[1][7].r.y}, {h[0][7].i.y, h[1][7].i.y}});
      lw<f32x4>(sa + (4 * wm + g) * 512, f32x4{e.r.x, e.r.y, e.i.x, e.i.y}); }
    gbar(L + L_SYNC, w, 2);
    u32x4 gx[4], gw[2];
    c2 cin;
    {
      c2 e[8];
#pragma unroll
      for (int j = 0; j < 8; ++j) e[j] = asc(lr<f32x4>(sa + j * 512));
      SB();
      const c2 e01 = cfma(l16, e[0], e[1]), e23 = cfma(l16, e[2], e[3]), e45 = cfma(l16, e[4], e[5]), e67 = cfma(l16, e[6], e[7]);
      const c2 e03 = cfma(l32, e01, e23), e47 = cfma(l32, e45, e67), tot = cfma(l64, e03, e47);
      st4c(__builtin_bit_cast(u32x4, f32x4{tot.r.x, tot.r.y, tot.i.x, tot.i.y}), wsr, vAs, wm ? tt * 8192 : WS_END);
      SB();
      const c2 zero = {{0.f, 0.f}, {0.f, 0.f}};
      const c2 c0 = csel(wm, e03, zero), c2_ = cfma(l32, c0, csel(wm, e45, e01)), cb = csel(g & 2, c2_, c0);
      cin = csel(g & 1, cfma(l16, cb, csel(wm, csel(g & 2, e[6], e[4]), csel(g & 2, e[2], e[0]))), cb);
    }
    pinc(cin);
    TS(4);
    SB();
      { const u32 xo = (bid * 8 + 2 * w) * 2048, vl = lane * 16; const rsrc_t gr = mkr((const void*)kb_[3], 2048);
        gx[0] = bl4<0>(xr, vl, xo); gx[1] = bl4<1024>(xr, vl, xo); gx[2] = bl4<2048>(xr, vl, xo); gx[3] = bl4<3072>(xr, vl, xo); gw[0] = bl4<0>(gr, vl, 0); gw[1] = bl4<1024>(gr, vl, 0); }
    SB();
    c2 ca, cz; u32x4 v[4];
    {
#pragma unroll
      for (int s = 0; s < 8; ++s) {
        f32x2 t[4];
#pragma unroll
        for (int ps = 0; ps < 2; ++ps) { const c2 a = bc(coef, ps); t[2 * ps] = a.r * h[ps][s].r; t[2 * ps + 1] = a.i * h[ps][s].r; }
        SB();
#pragma unroll
        for (int ps = 0; ps < 2; ++ps) { const c2 a = bc(coef, ps); h[ps][s].r = fma2(-a.i, h[ps][s].i, t[2 * ps]); h[ps][s].i = fma2(-a.r, h[ps][s].i, -t[2 * ps + 1]); }
        SB();
      }
#pragma unroll
      for (int s = 0; s < 16; ++s) pinc(h[s & 1][s >> 1]);
#pragma unroll
      for (int n = 0; n < 4; ++n) v[n] = bl4c<0>(wsr, pa[n], 0);
      SB();
      ca = cmul(coef, cin); cz = cmul(coef, cfma(l8, cin, ea));
      pinc(ca); pinc(cz);
      const u32 hs = L + 65536 + w * 8192;
#pragma unroll
      for (int i = 0; i < 8; ++i) {
        wa[i] = hs + l15 * 128 + ((4 * g + 16 * i) ^ (8 * (l15 & 1) + 4 * (l15 & 4) + 16 * (l15 & 2) + 8 * (l15 & 8))); PIN(wa[i]);
        ra[i] = hs + (8 * g + q) * 128 + ((8 * pq) ^ (8 * (q & 1) + 16 * (q & 2) + 64 * (g & 1)) ^ (16 * i)); PIN(ra[i]);
      }
    }
    {
      const u32 vG = WS_G + (u32)(((2 * w + (lane & 1)) * 2 + (((bid >> 4) ^ (bid >> 5)) & 1)) * 128 + ((bid >> 5) & 1) * 64 + (bid & 15) * 4 + (bid >> 6)) * 4;
      float sm[2] = {0.f, 0.f};
      WAITV(4); PIN(gw[0]); PIN(gw[1]);
#pragma unroll
      for (int i = 0; i < 4; ++i) {
        PIN(gx[i]);
#pragma unroll
        for (int e = 0; e < 4; ++e) { const u32 a = gx[i][e], b = gw[i & 1][e]; sm[i >> 1] = __builtin_amdgcn_fdot2_f32_bf16(__builtin_bit_cast(bf16x2, a), __builtin_bit_cast(bf16x2, b), sm[i >> 1], false); }
      }
      sm[0] = dppadd<0xB1>(sm[0]); sm[1] = dppadd<0xB1>(sm[1]);
      float t = (lane & 1) ? sm[1] : sm[0];
      t = dppadd<0x4E>(t); t = dppadd<0x124>(t); t = dppadd<0x128>(t); t = sum32(sum16(t));
      __builtin_amdgcn_raw_buffer_store_b32(__float_as_uint(rbf(1.f / (1.f + expf(-rbf(t))))), wsr, lane < 2 ? vG : WS_END, 0, 16);
    }
    SB();
    TS(5);
    WAITV(1);
    if (tt > 0) {
      auto vok = [&]() { u32 mx = 0;
#pragma unroll
        for (int n = 0; n < 4; ++n) { PIN(v[n]); mx = umax3(umax3(mx, v[n].x, v[n].y), v[n].z, v[n].w); }
        return __all(mx != SENT); };
      if (!vok()) {
        NOUNROLL for (u32 spn = 0; spn < SPIN_CAP; ++spn) {
#pragma unroll
          for (int n = 0; n < 4; ++n) v[n] = bl4c<0>(wsr, pa[n], 0);
          WAITV(0);
          if (vok()) break;
        }
      }
      c2 cb = cfma(wt[0], asc(__builtin_bit_cast(f32x4, v[0])), cmul(wt[1], asc(__builtin_bit_cast(f32x4, v[1]))));
      const c2 cd = cfma(wt[2], asc(__builtin_bit_cast(f32x4, v[2])), cmul(wt[3], asc(__builtin_bit_cast(f32x4, v[3]))));
      cb.r += cd.r; cb.i += cd.i;
      cb = c2{{sum32(sum16(cb.r.x)), sum32(sum16(cb.r.y))}, {sum32(sum16(cb.i.x)), sum32(sum16(cb.i.y))}};
      ca = cfma(lsa, cb, ca); cz = cfma(lsb, cb, cz);
    } else {
#pragma unroll
      for (int n = 0; n < 4; ++n) PIN(v[n]);
    }
    TS(6);
    {
      const c2 cc[2] = {{{ca.r.x, cz.r.x}, {ca.i.x, cz.i.x}}, {{ca.r.y, cz.r.y}, {ca.i.y, cz.i.y}}};
      u32x4 hq[8];
#pragma unroll
      for (int s = 0; s < 8; ++s) {
#pragma unroll
        for (int ps = 0; ps < 2; ++ps) { const c2 a = bc(pw[s], ps); h[ps][s].r = fma2(-a.i, cc[ps].i, h[ps][s].r); h[ps][s].i = fma2(-a.i, cc[ps].r, h[ps][s].i); }
        SB();
#pragma unroll
        for (int ps = 0; ps < 2; ++ps) { const c2 a = bc(pw[s], ps); h[ps][s].r = fma2(a.r, cc[ps].r, h[ps][s].r); h[ps][s].i = fma2(-a.r, cc[ps].i, h[ps][s].i); }
        SB();
#pragma unroll
        for (int ps = 0; ps < 2; ++ps) { lw<u32>(wa[s] + ps * 2048, pk2(h[ps][s].r)); lw<u32>(wa[s] + ps * 2048 + 4096, pk2(h[ps][s].i)); }
        FENCE();
        if (s & 1) {
          hq[s - 1] = __builtin_bit_cast(u32x4, cat(trr(ra[s - 1]), trr(ra[s] + 512))); hq[s] = __builtin_bit_cast(u32x4, cat(trr(ra[s - 1] + 4096), trr(ra[s] + 4608)));
          if (s > 1) { st4c(hq[s - 3], wsr, vH, WS_H + ((s - 3) >> 1) * (2u << 20)); st4c(hq[s - 2], wsr, vH, WS_H + ((s - 3) >> 1) * (2u << 20) + 32768); }
        }
        SB();
      }
      st4c(hq[6], wsr, vH, WS_H + 3 * (2u << 20)); st4c(hq[7], wsr, vH, WS_H + 3 * (2u << 20) + 32768);
    }
    TS(7);
    {
      bf16x8 fh[2][4], fc[2][4], cp[6][4];
      u32 ha = L + 114688 + wm * 8192 + lane * 16, ca[2][2];
#pragma unroll
      for (int i = 0; i < 4; ++i) { ca[i & 1][i >> 1] = L + (i >> 1) * 65536 + wn * 8192 + l15 * 128 + 16 * ((4 * (i & 1) + g) ^ (l15 >> 1)); PIN(ca[i & 1][i >> 1]); }
      PIN(ha);
      auto rd2 = [&](int u, int ks, int i) {
        if (i & 1) { if (u >= 3) fc[ks][i >> 1] = lr<bf16x8>(ca[ks][(u % 7) >> 2] + ((u % 7) & 3) * 16384 + (i >> 1) * 2048); } else fh[ks][i >> 1] = lr<bf16x8>(ha + (u % 3) * 16384 + (i >> 1) * 2048 + ks * 1024);
      };
      tag = lr<u64>(L + L_SYNC + 32); PIN(tag);
#pragma unroll
      for (int e = 4 * NC0; e < 16; ++e) c_piece(e >> 2, e & 3);
      WAITV(16 - 4 * NC0); BAR();
      TS(28);
      WAITV(0); BAR();
      gq[0] = bl1c(wsr, vGo, 0); gq[1] = bl1c(wsr, vGo + 128, 0);
#pragma unroll
      for (int i = 0; i < 24; ++i) cp[i >> 2][i & 3] = lr<bf16x8>(ca[(i >> 2) & 1][0] + (i >> 3) * 16384 + (i & 3) * 2048);
      LBAR();
#pragma unroll
      for (int i = 0; i < 24; ++i) PIN(cp[i >> 2][i & 3]);
      TS(12);
      u64 cy0 = 0; CY0();
#pragma unroll
      for (int i = 0; i < 8; ++i) rd2(0, 0, i);
#pragma unroll
      for (int u = 0; u < 16; ++u) {
#pragma unroll
        for (int ks = 0; ks < 2; ++ks) {
          if (ks == 1 && u < 15) LBAR();
          SB();
#pragma unroll
          for (int qq = 0; qq < 16; ++qq) {
            const int m = qq >> 2, n = (m & 1) ? 3 - (qq & 3) : (qq & 3);
            if (u | ks) mmc(acc[m][n], u < 3 ? cp[(2 * u + ks) % 6][n] : fc[ks][n], fh[ks][m]); else mmz(acc[m][n], cp[0][n], fh[ks][m]);
            SB();
            if ((ks == 0 || u < 15) && qq < 8) rd2(u + ks, ks ^ 1, qq);
            SB();
          }
        }
      }
      NOPS();
      CY1(21);
    }
    TS(13);
    {
      const u32 vl = lane * 16;
#pragma unroll
      for (int m = 0; m < 4; ++m) st4c(__builtin_bit_cast(u32x4, pk8(acc[m][2], acc[m][3])), wsr, vl, WS_X + (((bid ^ 32) * 4 + w) * 4 + m) * 1024);
      BAR();
      WAITV(0); PIN(gq[0]); PIN(gq[1]);
      st2c(tag, wsr, lane == 0 ? 0 : WS_END, WS_FP + ((bid ^ 32) * 4 + w) * 128);
      TS(29);
      NOUNROLL for (u32 spn = 0; spn < SPIN_CAP; ++spn) {
        u64 a; asm volatile("buffer_load_dwordx2 %0, off, %1, %2 sc1\n\ts_waitcnt vmcnt(0)" : "=&v"(a) : "s"(wsr), "s"(WS_FP + (bid * 4 + w) * 128) : "memory");
        if (__all(a == tag)) break;
      }
      const u32 xo = WS_X + (bid * 4 + w) * 4096;
      u32x4 xv[4] = {bl4c<0>(wsr, vl, xo), bl4c<1024>(wsr, vl, xo), bl4c<2048>(wsr, vl, xo), bl4c<3072>(wsr, vl, xo)};
      const u32 pa = L + 131072 + wm * 16384 + wn * 2048 + g * 256 + (l15 ^ (4 * g)) * 16;
      WAITV(0);
      TS(31);
#pragma unroll
      for (int m = 0; m < 4; ++m) {
        PIN(xv[m]);
        lw<f32x4>(pa + m * 4096, acc[m][0] + f32x4{bf2f(xv[m].x & 0xffffu), bfhi(xv[m].x), bf2f(xv[m].y & 0xffffu), bfhi(xv[m].y)});
        lw<f32x4>(pa + m * 4096 + 1024, acc[m][1] + f32x4{bf2f(xv[m].z & 0xffffu), bfhi(xv[m].z), bf2f(xv[m].w & 0xffffu), bfhi(xv[m].w)});
      }
    }
  }
  TS(14);
  {
    const rsrc_t outr = mkr((const void*)outp, 8u << 20);
    u32 pa[2], ba[4], oa[2]; float og[2];
#pragma unroll
    for (int hi = 0; hi < 2; ++hi) {
      const int row = lr8 + 8 * hi, s1 = 32 * wv + row + 16 * (sh ^ (w & 1));
      pa[hi] = L + 131072 + wv * 4096 + lc8 * 256 + (row ^ (4 * pq)) * 16; oa[hi] = (u32)(s2 + 8 * s1) * 4096u + lc8 * 16; og[hi] = rbf(1.f - __uint_as_float(gq[hi]));
      ba[2 * hi] = L + 81920 + (wv * 16 + row) * 256 + 16 * (lc8 ^ row); ba[2 * hi + 1] = ba[2 * hi] ^ 128;
      PIN(pa[hi]); PIN(oa[hi]); PIN(og[hi]); PIN(ba[2 * hi]); PIN(ba[2 * hi + 1]);
    }
    LBAR();
    f32x4 v[4][2];
#pragma unroll
    for (int i = 0; i < 4; ++i) {
      v[i][0] = lr<f32x4>(pa[i >> 1] + (i & 1) * 2048); v[i][1] = lr<f32x4>(ba[i]);
    }
    SB();
#pragma unroll
    for (int i = 0; i < 4; ++i)
      st4(__builtin_bit_cast(u32x4, og[i >> 1] * v[i][0] + v[i][1]), outr, oa[i >> 1], 128 * ct + 2048 * (i & 1));
  }
  TS(15);
}

extern "C" void kernel_launch(void* const* d_in, const int* in_sizes, int n_in, void* d_out, int out_size, void* d_ws, size_t ws_size, hipStream_t stream) {
  (void)in_sizes; (void)n_in; (void)out_size; (void)ws_size;
  (void)hipFuncSetAttribute((const void*)k_all, hipFuncAttributeMaxDynamicSharedMemorySize, L_TOT);
  const u16* const* in = (const u16* const*)d_in;
  k_all<<<256, 512, L_TOT, stream>>>(in[0], in[6], in[7], in[10], in[4], in[5], in[1], in[2], in[8], in[9], in[11], in[12], (char*)d_ws, (float*)d_out);
}
```

```cpp
#include <hip/hip_runtime.h>
#include <hip/hip_bf16.h>
#include <stdint.h>

typedef unsigned short u16;
typedef unsigned int u32;
typedef unsigned long long u64;
typedef __attribute__((ext_vector_type(4))) short s16x4;
typedef __attribute__((ext_vector_type(8))) __bf16 bf16x8;
typedef __attribute__((ext_vector_type(2))) __bf16 bf16x2;
typedef __attribute__((ext_vector_type(2))) float f32x2;
typedef __attribute__((ext_vector_type(4))) float f32x4;
typedef __attribute__((ext_vector_type(4))) u32 u32x4;
typedef __attribute__((ext_vector_type(2))) u32 u32x2;
#define DI __device__ __forceinline__
#define LDSP __attribute__((address_space(3)))
#define RLX __ATOMIC_RELAXED, __HIP_MEMORY_SCOPE_AGENT

#define NREP 8
constexpr u32 WS_E = 0, WS_G = 16384, WS_AGG = 65536, WS_FU = 0x40000, WS_FH = 0x60000, WS_FT = 0x80000, WS_FP = 0xa0000;
constexpr u32 WS_U = 1u << 20, WS_T = 5u << 20, WS_H = 9u << 20, WS_X = 17u << 20, WS_END = 21u << 20;
constexpr u32 L_RA = 65536, L_W = 98304, L_WS = 17408, L_V = 133120, L_CH = 5264, L_PS = 144, L_SYNC = 154176, L_SEG = 154688, L_TOT = 163840;
constexpr u32 SPIN_CAP = 1u << 18, SENT = 0xffffffffu;

DI float bf2f(u32 h) { return __builtin_bit_cast(float, h << 16); }
DI float bfhi(u32 h) { return __builtin_bit_cast(float, h & 0xffff0000u); }
DI u32 pk2(float a, float b) { f32x2 v = {a, b}; return __builtin_bit_cast(u32, __builtin_convertvector(v, bf16x2)); }
DI u32 pk2(f32x2 v) { return __builtin_bit_cast(u32, __builtin_convertvector(v, bf16x2)); }
DI float rbf(float a) { return bf2f(pk2(a, a) & 0xffffu); }
typedef __amdgpu_buffer_rsrc_t rsrc_t;
DI rsrc_t mkr(const void* p, u32 bytes) { return __builtin_amdgcn_make_buffer_rsrc((void*)p, 0, bytes, 0x00020000); }
DI void bldsc(rsrc_t r, u32 vo, u32 so, u32 l, u32 lo) { asm volatile("s_add_u32 m0, %3, %4\n\ts_nop 0\n\tbuffer_load_dwordx4 %0, %1, %2 offen sc1 lds" :: "v"(vo), "s"(r), "s"(so), "s"(l), "i"(lo) : "memory", "scc"); }
DI void blds(rsrc_t r, u32 vo, u32 so, u32 l, u32 lo) { asm volatile("s_add_u32 m0, %3, %4\n\ts_nop 0\n\tbuffer_load_dwordx4 %0, %1, %2 offen lds" :: "v"(vo), "s"(r), "s"(so), "s"(l), "i"(lo) : "memory", "scc"); }
template <class T> DI T lr(u32 a) { return *(const LDSP T*)(uintptr_t)a; }
template <class T> DI void lw(u32 a, T v) { *(LDSP T*)(uintptr_t)a = v; }
DI u32x2 trr(u32 a) { return __builtin_bit_cast(u32x2, __builtin_amdgcn_ds_read_tr16_b64_v4i16((LDSP s16x4*)(uintptr_t)a)); }
DI bf16x8 cat(u32x2 a, u32x2 b) { u32x4 v = {a.x, a.y, b.x, b.y}; return __builtin_bit_cast(bf16x8, v); }
DI bf16x8 pk8(f32x4 a, f32x4 b) { u32x4 v = {pk2(a[0], a[1]), pk2(a[2], a[3]), pk2(b[0], b[1]), pk2(b[2], b[3])}; return __builtin_bit_cast(bf16x8, v); }
DI f32x4 mma(bf16x8 a, bf16x8 b, f32x4 c) { return __builtin_amdgcn_mfma_f32_16x16x32_bf16(a, b, c, 0, 0, 0); }
DI void mmz(f32x4& c, bf16x8 a, bf16x8 b) { asm volatile("v_mfma_f32_16x16x32_bf16 %0, %1, %2, 0" : "=&v"(c) : "v"(a), "v"(b)); }
DI void mmc(f32x4& c, bf16x8 a, bf16x8 b) { asm volatile("v_mfma_f32_16x16x32_bf16 %0, %1, %2, %0" : "+v"(c) : "v"(a), "v"(b)); }
DI bf16x8 asf(u32x4 v) { return __builtin_bit_cast(bf16x8, v); }
DI float cosr(float rev) { return __builtin_amdgcn_cosf(rev); }
DI float sinr(float rev) { return __builtin_amdgcn_sinf(rev); }
template <int O> DI u32x4 bl4(rsrc_t r, u32 vo, u32 so) { u32x4 v; asm volatile("buffer_load_dwordx4 %0, %1, %2, %3 offen offset:%4" : "=v"(v) : "v"(vo), "s"(r), "s"(so), "n"(O) : "memory"); return v; }
template <int O> DI u32x4 bl4c(rsrc_t r, u32 vo, u32 so) { u32x4 v; asm volatile("buffer_load_dwordx4 %0, %1, %2, %3 offen offset:%4 sc1" : "=v"(v) : "v"(vo), "s"(r), "s"(so), "n"(O) : "memory"); return v; }
DI u32 bl1c(rsrc_t r, u32 vo, u32 so) { u32 v; asm volatile("buffer_load_dword %0, %1, %2, %3 offen sc1" : "=v"(v) : "v"(vo), "s"(r), "s"(so) : "memory"); return v; }
DI void blh(rsrc_t r, u32 vo, u32 l, u32 lo) { asm volatile("s_add_u32 m0, %2, %3\n\ts_nop 0\n\tbuffer_load_ushort %0, %1, 0 offen lds" :: "v"(vo), "s"(r), "s"(l), "i"(lo) : "memory", "scc"); }
DI u32 gl1(const void* p) { u32 v; asm volatile("global_load_dword %0, %1, off" : "=v"(v) : "v"(p) : "memory"); return v; }
DI u32 glh(const void* p) { u32 v; asm volatile("global_load_ushort %0, %1, off" : "=v"(v) : "v"(p) : "memory"); return v; }
template <int O> DI u32x4 gl4(const void* p) { u32x4 v; asm volatile("global_load_dwordx4 %0, %1, off offset:%2" : "=v"(v) : "v"(p), "n"(O) : "memory"); return v; }
DI void st4c(u32x4 v, rsrc_t r, u32 vo, u32 so) { asm volatile("buffer_store_dwordx4 %0, %1, %2, %3 offen sc1\n\ts_nop 1" :: "v"(v), "v"(vo), "s"(r), "s"(so) : "memory"); }
DI void st2c(u64 v, rsrc_t r, u32 vo, u32 so) { asm volatile("buffer_store_dwordx2 %0, %1, %2, %3 offen sc1" :: "v"(v), "v"(vo), "s"(r), "s"(so) : "memory"); }
DI void st4(u32x4 v, rsrc_t r, u32 vo, u32 so) { asm volatile("buffer_store_dwordx4 %0, %1, %2, %3 offen\n\ts_nop 1" :: "v"(v), "v"(vo), "s"(r), "s"(so) : "memory"); }
#define POLLSLP 2
#define NOUNROLL _Pragma("unroll 1")
#define SB() __builtin_amdgcn_sched_barrier(0)
template <class T> DI void PINS(T& v) { asm volatile("" : "+s"(v)); }
template <class T> DI void PIN(T& v) { asm volatile("" : "+v"(v)); }
#define NOPS() do { SB(); asm volatile("s_nop 7\n\ts_nop 1"); SB(); } while (0)
#define WAITV(n) asm volatile("s_waitcnt vmcnt(%0)" :: "n"(n) : "memory")
#define WAITL(n) asm volatile("s_waitcnt lgkmcnt(%0)" :: "n"(n) : "memory")
#define FENCE() asm volatile("" ::: "memory")
#define BAR() do { FENCE(); __builtin_amdgcn_s_barrier(); FENCE(); } while (0)
#define LBAR() do { WAITL(0); __builtin_amdgcn_s_barrier(); FENCE(); } while (0)
DI u32 umax3(u32 a, u32 b, u32 c) { return max(max(a, b), c); }
DI void gwait(u32 words, u32 phase) {
  NOUNROLL for (u32 s = 0; s < SPIN_CAP; ++s) {
    u32x4 v; asm volatile("ds_read_b128 %0, %1\n\ts_waitcnt lgkmcnt(0)" : "=v"(v) : "v"(words) : "memory");
    if (__builtin_amdgcn_readfirstlane(min(min(v.x, v.y), min(v.z, v.w))) >= phase) break;
  }
  FENCE();
}
DI void gpost(u32 words, int wq, u32 phase) { FENCE(); lw<u32>(words + 4 * wq, phase); FENCE(); }
DI void gbar(u32 words, int wq, u32 phase) { gpost(words, wq, phase); gwait(words, phase); }
DI bool tagged(u32x4 a, u32x4 b, u64 tag) { const u32 lo = (u32)tag, hi = (u32)(tag >> 32); return __all((((a.x ^ lo) | (a.y ^ hi) | (a.z ^ lo)) | ((a.w ^ hi) | (b.x ^ lo) | (b.y ^ hi)) | ((b.z ^ lo) | (b.w ^ hi))) == 0); }
DI void pollflags(rsrc_t wr, u32 off, int lane, u64 tag) {
  NOUNROLL for (u32 s = 0; s < SPIN_CAP; ++s) {
    u32x4 a = bl4c<0>(wr, lane * 16, off), b = bl4c<1024>(wr, lane * 16, off);
    WAITV(0); PIN(a); PIN(b);
    if (tagged(a, b, tag)) break;
    __builtin_amdgcn_s_sleep(2);
  }
}
DI void pollq(rsrc_t wr, u32 off, int lane, u64 tag) {
  NOUNROLL for (u32 s = 0; s < SPIN_CAP; ++s) {
    u64 a; asm volatile("buffer_load_dwordx2 %0, %1, %2, %3 offen sc1\n\ts_waitcnt vmcnt(0)" : "=&v"(a) : "v"(lane * 8), "s"(wr), "s"(off) : "memory");
    if (__all(a == tag)) break;
    __builtin_amdgcn_s_sleep(POLLSLP);
  }
}
#define TSW(n, t) do {} while (0)
#define CY0()
#define CY1(n)
#define ITS(p, j) do {} while (0)
#define TS(n) TSW(n, 0)
#define TX(n) TSW(n, 256)
#define ROT1 ((ti + pj + 8 * (xcd & 1) + 4 * (xcd >> 1)) & 15)
#define ROT2 tt
#define PRI 0
#define WPOS 0
#define NC0 1
#define NW13 4
#define JF 5
struct c2 { f32x2 r, i; };
DI f32x2 fma2(f32x2 a, f32x2 b, f32x2 c) { return __builtin_elementwise_fma(a, b, c); }
DI c2 cfma(c2 a, c2 b, c2 c) { return {fma2(a.r, b.r, fma2(-a.i, b.i, c.r)), fma2(a.r, b.i, fma2(a.i, b.r, c.i))}; }
DI c2 cmul(c2 a, c2 b) { return {fma2(-a.i, b.i, a.r * b.r), fma2(a.r, b.i, a.i * b.r)}; }
DI c2 csq(c2 a) { return {fma2(a.r, a.r, -a.i * a.i), (a.r + a.r) * a.i}; }
DI c2 bc(c2 a, int ps) { return ps ? c2{a.r.yy, a.i.yy} : c2{a.r.xx, a.i.xx}; }
DI c2 csel(bool c, c2 a, c2 b) { return {c ? a.r : b.r, c ? a.i : b.i}; }
DI c2 asc(f32x4 v) { return {v.xy, v.zw}; }
DI void pinc(c2& a) { PIN(a.r); PIN(a.i); }
DI f32x2 spread16(float v) { const u32 u = __float_as_uint(v); const auto e = __builtin_amdgcn_permlane16_swap(u, u, false, false); return f32x2{__uint_as_float(e[0]), __uint_as_float(e[1])}; }
DI float sum16(float v) { const f32x2 e = spread16(v); return e.x + e.y; }
DI float sum32(float v) { const u32 u = __float_as_uint(v); const auto e = __builtin_amdgcn_permlane32_swap(u, u, false, false); return __uint_as_float(e[0]) + __uint_as_float(e[1]); }
template <int CTRL> DI float dppadd(float v) { return v + __int_as_float(__builtin_amdgcn_mov_dpp(__float_as_int(v), CTRL, 0xf, 0xf, true)); }

DI u32x4 cos8(float a0, float kk, float c2d) {
  float c[8]; c[0] = cosr(a0); c[1] = cosr(fmaf(kk, 4.f, a0));
#pragma unroll
  for (int j = 2; j < 8; ++j) c[j] = fmaf(c2d, c[j - 1], -c[j - 2]);
  return u32x4{pk2(c[0], c[1]), pk2(c[2], c[3]), pk2(c[4], c[5]), pk2(c[6], c[7])};
}
DI void hart_table(u32x4 (&tab)[2][4], int kap, bool sp, int g) {
  const float kp = (float)kap * (1.f / 256.f), km = sp ? 0.125f : kp, cp = 2.f * cosr(4.f * kp), cm = sp ? -2.f : cp;
#pragma unroll
  for (int r = 0; r < 4; ++r) {
    const float kk = r == 1 && sp ? km : -km, fr_ = (float)(32 * g + r);
    tab[0][r] = cos8(fmaf(kp, fr_, -0.125f), kp, cp); tab[1][r] = cos8(fmaf(kk, fr_, -0.125f), kk, cm);
  }
}
template <bool INV> DI void hart256(f32x2 (&o)[8][2], u32 ap, const u32x4 (&tab)[2][4], int kap, bool sp) {
  const float sg1 = (kap & 1) ? -1.f : 1.f, al1 = sp ? 0.f : 1.f;
  const f32x2 sgn = {sg1, sg1}, al = {al1, al1};
  bf16x8 a[4][2]; f32x4 m[2][4][2];
#pragma unroll
  for (int r = 0; r < 4; ++r) { a[r][0] = cat(trr(ap + r * 8192), trr(ap + r * 8192 + 512)); a[r][1] = cat(trr(ap + r * 8192 + 4096), trr(ap + r * 8192 + 4608)); }
  SB();
#pragma unroll
  for (int r = 0; r < 4; ++r)
#pragma unroll
    for (int i = 0; i < 4; ++i) m[i >> 1][r][i & 1] = mma(a[r][i & 1], asf(tab[(i >> 1) ^ (INV & i & 1)][r]), f32x4{});
  SB();
#pragma unroll
  for (int hh = 0; hh < 2; ++hh) {
    f32x2 p[2][4];
#pragma unroll
    for (int i = 0; i < 8; ++i) { const f32x2 sg_ = INV && i >= 4 ? -sgn : sgn; p[i >> 2][i & 3] = hh ? fma2(sg_, m[i >> 2][i & 3][1].zw, m[i >> 2][i & 3][0].zw) : fma2(sg_, m[i >> 2][i & 3][1].xy, m[i >> 2][i & 3][0].xy); }
    const f32x2 s0 = p[0][1] + p[0][3], d0 = p[0][1] - p[0][3], s1 = INV ? p[1][1] + p[1][3] : fma2(al, p[1][1], p[1][3]), d1 = INV ? p[1][1] - p[1][3] : fma2(-al, p[1][3], p[1][1]);
    const f32x2 x0 = sp ? d0 : d1, x1 = sp ? d1 : d0;
#pragma unroll
    for (int sg = 0; sg < 2; ++sg) {
      const f32x2 A = p[sg][0] + p[sg][2], B = p[sg][0] - p[sg][2], S = sg ? s1 : s0, X = sg ? x1 : x0;
      o[4 * sg][hh] = A + S; o[4 * sg + 2][hh] = A - S; o[4 * sg + 1][hh] = B + X; o[4 * sg + 3][hh] = B - X;
    }
  }
  SB();
#pragma unroll
  for (int i = 0; i < 8; ++i) {
    const auto e0 = __builtin_amdgcn_permlane32_swap(__float_as_uint(o[i][0].x), __float_as_uint(o[i][1].x), false, false);
    const auto e1 = __builtin_amdgcn_permlane32_swap(__float_as_uint(o[i][0].y), __float_as_uint(o[i][1].y), false, false);
    o[i][0] = f32x2{__uint_as_float(e0[0]), __uint_as_float(e1[0])}; o[i][1] = f32x2{__uint_as_float(e0[1]), __uint_as_float(e1[1])};
  }
  SB();
#pragma unroll
  for (int i = 0; i < 8; ++i) { const f32x2 a_ = o[i][0], b_ = o[i][1]; o[i][0] = a_ + b_; o[i][1] = a_ - b_; }
}

struct TileD { int k, c, src, w; };
DI TileD tile_desc(int tau, int k1, int kd1) {
  TileD t; const int i = tau >> 1, ka = tau & 1, hi = i >= 5;
  t.c = kd1 + 2 * ka;
  t.src = k1 ? -1 : (kd1 ? ka : hi);
  t.k = 256 * i + (k1 ? k1 : (kd1 ? 128 * ka : -1152 * hi));
  t.w = k1 ? 1 : (kd1 ? 2 : (tau >= 18 ? 0 : ((i & 3) == 0 && i < 8 ? 1 : 2)));
  return t;
}
DI void m0_fwd(bf16x8& o, int n, int l15, int g, int k1, int kd1, bool inv = false) {
  const TileD t = tile_desc(8 * n + (l15 >> 1), k1, kd1); f32x4 v[2];
  const float ak = (float)t.k * (-1.f / 2048.f), a0 = fmaf(ak, (float)(2 * g), -0.25f * (float)(l15 & 1)) + (inv ? (float)k1 * (1.f / 512.f) : 0.f), ac = -0.25f * (float)t.c;
#pragma unroll
  for (int j = 0; j < 8; ++j) {
    const int rs = j & 1;
    const float c = cosr(a0 + ((j & 2) ? ak : 0.f) + ((j & 4) ? ac : 0.f) + (t.src < 0 ? (rs ? (inv ? 0.625f : 0.125f) : -0.125f) : 0.f));
    v[j >> 2][j & 3] = (t.src >= 0 && (t.w == 0 || t.src != rs)) ? 0.f : c;
  }
  o = pk8(v[0], v[1]);
}
DI void tr2(bf16x8& X, bf16x8& Y, bf16x8 A, bf16x8 B, u32 wr, u32 rd) {
  const u32x4 a = __builtin_bit_cast(u32x4, A), b = __builtin_bit_cast(u32x4, B);
  FENCE(); lw<u32x2>(wr, a.xy); lw<u32x2>(wr + 512, b.xy); lw<u32x2>(wr + 1024, a.zw); lw<u32x2>(wr + 1536, b.zw); FENCE();
  X = cat(trr(rd), trr(rd + 512)); Y = cat(trr(rd + 1024), trr(rd + 1536)); FENCE();
}

template <int O> DI void ldhi(float& v, u32 a) { asm volatile("ds_read_u16_d16_hi %0, %1 offset:%2" : "+v"(v) : "v"(a), "n"(O) : "memory"); }
constexpr int g1_post(int j) { return j <= 1 ? 2 : 0; }
constexpr int g1_wait(int j) { int c = j >= 3 ? g1_post(j - 3) : 0; for (int u = j - 2; u < j; ++u) c += (u + 3 < 16 ? 8 : 0) + (u >= 0 ? g1_post(u) : 0); return c; }
#define TU 12
constexpr int g2_c0(int u) { return u >= 13 ? 36 : TU ? u * 36 / 13 : u < 4 ? 2 * u : 8 + (u - 4) * 28 / 9; }
constexpr int g2_post(int u) { return g2_c0(u + 1) - g2_c0(u) + (u >= TU && u < TU + 4 ? 3 : 0) + (u == TU - 4 ? 1 : 0); }
constexpr int g2_wait(int u) { return (u >= 2 ? g2_post(u - 2) : 0) + (u + 1 < 16 ? 4 : 0) + (u >= 1 ? g2_post(u - 1) : 0); }
template <int N> struct IC { static constexpr int v = N; };
template <int I, int N, class F> DI void sfor(F&& f) { if constexpr (I < N) { f(IC<I>{}); sfor<I + 1, N>(f); } }

typedef __attribute__((ext_vector_type(4))) u64 u64x4;
typedef __attribute__((ext_vector_type(2))) u64 u64x2;
__global__ __launch_bounds__(512, 2) void k_all(const u16*, const u16*, const u16*, const u16*, const u16*, const u16*, const u16* __restrict__ wre, const u16* __restrict__ wim,
    const u16* __restrict__ c_re, const u16* __restrict__ c_im, const u16* __restrict__ dskip, const u16* __restrict__ gate_w, char* __restrict__ ws, float* __restrict__ out) {
  u64x4 ka; u64x2 kb_;
  asm volatile("s_load_dwordx8 %0, %2, 0x0\n\ts_load_dwordx4 %1, %2, 0x20" : "=&s"(ka), "=&s"(kb_) : "s"(__builtin_amdgcn_kernarg_segment_ptr()) : "memory");
  extern __shared__ __attribute__((aligned(16))) char lds_[];
  const u32 L = (u32)(uintptr_t)(LDSP char*)lds_;
  const int lane = threadIdx.x & 63, wv = __builtin_amdgcn_readfirstlane(threadIdx.x >> 6), w = wv & 3, rh = wv >> 2, l15 = lane & 15, g = lane >> 4;
  const int bid = blockIdx.x, xcd = bid & 7, jj = bid >> 3, ti = jj & 3, pj = jj >> 2;
  const int tt = (xcd >> 1) * 4 + ti, pt = (xcd & 1) * 8 + pj;
  const int s2 = tt >> 1, sh = tt & 1, ct = pt;
  const int lr8 = lane >> 3, lc8 = lane & 7, q = l15 >> 2, pq = lane & 3;
  int rot1 = ROT1, rot2 = ROT2;
  {
    u64 pc; u32 nb; asm volatile("s_getpc_b64 %0\n.Lcode_lo:\n\ts_mov_b32 %1, .Lcode_hi-.Lcode_lo\n\t.subsection 1\n.Lcode_hi:\n\t.subsection 0" : "=s"(pc), "=s"(nb));
    if (wv == 7) { const rsrc_t pr = mkr((const void*)(pc & ~127ull), min(nb, 61440u)); asm volatile("s_mov_b32 m0, %2\n\ts_nop 0\n\tbuffer_load_dword %0, %1, 0 offen lds" :: "v"((lane * 32 + jj) * 128), "s"(pr), "s"(L + L_SYNC + 256) : "memory"); }
  }
  const int ii = 8 * (w & 1) + lr8, swz = 16 * (lc8 ^ ((ii >> 1) & 7)), ac = lc8 ^ (((lr8 >> 1) & 1) | ((w & 1) << 1)), r0 = 2 * sh + (ct & 1);
  u32 vB = (u32)(64 * pt + ii) * 2048u + swz, vA = (u32)(s2 + 32 * (8 * w + lr8)) * 2048u + 64 * ct + 16 * (ac & 3) + 1024 * (ac >> 2);
  u32 vP = (64 * pt + 32 * (w & 1) + (lane & 31)) * 2;
  PIN(vA); PIN(vB); PIN(vP);
  lw<u32>(L + L_SYNC + 4 * wv, 0u);
  asm volatile("s_waitcnt lgkmcnt(0)" : "+s"(ka), "+s"(kb_) :: "memory");
  const u16 *x = (const u16*)ka[0], *b_re = (const u16*)ka[1], *b_im = (const u16*)ka[2], *log_dt = (const u16*)ka[3], *lam_re = (const u16*)kb_[0], *lam_im = (const u16*)kb_[1];
  const rsrc_t xr = mkr(x, 4u << 20), br = mkr((w & 2) ? b_im : b_re, 2u << 20);
  auto issue_a = [&](int rr, int hf) { const int r = (rr + r0) & 3; blds(xr, vA, (8 * r + 1024 * hf) * 2048, L + r * 8192 + hf * 4096 + w * 1024, L_RA); };
  auto issue_b1 = [&](int j, int i) { blds(br, vB, 16 * i * 2048 + ((j + rot1) & 15) * 128, L + w * 1024, ((j + 3) & 3) * 32768 + 16384 + i * 4096); };
  if (!rh) {
    blh(mkr(log_dt, 2048), vP, L + w * 1024, L_V + 16384); blh(mkr(lam_re, 2048), vP, L + w * 1024, L_V + 16640); blh(mkr(lam_im, 2048), vP, L + w * 1024, L_V + 16896);
    issue_a(0, 0); issue_a(0, 1);
  } else { issue_b1(0, 0); issue_b1(0, 1); issue_b1(0, 2); issue_b1(0, 3); }
  LBAR();
  SB();
  const rsrc_t wsr = mkr(ws, WS_END);
  const u32 vX = (u32)(128 * tt + 16 * (ii >> 2) + 8 * (ii & 1) + 2 * (w >> 1) + ((ii >> 1) & 1)) * 2048u + swz;
  auto issue_x1 = [&](int j, int i) { blds(xr, vX, (64 * (i >> 1) + 4 * (i & 1)) * 2048 + ((j + rot1) & 15) * 128, L + w * 1024, ((j + 3) & 3) * 32768 + i * 4096); };
  const rsrc_t cr = mkr((ct & 1) ? c_im : c_re, 2u << 20);
  u32 vC[2]; vC[0] = (u32)(32 * (ct ^ (w & 1)) + 512 * (w >> 1) + lr8) * 2048u + 16 * (lc8 ^ (lr8 >> 1)); vC[1] = vC[0] ^ 64;
  auto c_piece = [&](int i, int e) { blds(cr, vC[e & 1], e * 8 * 2048 + ((i + rot2) & 15) * 128, L + w * 4096, (i % 7) * 16384 + e * 1024); };
  const u32 vGo = WS_G + tt * 512 + (u32)((w & 1) * 64 + lr8 * 4 + 2 * rh + (w >> 1)) * 4;
  u32 gq[2];

  if (rh) {
    if (PRI) __builtin_amdgcn_s_setprio(3);
    const u32 vl = lane * 16;
    u64 tag; asm volatile("buffer_load_dwordx2 %0, off, %1, 0 sc1" : "=v"(tag) : "s"(wsr) : "memory");
    if (w == 2) {
      const u32 o = lane < (3 * NREP + 15) / 16 ? WS_FU + ((bid + 256 * lane) / (16 * NREP)) * 0x20000 + ((bid + 256 * lane) % (16 * NREP)) * 128 : lane >= 60 ? WS_FP + (bid * 4 + (lane & 3)) * 128 : (lane == 59 && bid < 64 ? WS_G + bid * 128 : WS_END);
      asm volatile("s_mov_b32 m0, %2\n\ts_nop 0\n\tbuffer_load_dword %0, %1, 0 offen lds" :: "v"(o), "s"(wsr), "s"(L + L_SYNC + 256) : "memory");
    }
    if (w == 1) asm volatile("s_mov_b32 m0, %2\n\ts_nop 0\n\tbuffer_load_dword %0, %1, 0 offen lds" :: "v"(((jj & 15) + 16 * lane) * 128), "s"(mkr(gate_w, 2048)), "s"(L + L_SYNC + 256) : "memory");
    const int kap = 16 * sh + l15, ecol = 8 * w + 4 * (g & 1) + 2 * (g >> 1);
    u32 q_ds[2] = {gl1(dskip + 32 * ct + ecol), gl1(dskip + 512 + 32 * ct + ecol)};
    const rsrc_t w0 = mkr(wre, 2048 * 1026), w1 = mkr(wim, 2048 * 1026);
    const u32 rep = (bid & (NREP - 1)) * 2048;
    TX(16);
    const int k1 = xcd * 16 + (jj >> 1), kd1 = jj & 1, cc = kd1 + 2 * (w & 1);
    const bool sp0 = (k1 == 0 && kd1 == 0);
    bf16x8 m0[2], mi[2], m0x, mix[2], fa_r, fa_i, fb_r, fb_i, gb_r, gb_i, ga_r, ga_i; f32x4 t1c, t1s, t2c, t2s;
    u32 vW[5], wa[5], wo[5]; float fr[4][4], fi[4][4], er[4], ei[4];
    const float fl = (float)l15, fg = (float)(4 * g), fc = (float)cc;
    const u32 tw = L + L_SEG + w * 2048 + l15 * 32 + 8 * g, tr = L + L_SEG + w * 2048 + (4 * g + q) * 32 + 8 * pq;
    SB();
    {
      const float st = fmaf(fl, -4.f / 64.f, fc * (-1.f / 64.f));
#pragma unroll
      for (int e = 0; e < 4; ++e) { const float a = (fg + (float)e) * st; t2c[e] = cosr(a); t2s[e] = sinr(a); t1c[e] = cosr(a * (1.f / 16.f)); t1s[e] = sinr(a * (1.f / 16.f)); }
      fa_r = pk8(t2c, -t2s); fa_i = pk8(t2s, t2c); PIN(t1c); PIN(t1s);
      tr2(ga_r, ga_i, fa_r, fa_i, tw, tr);
      const float fm = (float)(l15 < 8 ? l15 : l15 ^ 3) * (-1.f / 16.f); const u32 sl = l15 < 8 ? 0u : 0x80008000u;
#pragma unroll
      for (int e = 0; e < 4; ++e) { const float a = (fg + (float)e) * fm; t2c[e] = cosr(a); t2s[e] = sinr(a); }
      fb_r = pk8(t2c, -t2s); fb_i = asf(__builtin_bit_cast(u32x4, pk8(t2s, t2c)) ^ sl);
      tr2(gb_r, gb_i, fb_r, fb_i, tw, tr);
    }
    u32 xa[2], ua, us, us0, fo, ap0, wp0, tr_, tw_;
    const u32 vo = L + L_V + w * L_CH;
    int wid, wsl; float ma[2], mk[2], mc, sft; u32 mv[4][4];
    auto fill = [&](auto k_) {
      constexpr int k = decltype(k_)::v;
      SB();
      if constexpr (k == -4) { mk[0] = (float)(k1 + 256 * (l15 >> 2)) * (-1.f / 2048.f); mk[1] = mk[0] - 0.5f; mc = -0.25f * (float)(kd1 + (l15 & 2)); sft = (float)k1 * (1.f / 512.f); PIN(mk[0]); PIN(mk[1]); PIN(mc); PIN(sft); }
      if constexpr (k == -3) { const float t = fmaf((float)(l15 & 1), -0.25f, -0.125f); ma[0] = fmaf(mk[0], 0.5f * fg, t); ma[1] = fmaf(mk[1], 0.5f * fg, t); PIN(ma[0]); PIN(ma[1]); }
      if constexpr (k == -2 || k == -1) { constexpr int i = k + 2; const int n = i ? 63 - kap : kap, rho = 64 * (n & 3) + (n >> 2); xa[i] = L + L_RA + rho * 128 + 16 * (w ^ (((rho >> 1) & 1) | ((rho >> 3) & 1) << 1)) + 2 * (ecol & 7); PIN(xa[i]); }
      if constexpr (k >= 0 && k < 8) {
        constexpr int n = k >> 2, e = k & 3;
        const float a = ma[n] + ((e & 1) ? mk[n] : 0.f) + ((e & 2) ? mc : 0.f);
        mv[n][e] = pk2(cosr(a), -sinr(a)); mv[2 + n][e] = pk2(cosr(a + sft), sinr(a + sft)); PIN(mv[n][e]); PIN(mv[2 + n][e]);
      }
      if constexpr (k >= 8 && k < 12) { const float a = fl * fmaf(fg + (float)(k & 3), 4.f / 1024.f, fc * (1.f / 1024.f)); t2c[k & 3] = cosr(a) * (1.f / 1048576.f); t2s[k & 3] = sinr(a) * (1.f / 1048576.f); PIN(t2c); PIN(t2s); }
      if constexpr (k == 12) { ua = L + L_V + (16 * w + g) * 256 + (lr8 & 1) * 128 + 16 * (lc8 ^ g); PIN(ua); }
      if constexpr (k == 13) { us = (u32)(64 * w + 2 * g + (lr8 & 1) + 32 * (sh ^ (w & 1))) * 16384u + lc8 * 16; PIN(us); }
      if constexpr (k == 14) { us0 = (w & 1) && g == 0 ? us ^ (32 * 16384) : us; PIN(us0); }
      if constexpr (k == 32) { fo = lane < NREP ? lane * 2048 + bid * 8 : WS_END; PIN(fo); }
      if constexpr (k == 33) { ap0 = vo + (4 * g + q) * 64 + 16 * pq; PIN(ap0); }
      if constexpr (k == 34) { wp0 = vo + l15 * L_PS + 16 * g; PIN(wp0); }
      if constexpr (k == 35) { tr_ = L + L_V + w * 2 * L_PS + g * L_CH + q * 32 + 8 * pq; PIN(tr_); }
      if constexpr (k == 36) { tw_ = L + L_V + w * 2 * L_PS + q * L_CH + pq * 32 + 8 * g; PIN(tw_); }
      if constexpr (k == 37) { PIN(vC[0]); PIN(vC[1]); }
      if constexpr (k == 40) { wid = 2 * cc + 8 * l15 + 512 * g; PIN(wid); }
      if constexpr (k == 41) { wa[0] = g < 2 ? L + L_W + (w >> 1) * 1056 + ((k1 * 1026) & 15) + wid : L + L_W + (8 + (w >> 1)) * 1056 + ((-k1 * 1026) & 15) + 1664 - wid; PIN(wa[0]); }
      if constexpr (k == 42) { wo[0] = g < 2 ? L + L_W + (8 + (w >> 1)) * 1056 + ((-k1 * 1026) & 15) : L + L_W + (w >> 1) * 1056 + ((k1 * 1026) & 15) + 1024; PIN(wo[0]); }
      if constexpr (k >= 43 && k < 46) { wa[k - 42] = wa[0] + 2112 * (k - 42); wo[k - 42] = wo[0] + 2112 * (k - 42); PIN(wa[k - 42]); PIN(wo[k - 42]); }
      if constexpr (k == 46) {
        if (__builtin_expect(k1 == 0, 0)) {
#pragma unroll
          for (int i = 0; i < 5; ++i) {
            const int kk = tile_desc(w + 4 * i, 0, kd1).k;
            const u32 wp = L + L_W + (kk >> 7) * 1056, wq = L + L_W + (((2048 - kk) & 2047) >> 7) * 1056;
            wa[i] = g < 2 ? wp + wid : wq + 1664 - wid; wo[i] = g < 2 ? wq : wp + 1024;
          }
        }
      }
      if constexpr (k >= 48 && k < 73) {
        constexpr int i = (k - 48) / 5, ph = (k - 48) % 5;
        if (ph == 0) { wid = 64 * (w + 4 * i) + lane; wsl = (wid * 993) >> 16; PIN(wid); PIN(wsl); }
        if (ph == 1) { wsl = min(wsl, 15); wid = 16 * wid - 1056 * wsl; PIN(wid); PIN(wsl); }
        if (ph == 2) { wsl = k1 + (k1 ? 256 : 128) * wsl; PIN(wsl); }
        if (ph == 3) { wsl = wsl < 2048 ? wsl : 4096 - wsl; PIN(wsl); }
        if (ph == 4) { vW[i] = ((wsl * 1026) & ~15) + wid; PIN(vW[i]); }
      }
      if constexpr (k >= 76 && k < 96) {
#pragma unroll
        for (int r = 2 * (k & 1); r < 2 * (k & 1) + 2; ++r) { constexpr int z_ = (k - 76) >> 1; float& z = z_ < 4 ? fr[z_ & 3][r] : z_ < 8 ? fi[z_ & 3][r] : z_ == 8 ? er[r] : ei[r]; z = 0.f; PIN(z); }
      }
      SB();
    };
    auto issue_t = [&](auto j_) {
      constexpr int j = decltype(j_)::v;
      sfor<0, 8>([&](auto i_) { constexpr int i = decltype(i_)::v; if (i < 4) issue_b1(j, i); else issue_x1(j, i - 4); if constexpr (j > 2) fill(IC<8 * j - 8 + i>{}); });
    };
    sfor<-4, 15>(fill);
#pragma unroll
    for (int n = 0; n < 2; ++n) { m0[n] = asf(u32x4{mv[n][0], mv[n][1], mv[n][2], mv[n][3]}); mi[n] = asf(u32x4{mv[2 + n][0], mv[2 + n][1], mv[2 + n][2], mv[2 + n][3]}); }
    if (__builtin_expect(k1 == 0, 0)) { m0_fwd(m0[0], 0, l15, g, 0, kd1); m0_fwd(m0[1], 1, l15, g, 0, kd1); m0_fwd(m0x, 2, l15, g, 0, 0); tr2(mix[0], mix[1], m0x, bf16x8{}, tw, tr); mi[0] = m0[0]; mi[1] = m0[1]; }
    tr2(mi[0], mi[1], mi[0], mi[1], tw, tr);
    TX(17);
#pragma unroll
    for (int i = 0; i < 4; ++i) issue_x1(0, i);
    issue_t(IC<1>{}); issue_t(IC<2>{});
    gwait(L + L_SYNC, 1);
    u32 sk[8][2];
#pragma unroll
    for (int i = 0; i < 8; ++i) { const u32 a = xa[i >> 2] + 2048 * (i < 4 ? i : (i + 3) & 3); sk[i][0] = lr<u32>(a); sk[i][1] = lr<u32>(a + 64); }
    WAITV(16);
    PIN(tag); PIN(q_ds[0]); PIN(q_ds[1]);
    PIN(fa_r); PIN(fa_i); PIN(fb_r); PIN(fb_i); PIN(ga_r); PIN(ga_i); PIN(gb_r); PIN(gb_i); PIN(m0[0]); PIN(m0[1]); PIN(mi[0]); PIN(mi[1]);
    tag += 0x9e3779b97f4a7c15ull;
    if (w == 0) lw<u64>(L + L_SYNC + 32, tag);
    TX(18);
    u32x4 uq[4];
    const f32x4 z4 = {};
    bf16x8 a0[2][2];
    sfor<0, 14>([&](auto j_) {
      constexpr int j = decltype(j_)::v;
      WAITV(g1_wait(j));
      if (j == 0) LBAR(); else BAR();
      ITS(1, j);
      PINS(rot1);
      if (j == 0) {
#pragma unroll
        for (int i = 0; i < 4; ++i) uq[i] = lr<u32x4>((ua ^ (64 * (i & 1))) + 1024 * i);
        if (w == 3) st4c(u32x4{SENT, SENT, SENT, SENT}, wsr, lane < 32 ? vl : WS_END, WS_AGG + tt * 8192 + pt * 512);
      }
      if (j == JF && w == 0) st2c(tag, wsr, fo, WS_FU);
      if (j == 9 && w == 1) { bldsc(wsr, vl, WS_FU + rep, L, L_SEG + 4096); bldsc(wsr, vl, WS_FU + rep + 1024, L, L_SEG + 5120); }
      if (j == 12) { uq[0] = lr<u32x4>(L + L_SEG + 4096 + vl); uq[1] = lr<u32x4>(L + L_SEG + 5120 + vl); }
      SB();
      if constexpr (j < 13) issue_t(IC<j + 3>{});
      SB();
      if constexpr (j <= 1) { st4c(uq[2 * j], wsr, j == 0 ? us0 : us, WS_U + ct * 1024 + s2 * 128 + 2 * j * 8 * 16384); st4c(uq[2 * j + 1], wsr, us, WS_U + ct * 1024 + s2 * 128 + (2 * j + 1) * 8 * 16384); }
      if (j == 12 && __builtin_expect(!tagged(uq[0], uq[1], tag), 0)) pollflags(wsr, WS_FU + rep, lane, tag);
      SB();
    });
#pragma unroll
    for (int i = 0; i < 4; ++i) bldsc(wsr, vl, WS_U + (u32)(2 * k1 + kd1) * 16384u + w * 2048 + (i >> 1) * 8192 + (i & 1) * 1024, L + w * L_CH, L_V + i * 1024);
#pragma unroll
    for (int i = 0; i < NW13; ++i) blds(w0, vW[i], 0, L + w * 1024, L_W + i * 4096);
    WAITV(4 + NW13); BAR();
    ITS(1, 14);
    auto wissue = [&]() {
#pragma unroll
      for (int i = 0; i < 4; ++i) { if (i >= NW13) blds(w0, vW[i], 0, L + w * 1024, L_W + i * 4096); blds(w1, vW[i], 0, L + w * 1024, L_W + L_WS + i * 4096); }
      if (w == 0) { blds(w0, vW[4], 0, L, L_W + 16384); blds(w1, vW[4], 0, L, L_W + L_WS + 16384); }
#pragma unroll
      for (int e = 0; e < 4 * NC0; ++e) c_piece(e >> 2, e & 3);
    };
    {
      WAITV(NW13);
#pragma unroll
      for (int cb = 0; cb < 2; ++cb) { a0[cb][0] = cat(trr(ap0 + 1024 * cb), trr(ap0 + 1024 * cb + 2048)); a0[cb][1] = cat(trr(ap0 + 1024 * cb + 8), trr(ap0 + 1024 * cb + 2056)); }
      FENCE();
      if (WPOS == 0) wissue();
      f32x4 d[2][2][2];
#pragma unroll
      for (int i = 0; i < 8; ++i) d[i >> 2][(i >> 1) & 1][i & 1] = mma(a0[(i >> 1) & 1][i & 1], m0[i >> 2], z4);
#pragma unroll
      for (int i = 0; i < 4; ++i) lw<bf16x8>(wp0 + 16 * (i >> 1) * L_PS + 64 * (i & 1), pk8(d[i >> 1][i & 1][0], d[i >> 1][i & 1][1]));
      if (__builtin_expect(sp0, 0)) {
#pragma unroll
        for (int cb = 0; cb < 2; ++cb) { const bf16x8 o = pk8(mma(a0[cb][0], m0x, z4), mma(a0[cb][1], m0x, z4)); if (l15 < 4) lw<bf16x8>(wp0 + 32 * L_PS + 64 * cb, o); }
      }
      gbar(L + L_SYNC + 16, w, 2);
    }
    TX(8);
    auto tiles = [&](auto nt_, auto i0_) {
      constexpr int NT = decltype(nt_)::v, i0 = decltype(i0_)::v;
      const u32 pr = tr_ + i0 * 8 * L_PS, pw_ = tw_ + i0 * 8 * L_PS;
      bf16x8 a[NT]; f32x4 yr[NT], yi[NT];
#pragma unroll
      for (int i = 0; i < NT; ++i) a[i] = cat(trr(pr + i * 8 * L_PS), trr(pr + i * 8 * L_PS + L_PS));
      SB();
      if (!i0 && WPOS == 1) wissue();
#pragma unroll
      for (int i = 0; i < NT; ++i) { yr[i] = mma(a[i], fa_r, z4); yi[i] = mma(a[i], fa_i, z4); }
      SB();
#pragma unroll
      for (int i = 0; i < NT; ++i) { a[i] = pk8(yr[i] * t1c - yi[i] * t1s, yr[i] * t1s + yi[i] * t1c); yr[i] = mma(fb_r, a[i], z4); yi[i] = mma(fb_i, a[i], z4); SB(); }
      if (!i0) { WAITV(4 * NC0); gbar(L + L_SYNC + 16, w, 3); }
      TX(9);
#pragma unroll
      for (int i = 0; i < NT; ++i) {
        if (i0) {
#pragma unroll
          for (int r = 0; r < 4; ++r) fr[i][r] = fi[i][r] = er[i] = ei[i] = 0.f;
        }
        ldhi<0>(fr[i][0], wa[i0 + i]); ldhi<128>(fr[i][1], wa[i0 + i]); ldhi<256>(fr[i][2], wa[i0 + i]); ldhi<384>(fr[i][3], wa[i0 + i]);
        ldhi<L_WS>(fi[i][0], wa[i0 + i]); ldhi<L_WS + 128>(fi[i][1], wa[i0 + i]); ldhi<L_WS + 256>(fi[i][2], wa[i0 + i]); ldhi<L_WS + 384>(fi[i][3], wa[i0 + i]);
        ldhi<0>(er[i], wo[i0 + i]); ldhi<L_WS>(ei[i], wo[i0 + i]);
      }
      WAITL(0);
#pragma unroll
      for (int i = 0; i < NT; ++i)
#pragma unroll
        for (int r = 0; r < 4; ++r) { PIN(fr[i][r]); PIN(fi[i][r]); PIN(er[i]); PIN(ei[i]); }
      if (cc == 0) {
        const float b0 = lane == 0 ? 0.5f : 0.f, b3 = lane == 32 ? 0.5f : 0.f;
#pragma unroll
        for (int i = 0; i < NT; ++i) {
          float t;
          asm volatile("v_sub_f32 %4, %5, %0\n\tv_fmac_f32 %0, %7, %4\n\tv_add_f32 %4, %6, %1\n\tv_fma_f32 %1, -%7, %4, %1\n\t"
                       "v_sub_f32 %4, %5, %2\n\tv_fmac_f32 %2, %8, %4\n\tv_add_f32 %4, %6, %3\n\tv_fma_f32 %3, -%8, %4, %3"
                       : "+v"(fr[i][0]), "+v"(fi[i][0]), "+v"(fr[i][3]), "+v"(fi[i][3]), "=&v"(t) : "v"(er[i]), "v"(ei[i]), "v"(b0), "v"(b3));
        }
      }
      if (__builtin_expect(k1 == 0, 0)) {
#pragma unroll
        for (int i = 0; i < NT; ++i) { const float wt_ = (float)tile_desc(w + 4 * (i0 + i), 0, kd1).w; yr[i] *= wt_; yi[i] *= wt_; }
      }
      SB();
#pragma unroll
      for (int i = 0; i < NT; ++i) {
        const f32x4 vr = {fr[i][0], fr[i][1], fr[i][2], fr[i][3]}, vi = {fi[i][0], fi[i][1], fi[i][2], fi[i][3]};
        a[i] = pk8(yr[i] * vr - yi[i] * vi, yr[i] * vi + yi[i] * vr); yr[i] = mma(a[i], gb_r, z4); yi[i] = mma(a[i], gb_i, z4);
        SB();
      }
#pragma unroll
      for (int i = 0; i < NT; ++i) {
        a[i] = pk8(yr[i] * t2c - yi[i] * t2s, yr[i] * t2s + yi[i] * t2c); yr[i] = mma(a[i], ga_r, z4); yi[i] = mma(a[i], ga_i, z4);
        SB();
      }
#pragma unroll
      for (int i = 0; i < NT; ++i) { lw<u32x2>(pw_ + i * 8 * L_PS, u32x2{pk2(yr[i].xy), pk2(yr[i].zw)}); lw<u32x2>(pw_ + i * 8 * L_PS + L_PS, u32x2{pk2(yi[i].xy), pk2(yi[i].zw)}); }
    };
    tiles(IC<4>{}, IC<0>{});
    if (__builtin_expect(sp0 && w < 2, 0)) tiles(IC<1>{}, IC<4>{});
    TX(23);
    gbar(L + L_SYNC + 16, w, 4);
    TX(24);
    u32x4 tq[4];
    {
      f32x4 o[2][2][2];
      const u32 ap = vo + (4 * g + q) * L_PS + 16 * pq;
#pragma unroll
      for (int cb = 0; cb < 2; ++cb) { a0[cb][0] = cat(trr(ap + 64 * cb), trr(ap + 64 * cb + 16 * L_PS)); a0[cb][1] = cat(trr(ap + 64 * cb + 8), trr(ap + 64 * cb + 16 * L_PS + 8)); }
#pragma unroll
      for (int i = 0; i < 8; ++i) o[i >> 2][(i >> 1) & 1][i & 1] = mma(a0[(i >> 1) & 1][i & 1], mi[i >> 2], z4);
      if (__builtin_expect(sp0, 0)) {
        const u32 ax = vo + (32 + q) * L_PS + 16 * pq;
#pragma unroll
        for (int i = 0; i < 4; ++i) { const u32x2 t = trr(ax + 64 * (i >> 1) + 8 * (i & 1)); const bf16x8 a = cat(t, t); o[0][i >> 1][i & 1] = mma(a, mix[0], o[0][i >> 1][i & 1]); o[1][i >> 1][i & 1] = mma(a, mix[1], o[1][i >> 1][i & 1]); }
      }
      FENCE();
#pragma unroll
      for (int i = 0; i < 4; ++i) lw<bf16x8>(vo + i * 1280 + l15 * 80 + 16 * g, pk8(o[i >> 1][i & 1][0], o[i >> 1][i & 1][1]));
      FENCE();
#pragma unroll
      for (int i = 0; i < 4; ++i) { tq[i] = lr<u32x4>(vo + i * 1280 + (lane >> 2) * 80 + pq * 16); PIN(tq[i]); }
    }
    TX(10);
    u32x4 tab[2][4];
    if (__builtin_expect(bid == 0 && w == 0, 0)) st2c(tag, wsr, lane == 0 ? WS_E : WS_END, 0);
#pragma unroll
    for (int i = 0; i < 16; ++i) { const u32 xv = sk[i >> 1][i & 1], dv = q_ds[i & 1]; sk[i >> 1][i & 1] = pk2(bf2f(xv & 0xffffu) * bf2f(dv & 0xffffu), bfhi(xv) * bfhi(dv)); PIN(sk[i >> 1][i & 1]); }
    TX(11);
    WAITV(0); BAR();
    if (w == 0) st2c(tag, wsr, fo, WS_FH);
    TX(25);
    {
      const u32 to = (u32)lr8 * (16 * 32768) + (u32)(2 * k1 + kd1) * 128 + lc8 * 16;
#pragma unroll
      for (int i = 0; i < 4; ++i) st4c(tq[i], wsr, to, WS_T + (8 * (i >> 1) + 2 * w + (i & 1)) * 32768);
    }
#pragma unroll
    for (int e = 0; e < 12; ++e) c_piece(4 + (e >> 2), e & 3);
    TX(26);
    pollq(wsr, WS_FH + rep + w * 512, lane, tag);
    BAR();
    TX(27);
    const u32 hb = WS_H + (u32)(tt * 8 + 2 * w) * 65536u + (ct & 1) * 32768;
    auto h_issue = [&](int u) {
      const u32 so = hb + ((u + rot2) & 15) * 2048;
#pragma unroll
      for (int m = 0; m < 4; ++m) bldsc(wsr, vl, so + (m >> 1) * 65536 + (m & 1) * 1024, L + w * 4096, 114688 + (u % 3) * 16384 + m * 1024);
    };
    h_issue(0); h_issue(1);
    u64 ft;
    u32x4 tv[8], gv[2]; u32 ta[8]; float tk, tc;
    f32x2 o[8][2];
    sfor<0, 16>([&](auto u_) {
      constexpr int u = decltype(u_)::v;
      WAITV(g2_wait(u));
      BAR();
      ITS(2, u);
      PINS(rot2);
      if (u == 0 && w == 0) st2c(tag, wsr, fo, WS_FT);
      if (u + 2 < 16) h_issue(u + 2);
#pragma unroll
      for (int e = g2_c0(u); e < g2_c0(u + 1); ++e) c_piece(7 + (e >> 2), e & 3);
      if constexpr (u >= TU && u < TU + 4) {
#pragma unroll
        for (int i = 2 * (u - TU); i < 2 * (u - TU) + 2; ++i) tv[i] = bl4c<0>(wsr, vl, WS_T + (s2 * 16 + ct) * 32768 + i * 4096 + w * 1024);
        if (u < TU + 2) gq[(u - TU) & 1] = bl1c(wsr, vGo + 128 * (u - TU), 0); else if (u == TU + 2) gv[0] = bl4c<0>(wsr, WS_G + tt * 512 + l15 * 16, 0); else gv[1] = bl4c<256>(wsr, WS_G + tt * 512 + (15 - l15) * 16, 0);
      }
      if (u == TU - 4) asm volatile("buffer_load_dwordx2 %0, %1, %2, %3 offen sc1" : "=v"(ft) : "v"(lane * 8), "s"(wsr), "s"(WS_FT + rep + w * 512) : "memory");
      if (u == TU - 1) { PIN(ft); if (__builtin_expect(!__all(ft == tag), 0)) pollq(wsr, WS_FT + rep + w * 512, lane, tag); }
      SB();
      if constexpr (u < 8) {
        if (u == 0) { tk = (float)(2 * kap + 1) * (1.f / 512.f); tc = 2.f * cosr(4.f * tk); PIN(tk); PIN(tc); }
        const float kk = (u & 4) ? -tk : tk;
        tab[u >> 2][u & 3] = cos8(fmaf(kk, 8.f * fg + (float)(u & 3), -0.125f), kk, tc);
        PIN(tab[u >> 2][u & 3]);
      }
      if constexpr (u == 8 || u == 9 || u == 13 || u == 14) {
#pragma unroll
        for (int i = (u < 10 ? 2 * u - 16 : 2 * u - 22); i < (u < 10 ? 2 * u - 14 : 2 * u - 20); ++i) {
          const int kb = 16 * i + 4 * w + g, k = (lane & 4) ? (kb ? 256 - kb : 128) : kb, rho = 64 * (k & 3) + (k >> 2);
          ta[i] = L + 49152 + rho * 128 + 16 * ((((lane >> 1) & 4) | pq) ^ (((rho >> 1) & 1) | ((rho >> 3) & 1) << 1)); PIN(ta[i]);
        }
      }
      SB();
    });
    WAITV(0);
#pragma unroll
    for (int i = 0; i < 8; ++i) { PIN(tv[i]); lw<u32x4>(ta[i], tv[i]); }
    LBAR();
    hart256<true>(o, L + 49152 + (8 * g + q) * 128 + 16 * ((4 * (pq >> 1) + w) ^ (((q >> 1) & 1) | ((g & 1) << 1))) + 8 * (pq & 1), tab, kap, false);
    PIN(gq[0]); PIN(gq[1]); PIN(gv[0]); PIN(gv[1]);
#pragma unroll
    for (int i = 0; i < 8; ++i) {
      const float gg = __uint_as_float(gv[i >> 2][i < 4 ? i : (i + 3) & 3]), og = rbf(1.f - gg);
      const int pos = i < 4 ? l15 : 15 - l15;
#pragma unroll
      for (int hf = 0; hf < 2; ++hf)
        lw<f32x2>(L + 81920 + pos * 256 + 16 * ((8 * hf + 2 * w + (g & 1)) ^ pos) + 8 * (g >> 1) + 4096 * (i < 4 ? 2 * i : 2 * ((i + 3) & 3) + 1), fma2(f32x2{og, og}, f32x2{bf2f(sk[i][hf] & 0xffffu), bfhi(sk[i][hf])}, gg * o[i][hf]));
    }
    TX(30);
  } else {
    const int wn = w & 1, wm = w >> 1;
    const int hpos = 8 * wm + lc8, hsh = (tt ^ (tt >> 1)) & 1;
    u32 vH = (u32)(lr8 & 1) * (1u << 20) + hsh * (1u << 19) + (tt >> 1) * 65536 + (2 * pt + wn) * 1024 + g * 256 + hpos * 16;
    const u32 vAg = WS_AGG + (u32)((pt * 2 + wn) * 16 + l15) * 16;
    const int kap = 16 * sh + l15, np = (16 - l15) & 15; const bool sp = kap == 0;
    const int ecol = 8 * w + 4 * (g & 1) + 2 * (g >> 1);
    TS(19);
    u32x4 tab[2][4];
    hart_table(tab, kap, sp, g);
#pragma unroll
    for (int i = 0; i < 8; ++i) PIN(tab[i >> 2][i & 3]);
    u32 ua[6], hap = L + L_RA + (8 * g + q) * 128 + 16 * ((4 * (pq >> 1) + w) ^ (((q >> 1) & 1) | ((g & 1) << 1))) + 8 * (pq & 1);
#pragma unroll
    for (int i = 0; i < 2; ++i) {
      const int lo = i ? np : l15;
      ua[i] = L + L_V + lo * 256 + ((2 * ecol) ^ ((lo & 7) << 4)); PIN(ua[i]);
    }
    ua[2] = (ua[1] ^ 64) + (sp ? 0 : 48 * 256); ua[3] = (ua[1] ^ 64) + (sp ? 32 * 256 : 16 * 256); ua[4] = (ua[0] ^ 64) + (sp ? 16 * 256 : 0);
    PIN(ua[2]); PIN(ua[3]); PIN(ua[4]); PIN(hap);
    TS(22);
#pragma unroll
    for (int i = 2; i < 8; ++i) issue_a(i >> 1, i & 1);
    SB();
    c2 lam, coef, l8, l16;
    WAITV(8);
    {
      const u32 qa = L + L_V + 16384 + w * 1024 + lane * 4, q_dt = lr<u16>(qa), q_lr = lr<u16>(qa + 256), q_li = lr<u16>(qa + 512);
      const float lre = bf2f(q_lr), lim = bf2f(q_li);
      const float dt = rbf(expf(bf2f(q_dt))), mg = expf(dt * lre);
      float sn, cs; sincosf(dt * lim, &sn, &cs);
      const float nr = mg * cs - 1.f, ni = mg * sn;
      const bool big = fabsf(lre) >= fabsf(lim);
      const float u = big ? lre : lim, v = big ? lim : lre, t = v * __builtin_amdgcn_rcpf(u), rd = 2.f * __builtin_amdgcn_rcpf(u + v * t);
      lam = {spread16(mg * cs), spread16(ni)};
      coef = {spread16(rd * (big ? nr + ni * t : nr * t + ni)), spread16(rd * (big ? ni - nr * t : ni * t - nr))};
      l8 = csq(csq(csq(lam))); l16 = csq(l8);
      pinc(lam); pinc(coef); pinc(l8); pinc(l16);
    }
      u32 aa[2][2], ab[2][2];
#pragma unroll
      for (int i = 0; i < 4; ++i) {
        const int co = 16 * ((4 * (i & 1) + g) ^ (l15 >> 1)) + (i >> 1) * 65536;
        aa[i & 1][i >> 1] = L + (64 * wm + l15) * 128 + co; ab[i & 1][i >> 1] = L + 16384 + (64 * wn + l15) * 128 + co; PIN(aa[i & 1][i >> 1]); PIN(ab[i & 1][i >> 1]);
      }
    WAITV(0);
    gbar(L + L_SYNC, w, 1);
    TS(1);
    {
      f32x2 o[8][2];
      hart256<false>(o, hap, tab, kap, sp);
#pragma unroll
      for (int i = 0; i < 8; ++i) {
        const u32 a = i == 0 ? ua[0] : i == 1 ? ua[0] + 32 * 256 : i == 2 ? ua[2] : i == 3 ? ua[3] : i == 4 ? ua[4] : i == 5 ? ua[1] + 16 * 256 : i == 6 ? ua[1] + 48 * 256 : ua[4] + 32 * 256;
        lw<u32>(a, pk2(o[i][0])); lw<u32>(a + 128, pk2(o[i][1]));
      }
    }
    SB();
    c2 pw[8], wt[4], lsa, lsb, l32 = csq(l16), l64 = csq(l32);
    u32 wa[8], ra[8], pa[4], vAs = lane < 16 ? vAg : WS_END, sa = L + L_SEG + wn * 256 + l15 * 16;
    {
      const c2 one = {{1.f, 1.f}, {0.f, 0.f}}, zero = {{0.f, 0.f}, {0.f, 0.f}};
      const c2 l128 = csq(l64), l256 = csq(l128), l512 = csq(l256);
      lsa = cmul(cmul(coef, csel(g & 1, l16, one)), cmul(csel(g & 2, l32, one), csel(wm, l64, one))); lsb = cmul(lsa, l8);
      wt[0] = cmul(csel(g & 1, l128, one), csel(g & 2, l256, one));
#pragma unroll
      for (int n = 1; n < 4; ++n) wt[n] = cmul(wt[n - 1], l512);
#pragma unroll
      for (int n = 0; n < 4; ++n) { wt[n] = csel(tt - 1 - g - 4 * n >= 0, wt[n], zero); pinc(wt[n]); pa[n] = vAg + max(tt - 1 - g - 4 * n, 0) * 8192; PIN(pa[n]); }
      pw[0] = lam; pw[1] = csq(lam); pw[2] = cmul(pw[1], lam); pw[3] = csq(pw[1]); pw[4] = cmul(pw[3], lam); pw[5] = csq(pw[2]); pw[6] = cmul(pw[3], pw[2]); pw[7] = l8;
#pragma unroll
      for (int s = 1; s < 7; ++s) pinc(pw[s]);
      pinc(lsa); pinc(lsb); pinc(l32); pinc(l64); PIN(vH); PIN(vAs); PIN(sa);
    }
    u64 tag;
    f32x4 acc[4][4];
    {
      bf16x8 fa[2][4], fb[2][4];
      auto rd1 = [&](int j, int ks, int i) {
        const int sl = (j + 3) & 3; const u32 bs = (sl & 1) * 32768 + (i >> 1) * 2048;
        if (i & 1) fb[ks][i >> 1] = lr<bf16x8>(ab[ks][sl >> 1] + bs); else fa[ks][i >> 1] = lr<bf16x8>(aa[ks][sl >> 1] + bs);
      };
      LBAR();
      TS(2);
      u64 cy0 = 0; CY0();
#pragma unroll
      for (int i = 0; i < 8; ++i) rd1(0, 0, i);
#pragma unroll
      for (int j = 0; j < 16; ++j) {
#pragma unroll
        for (int ks = 0; ks < 2; ++ks) {
          const bool nx = ks == 0 || j < 15;
          if (ks == 1 && j < 14) LBAR();
          SB();
#pragma unroll
          for (int qq = 0; qq < 16; ++qq) {
            const int m = qq >> 2, n = (m & 1) ? 3 - (qq & 3) : (qq & 3);
            if (j | ks) mmc(acc[m][n], fa[ks][m], fb[ks][n]); else mmz(acc[m][n], fa[ks][m], fb[ks][n]);
            SB();
            if (nx && qq < 8) rd1(j + ks, ks ^ 1, qq);
            SB();
          }
        }
      }
      NOPS();
      CY1(20);
    }
    TS(3);
    c2 h[2][8];
#pragma unroll
    for (int s = 0; s < 8; ++s) {
#pragma unroll
      for (int ps = 0; ps < 2; ++ps) {
        const f32x4 vr = acc[s >> 1][2 * ps], vi = acc[s >> 1][2 * ps + 1];
        h[ps][s] = (s & 1) ? c2{vr.zw, vi.zw} : c2{vr.xy, vi.xy};
        if (s) { const c2 a = bc(lam, ps); h[ps][s].r = fma2(-a.i, h[ps][s - 1].i, h[ps][s].r); h[ps][s].i = fma2(a.i, h[ps][s - 1].r, h[ps][s].i); }
      }
      SB();
#pragma unroll
      for (int ps = 0; ps < 2; ++ps) if (s) { const c2 a = bc(lam, ps); h[ps][s].r = fma2(a.r, h[ps][s - 1].r, h[ps][s].r); h[ps][s].i = fma2(a.r, h[ps][s - 1].i, h[ps][s].i); }
      SB();
    }
    const c2 ea = {{h[0][7].r.x, h[1][7].r.x}, {h[0][7].i.x, h[1][7].i.x}};
    { const c2 e = cfma(l8, ea, c2{{h[0][7].r.y, h[1][7].r.y}, {h[0][7].i.y, h[1][7].i.y}});
      lw<f32x4>(sa + (4 * wm + g) * 512, f32x4{e.r.x, e.r.y, e.i.x, e.i.y}); }
    gbar(L + L_SYNC, w, 2);
    u32x4 gx[4], gw[2];
    c2 cin;
    {
      c2 e[8];
#pragma unroll
      for (int j = 0; j < 8; ++j) e[j] = asc(lr<f32x4>(sa + j * 512));
      SB();
      const c2 e01 = cfma(l16, e[0], e[1]), e23 = cfma(l16, e[2], e[3]), e45 = cfma(l16, e[4], e[5]), e67 = cfma(l16, e[6], e[7]);
      const c2 e03 = cfma(l32, e01, e23), e47 = cfma(l32, e45, e67), tot = cfma(l64, e03, e47);
      st4c(__builtin_bit_cast(u32x4, f32x4{tot.r.x, tot.r.y, tot.i.x, tot.i.y}), wsr, vAs, wm ? tt * 8192 : WS_END);
      SB();
      const c2 zero = {{0.f, 0.f}, {0.f, 0.f}};
      const c2 c0 = csel(wm, e03, zero), c2_ = cfma(l32, c0, csel(wm, e45, e01)), cb = csel(g & 2, c2_, c0);
      cin = csel(g & 1, cfma(l16, cb, csel(wm, csel(g & 2, e[6], e[4]), csel(g & 2, e[2], e[0]))), cb);
    }
    pinc(cin);
    TS(4);
    SB();
      { const u32 xo = (bid * 8 + 2 * w) * 2048, vl = lane * 16; const rsrc_t gr = mkr(gate_w, 2048);
        gx[0] = bl4<0>(xr, vl, xo); gx[1] = bl4<1024>(xr, vl, xo); gx[2] = bl4<2048>(xr, vl, xo); gx[3] = bl4<3072>(xr, vl, xo); gw[0] = bl4<0>(gr, vl, 0); gw[1] = bl4<1024>(gr, vl, 0); }
    SB();
    c2 ca, cz; u32x4 v[4];
    {
#pragma unroll
      for (int s = 0; s < 8; ++s) {
        f32x2 t[4];
#pragma unroll
        for (int ps = 0; ps < 2; ++ps) { const c2 a = bc(coef, ps); t[2 * ps] = a.r * h[ps][s].r; t[2 * ps + 1] = a.i * h[ps][s].r; }
        SB();
#pragma unroll
        for (int ps = 0; ps < 2; ++ps) { const c2 a = bc(coef, ps); h[ps][s].r = fma2(-a.i, h[ps][s].i, t[2 * ps]); h[ps][s].i = fma2(-a.r, h[ps][s].i, -t[2 * ps + 1]); }
        SB();
      }
#pragma unroll
      for (int s = 0; s < 16; ++s) pinc(h[s & 1][s >> 1]);
#pragma unroll
      for (int n = 0; n < 4; ++n) v[n] = bl4c<0>(wsr, pa[n], 0);
      SB();
      ca = cmul(coef, cin); cz = cmul(coef, cfma(l8, cin, ea));
      pinc(ca); pinc(cz);
      const u32 hs = L + 65536 + w * 8192;
#pragma unroll
      for (int i = 0; i < 8; ++i) {
        wa[i] = hs + l15 * 128 + ((4 * g + 16 * i) ^ (8 * (l15 & 1) + 4 * (l15 & 4) + 16 * (l15 & 2) + 8 * (l15 & 8))); PIN(wa[i]);
        ra[i] = hs + (8 * g + q) * 128 + ((8 * pq) ^ (8 * (q & 1) + 16 * (q & 2) + 64 * (g & 1)) ^ (16 * i)); PIN(ra[i]);
      }
    }
    {
      const u32 vG = WS_G + (u32)(((2 * w + (lane & 1)) * 2 + (((bid >> 4) ^ (bid >> 5)) & 1)) * 128 + ((bid >> 5) & 1) * 64 + (bid & 15) * 4 + (bid >> 6)) * 4;
      float sm[2] = {0.f, 0.f};
      WAITV(4); PIN(gw[0]); PIN(gw[1]);
#pragma unroll
      for (int i = 0; i < 4; ++i) {
        PIN(gx[i]);
#pragma unroll
        for (int e = 0; e < 4; ++e) { const u32 a = gx[i][e], b = gw[i & 1][e]; sm[i >> 1] = __builtin_amdgcn_fdot2_f32_bf16(__builtin_bit_cast(bf16x2, a), __builtin_bit_cast(bf16x2, b), sm[i >> 1], false); }
      }
      sm[0] = dppadd<0xB1>(sm[0]); sm[1] = dppadd<0xB1>(sm[1]);
      float t = (lane & 1) ? sm[1] : sm[0];
      t = dppadd<0x4E>(t); t = dppadd<0x124>(t); t = dppadd<0x128>(t); t = sum32(sum16(t));
      __builtin_amdgcn_raw_buffer_store_b32(__float_as_uint(rbf(1.f / (1.f + expf(-rbf(t))))), wsr, lane < 2 ? vG : WS_END, 0, 16);
    }
    SB();
    TS(5);
    WAITV(1);
    if (tt > 0) {
      auto vok = [&]() { u32 mx = 0;
#pragma unroll
        for (int n = 0; n < 4; ++n) { PIN(v[n]); mx = umax3(umax3(mx, v[n].x, v[n].y), v[n].z, v[n].w); }
        return __all(mx != SENT); };
      if (!vok()) {
        NOUNROLL for (u32 spn = 0; spn < SPIN_CAP; ++spn) {
#pragma unroll
          for (int n = 0; n < 4; ++n) v[n] = bl4c<0>(wsr, pa[n], 0);
          WAITV(0);
          if (vok()) break;
        }
      }
      c2 cb = cfma(wt[0], asc(__builtin_bit_cast(f32x4, v[0])), cmul(wt[1], asc(__builtin_bit_cast(f32x4, v[1]))));
      const c2 cd = cfma(wt[2], asc(__builtin_bit_cast(f32x4, v[2])), cmul(wt[3], asc(__builtin_bit_cast(f32x4, v[3]))));
      cb.r += cd.r; cb.i += cd.i;
      cb = c2{{sum32(sum16(cb.r.x)), sum32(sum16(cb.r.y))}, {sum32(sum16(cb.i.x)), sum32(sum16(cb.i.y))}};
      ca = cfma(lsa, cb, ca); cz = cfma(lsb, cb, cz);
    } else {
#pragma unroll
      for (int n = 0; n < 4; ++n) PIN(v[n]);
    }
    TS(6);
    {
      const c2 cc[2] = {{{ca.r.x, cz.r.x}, {ca.i.x, cz.i.x}}, {{ca.r.y, cz.r.y}, {ca.i.y, cz.i.y}}};
      u32x4 hq[8];
#pragma unroll
      for (int s = 0; s < 8; ++s) {
#pragma unroll
        for (int ps = 0; ps < 2; ++ps) { const c2 a = bc(pw[s], ps); h[ps][s].r = fma2(-a.i, cc[ps].i, h[ps][s].r); h[ps][s].i = fma2(-a.i, cc[ps].r, h[ps][s].i); }
        SB();
#pragma unroll
        for (int ps = 0; ps < 2; ++ps) { const c2 a = bc(pw[s], ps); h[ps][s].r = fma2(a.r, cc[ps].r, h[ps][s].r); h[ps][s].i = fma2(-a.r, cc[ps].i, h[ps][s].i); }
        SB();
#pragma unroll
        for (int ps = 0; ps < 2; ++ps) { lw<u32>(wa[s] + ps * 2048, pk2(h[ps][s].r)); lw<u32>(wa[s] + ps * 2048 + 4096, pk2(h[ps][s].i)); }
        FENCE();
        if (s & 1) {
          hq[s - 1] = __builtin_bit_cast(u32x4, cat(trr(ra[s - 1]), trr(ra[s] + 512))); hq[s] = __builtin_bit_cast(u32x4, cat(trr(ra[s - 1] + 4096), trr(ra[s] + 4608)));
          if (s > 1) { st4c(hq[s - 3], wsr, vH, WS_H + ((s - 3) >> 1) * (2u << 20)); st4c(hq[s - 2], wsr, vH, WS_H + ((s - 3) >> 1) * (2u << 20) + 32768); }
        }
        SB();
      }
      st4c(hq[6], wsr, vH, WS_H + 3 * (2u << 20)); st4c(hq[7], wsr, vH, WS_H + 3 * (2u << 20) + 32768);
    }
    TS(7);
    {
      bf16x8 fh[2][4], fc[2][4], cp[6][4];
      u32 ha = L + 114688 + wm * 8192 + lane * 16, ca[2][2];
#pragma unroll
      for (int i = 0; i < 4; ++i) { ca[i & 1][i >> 1] = L + (i >> 1) * 65536 + wn * 8192 + l15 * 128 + 16 * ((4 * (i & 1) + g) ^ (l15 >> 1)); PIN(ca[i & 1][i >> 1]); }
      PIN(ha);
      auto rd2 = [&](int u, int ks, int i) {
        if (i & 1) { if (u >= 3) fc[ks][i >> 1] = lr<bf16x8>(ca[ks][(u % 7) >> 2] + ((u % 7) & 3) * 16384 + (i >> 1) * 2048); } else fh[ks][i >> 1] = lr<bf16x8>(ha + (u % 3) * 16384 + (i >> 1) * 2048 + ks * 1024);
      };
      tag = lr<u64>(L + L_SYNC + 32); PIN(tag);
#pragma unroll
      for (int e = 4 * NC0; e < 16; ++e) c_piece(e >> 2, e & 3);
      WAITV(16 - 4 * NC0); BAR();
      TS(28);
      WAITV(0); BAR();
      gq[0] = bl1c(wsr, vGo, 0); gq[1] = bl1c(wsr, vGo + 128, 0);
#pragma unroll
      for (int i = 0; i < 24; ++i) cp[i >> 2][i & 3] = lr<bf16x8>(ca[(i >> 2) & 1][0] + (i >> 3) * 16384 + (i & 3) * 2048);
      LBAR();
#pragma unroll
      for (int i = 0; i < 24; ++i) PIN(cp[i >> 2][i & 3]);
      TS(12);
      u64 cy0 = 0; CY0();
#pragma unroll
      for (int i = 0; i < 8; ++i) rd2(0, 0, i);
#pragma unroll
      for (int u = 0; u < 16; ++u) {
#pragma unroll
        for (int ks = 0; ks < 2; ++ks) {
          if (ks == 1 && u < 15) LBAR();
          SB();
#pragma unroll
          for (int qq = 0; qq < 16; ++qq) {
            const int m = qq >> 2, n = (m & 1) ? 3 - (qq & 3) : (qq & 3);
            if (u | ks) mmc(acc[m][n], u < 3 ? cp[(2 * u + ks) % 6][n] : fc[ks][n], fh[ks][m]); else mmz(acc[m][n], cp[0][n], fh[ks][m]);
            SB();
            if ((ks == 0 || u < 15) && qq < 8) rd2(u + ks, ks ^ 1, qq);
            SB();
          }
        }
      }
      NOPS();
      CY1(21);
    }
    TS(13);
    {
      const u32 vl = lane * 16;
#pragma unroll
      for (int m = 0; m < 4; ++m) st4c(__builtin_bit_cast(u32x4, pk8(acc[m][2], acc[m][3])), wsr, vl, WS_X + (((bid ^ 32) * 4 + w) * 4 + m) * 1024);
      BAR();
      WAITV(0); PIN(gq[0]); PIN(gq[1]);
      st2c(tag, wsr, lane == 0 ? 0 : WS_END, WS_FP + ((bid ^ 32) * 4 + w) * 128);
      TS(29);
      NOUNROLL for (u32 spn = 0; spn < SPIN_CAP; ++spn) {
        u64 a; asm volatile("buffer_load_dwordx2 %0, off, %1, %2 sc1\n\ts_waitcnt vmcnt(0)" : "=&v"(a) : "s"(wsr), "s"(WS_FP + (bid * 4 + w) * 128) : "memory");
        if (__all(a == tag)) break;
      }
      const u32 xo = WS_X + (bid * 4 + w) * 4096;
      u32x4 xv[4] = {bl4c<0>(wsr, vl, xo), bl4c<1024>(wsr, vl, xo), bl4c<2048>(wsr, vl, xo), bl4c<3072>(wsr, vl, xo)};
      const u32 pa = L + 131072 + wm * 16384 + wn * 2048 + g * 256 + (l15 ^ (4 * g)) * 16;
      WAITV(0);
      TS(31);
#pragma unroll
      for (int m = 0; m < 4; ++m) {
        PIN(xv[m]);
        lw<f32x4>(pa + m * 4096, acc[m][0] + f32x4{bf2f(xv[m].x & 0xffffu), bfhi(xv[m].x), bf2f(xv[m].y & 0xffffu), bfhi(xv[m].y)});
        lw<f32x4>(pa + m * 4096 + 1024, acc[m][1] + f32x4{bf2f(xv[m].z & 0xffffu), bfhi(xv[m].z), bf2f(xv[m].w & 0xffffu), bfhi(xv[m].w)});
      }
    }
  }
  TS(14);
  {
    const rsrc_t outr = mkr(out, 8u << 20);
    u32 pa[2], ba[4], oa[2]; float og[2];
#pragma unroll
    for (int hi = 0; hi < 2; ++hi) {
      const int row = lr8 + 8 * hi, s1 = 32 * wv + row + 16 * (sh ^ (w & 1));
      pa[hi] = L + 131072 + wv * 4096 + lc8 * 256 + (row ^ (4 * pq)) * 16; oa[hi] = (u32)(s2 + 8 * s1) * 4096u + lc8 * 16; og[hi] = rbf(1.f - __uint_as_float(gq[hi]));
      ba[2 * hi] = L + 81920 + (wv * 16 + row) * 256 + 16 * (lc8 ^ row); ba[2 * hi + 1] = ba[2 * hi] ^ 128;
      PIN(pa[hi]); PIN(oa[hi]); PIN(og[hi]); PIN(ba[2 * hi]); PIN(ba[2 * hi + 1]);
    }
    LBAR();
    f32x4 v[4][2];
#pragma unroll
    for (int i = 0; i < 4; ++i) {
      v[i][0] = lr<f32x4>(pa[i >> 1] + (i & 1) * 2048); v[i][1] = lr<f32x4>(ba[i]);
    }
    SB();
#pragma unroll
    for (int i = 0; i < 4; ++i)
      st4(__builtin_bit_cast(u32x4, og[i >> 1] * v[i][0] + v[i][1]), outr, oa[i >> 1], 128 * ct + 2048 * (i & 1));
  }
  TS(15);
}

extern "C" void kernel_launch(void* const* d_in, const int* in_sizes, int n_in, void* d_out, int out_size, void* d_ws, size_t ws_size, hipStream_t stream) {
  (void)in_sizes; (void)n_in; (void)out_size; (void)ws_size;
  (void)hipFuncSetAttribute((const void*)k_all, hipFuncAttributeMaxDynamicSharedMemorySize, L_TOT);
  const u16* const* in = (const u16* const*)d_in;
  k_all<<<256, 512, L_TOT, stream>>>(in[0], in[6], in[7], in[10], in[4], in[5], in[1], in[2], in[8], in[9], in[11], in[12], (char*)d_ws, (float*)d_out);
}
```

```cpp
#include <hip/hip_runtime.h>
#include <hip/hip_bf16.h>
#include <stdint.h>

typedef unsigned short u16;
typedef unsigned int u32;
typedef unsigned long long u64;
typedef __attribute__((ext_vector_type(4))) short s16x4;
typedef __attribute__((ext_vector_type(8))) __bf16 bf16x8;
typedef __attribute__((ext_vector_type(2))) __bf16 bf16x2;
typedef __attribute__((ext_vector_type(2))) float f32x2;
typedef __attribute__((ext_vector_type(4))) float f32x4;
typedef __attribute__((ext_vector_type(4))) u32 u32x4;
typedef __attribute__((ext_vector_type(2))) u32 u32x2;
#define OUTPOL " nt"
#define DI __device__ __forceinline__
#define LDSP __attribute__((address_space(3)))
#define RLX __ATOMIC_RELAXED, __HIP_MEMORY_SCOPE_AGENT

#define NREP 8
constexpr u32 WS_E = 0, WS_AGG = 65536, WS_FP = 0x40000, WS_FU = 0x60000, WS_FH = WS_FU + NREP * 2048, WS_FT = WS_FH + NREP * 2048, WS_G = WS_FT + NREP * 2048;
constexpr u32 WS_U = 1u << 20, WS_T = 5u << 20, WS_H = 9u << 20, WS_X = 17u << 20, WS_END = 21u << 20;
constexpr u32 L_RA = 65536, L_W = 98304, L_WS = 17408, L_V = 133120, L_CH = 5264, L_PS = 144, L_SYNC = 154176, L_SEG = 154688, L_TOT = 163840;
constexpr u32 SPIN_CAP = 1u << 18, SENT = 0xffffffffu;

DI float bf2f(u32 h) { return __builtin_bit_cast(float, h << 16); }
DI float bfhi(u32 h) { return __builtin_bit_cast(float, h & 0xffff0000u); }
DI u32 pk2(float a, float b) { f32x2 v = {a, b}; return __builtin_bit_cast(u32, __builtin_convertvector(v, bf16x2)); }
DI u32 pk2(f32x2 v) { return __builtin_bit_cast(u32, __builtin_convertvector(v, bf16x2)); }
DI float rbf(float a) { return bf2f(pk2(a, a) & 0xffffu); }
typedef __amdgpu_buffer_rsrc_t rsrc_t;
DI rsrc_t mkr(const void* p, u32 bytes) { return __builtin_amdgcn_make_buffer_rsrc((void*)p, 0, bytes, 0x00020000); }
DI void bldsc(rsrc_t r, u32 vo, u32 so, u32 l, u32 lo) { asm volatile("s_add_u32 m0, %3, %4\n\ts_nop 0\n\tbuffer_load_dwordx4 %0, %1, %2 offen sc1 lds" :: "v"(vo), "s"(r), "s"(so), "s"(l), "i"(lo) : "memory", "scc"); }
DI void blds(rsrc_t r, u32 vo, u32 so, u32 l, u32 lo) { asm volatile("s_add_u32 m0, %3, %4\n\ts_nop 0\n\tbuffer_load_dwordx4 %0, %1, %2 offen lds" :: "v"(vo), "s"(r), "s"(so), "s"(l), "i"(lo) : "memory", "scc"); }
template <class T> DI T lr(u32 a) { return *(const LDSP T*)(uintptr_t)a; }
template <class T> DI void lw(u32 a, T v) { *(LDSP T*)(uintptr_t)a = v; }
DI u32x2 trr(u32 a) { return __builtin_bit_cast(u32x2, __builtin_amdgcn_ds_read_tr16_b64_v4i16((LDSP s16x4*)(uintptr_t)a)); }
DI bf16x8 cat(u32x2 a, u32x2 b) { u32x4 v = {a.x, a.y, b.x, b.y}; return __builtin_bit_cast(bf16x8, v); }
DI bf16x8 pk8(f32x4 a, f32x4 b) { u32x4 v = {pk2(a[0], a[1]), pk2(a[2], a[3]), pk2(b[0], b[1]), pk2(b[2], b[3])}; return __builtin_bit_cast(bf16x8, v); }
DI f32x4 mma(bf16x8 a, bf16x8 b, f32x4 c) { return __builtin_amdgcn_mfma_f32_16x16x32_bf16(a, b, c, 0, 0, 0); }
DI void mmz(f32x4& c, bf16x8 a, bf16x8 b) { asm volatile("v_mfma_f32_16x16x32_bf16 %0, %1, %2, 0" : "=&v"(c) : "v"(a), "v"(b)); }
DI void mmc(f32x4& c, bf16x8 a, bf16x8 b) { asm volatile("v_mfma_f32_16x16x32_bf16 %0, %1, %2, %0" : "+v"(c) : "v"(a), "v"(b)); }
DI bf16x8 asf(u32x4 v) { return __builtin_bit_cast(bf16x8, v); }
DI float cosr(float rev) { return __builtin_amdgcn_cosf(rev); }
DI float sinr(float rev) { return __builtin_amdgcn_sinf(rev); }
template <int O> DI u32x4 bl4(rsrc_t r, u32 vo, u32 so) { u32x4 v; asm volatile("buffer_load_dwordx4 %0, %1, %2, %3 offen offset:%4" : "=v"(v) : "v"(vo), "s"(r), "s"(so), "n"(O) : "memory"); return v; }
template <int O> DI u32x4 bl4c(rsrc_t r, u32 vo, u32 so) { u32x4 v; asm volatile("buffer_load_dwordx4 %0, %1, %2, %3 offen offset:%4 sc1" : "=v"(v) : "v"(vo), "s"(r), "s"(so), "n"(O) : "memory"); return v; }
DI u32 bl1c(rsrc_t r, u32 vo, u32 so) { u32 v; asm volatile("buffer_load_dword %0, %1, %2, %3 offen sc1" : "=v"(v) : "v"(vo), "s"(r), "s"(so) : "memory"); return v; }
DI void touch(rsrc_t r, u32 vo, u32 l) { asm volatile("s_add_u32 m0, %2, %3\n\ts_nop 0\n\tbuffer_load_dword %0, %1, 0 offen lds" :: "v"(vo), "s"(r), "s"(l), "i"(L_SYNC + 256) : "memory", "scc"); }
DI u32 bl1(rsrc_t r, u32 vo, u32 so) { u32 v; asm volatile("buffer_load_dword %0, %1, %2, %3 offen" : "=v"(v) : "v"(vo), "s"(r), "s"(so) : "memory"); return v; }
DI void blh(rsrc_t r, u32 vo, u32 l, u32 lo) { asm volatile("s_add_u32 m0, %2, %3\n\ts_nop 0\n\tbuffer_load_ushort %0, %1, 0 offen lds" :: "v"(vo), "s"(r), "s"(l), "i"(lo) : "memory", "scc"); }
DI u32 gl1(const void* p) { u32 v; asm volatile("global_load_dword %0, %1, off" : "=v"(v) : "v"(p) : "memory"); return v; }
DI u32 glh(const void* p) { u32 v; asm volatile("global_load_ushort %0, %1, off" : "=v"(v) : "v"(p) : "memory"); return v; }
template <int O> DI u32x4 gl4(const void* p) { u32x4 v; asm volatile("global_load_dwordx4 %0, %1, off offset:%2" : "=v"(v) : "v"(p), "n"(O) : "memory"); return v; }
DI void st4c(u32x4 v, rsrc_t r, u32 vo, u32 so) { asm volatile("buffer_store_dwordx4 %0, %1, %2, %3 offen sc1\n\ts_nop 1" :: "v"(v), "v"(vo), "s"(r), "s"(so) : "memory"); }
DI void st2c(u64 v, rsrc_t r, u32 vo, u32 so) { asm volatile("buffer_store_dwordx2 %0, %1, %2, %3 offen sc1" :: "v"(v), "v"(vo), "s"(r), "s"(so) : "memory"); }
DI void st4(u32x4 v, rsrc_t r, u32 vo, u32 so) { asm volatile("buffer_store_dwordx4 %0, %1, %2, %3 offen" OUTPOL "\n\ts_nop 1" :: "v"(v), "v"(vo), "s"(r), "s"(so) : "memory"); }
#define POLLSLP 2
#define NOUNROLL _Pragma("unroll 1")
#define SB() __builtin_amdgcn_sched_barrier(0)
template <class T> DI void PINS(T& v) { asm volatile("" : "+s"(v)); }
template <class T> DI void PIN(T& v) { asm volatile("" : "+v"(v)); }
#define NOPS() do { SB(); asm volatile("s_nop 7\n\ts_nop 1"); SB(); } while (0)
#define WAITV(n) asm volatile("s_waitcnt vmcnt(%0)" :: "n"(n) : "memory")
#define WAITL(n) asm volatile("s_waitcnt lgkmcnt(%0)" :: "n"(n) : "memory")
#define FENCE() asm volatile("" ::: "memory")
#define BAR() do { FENCE(); __builtin_amdgcn_s_barrier(); FENCE(); } while (0)
#define LBAR() do { WAITL(0); __builtin_amdgcn_s_barrier(); FENCE(); } while (0)
DI u32 umax3(u32 a, u32 b, u32 c) { return max(max(a, b), c); }
DI void gwait(u32 words, u32 phase) {
  NOUNROLL for (u32 s = 0; s < SPIN_CAP; ++s) {
    u32x4 v; asm volatile("ds_read_b128 %0, %1\n\ts_waitcnt lgkmcnt(0)" : "=v"(v) : "v"(words) : "memory");
    if (__builtin_amdgcn_readfirstlane(min(min(v.x, v.y), min(v.z, v.w))) >= phase) break;
  }
  FENCE();
}
DI void gpost(u32 words, int wq, u32 phase) { FENCE(); lw<u32>(words + 4 * wq, phase); FENCE(); }
DI void gbar(u32 words, int wq, u32 phase) { gpost(words, wq, phase); gwait(words, phase); }
DI void pollq(rsrc_t wr, u32 off, int lane, u64 tag) {
  NOUNROLL for (u32 s = 0; s < SPIN_CAP; ++s) {
    u64 a; asm volatile("buffer_load_dwordx2 %0, %1, %2, %3 offen sc1\n\ts_waitcnt vmcnt(0)" : "=&v"(a) : "v"(lane * 8), "s"(wr), "s"(off) : "memory");
    if (__all(a == tag)) break;
    __builtin_amdgcn_s_sleep(POLLSLP);
  }
}
#define TSW(n, t) do {} while (0)
#define CY0()
#define CY1(n)
#define ITS(p, j) do {} while (0)
#define TS(n) TSW(n, 0)
#define TX(n) TSW(n, 256)
#define ROT1 tt
#define PX 8
#define BLK 0
#define ORD2(u) (BLK ? (((((u) & 12) + rot2) & 12) | (((u) + rot2) & 3)) : (((u) + rot2) & 15))
#define ROT2 (BLK ? (tt & 12) + ((ti + (PX == 8 ? pj : pj >> 1)) & 3) : tt)
#define PRI 0
#define WPOS 0
#define NC0 2
#define NW13 4
#define TRP 1
#define FMASK 63u
#define BLATE 0
#define FTU 0
#define CFL 1
#define CPOS 3
#define JF (U0 + UE + LEAD)
struct c2 { f32x2 r, i; };
DI f32x2 fma2(f32x2 a, f32x2 b, f32x2 c) { return __builtin_elementwise_fma(a, b, c); }
DI c2 cfma(c2 a, c2 b, c2 c) { return {fma2(a.r, b.r, fma2(-a.i, b.i, c.r)), fma2(a.r, b.i, fma2(a.i, b.r, c.i))}; }
DI c2 cmul(c2 a, c2 b) { return {fma2(-a.i, b.i, a.r * b.r), fma2(a.r, b.i, a.i * b.r)}; }
DI c2 csq(c2 a) { return {fma2(a.r, a.r, -a.i * a.i), (a.r + a.r) * a.i}; }
DI c2 bc(c2 a, int ps) { return ps ? c2{a.r.yy, a.i.yy} : c2{a.r.xx, a.i.xx}; }
DI c2 csel(bool c, c2 a, c2 b) { return {c ? a.r : b.r, c ? a.i : b.i}; }
DI c2 asc(f32x4 v) { return {v.xy, v.zw}; }
DI void pinc(c2& a) { PIN(a.r); PIN(a.i); }
DI f32x2 spread16(float v) { const u32 u = __float_as_uint(v); const auto e = __builtin_amdgcn_permlane16_swap(u, u, false, false); return f32x2{__uint_as_float(e[0]), __uint_as_float(e[1])}; }
DI float sum16(float v) { const f32x2 e = spread16(v); return e.x + e.y; }
DI float sum32(float v) { const u32 u = __float_as_uint(v); const auto e = __builtin_amdgcn_permlane32_swap(u, u, false, false); return __uint_as_float(e[0]) + __uint_as_float(e[1]); }
template <int CTRL> DI float dppadd(float v) { return v + __int_as_float(__builtin_amdgcn_mov_dpp(__float_as_int(v), CTRL, 0xf, 0xf, true)); }

DI u32x4 cos8(float a0, float kk, float c2d) {
  float c[8]; c[0] = cosr(a0); c[1] = cosr(fmaf(kk, 4.f, a0));
#pragma unroll
  for (int j = 2; j < 8; ++j) c[j] = fmaf(c2d, c[j - 1], -c[j - 2]);
  return u32x4{pk2(c[0], c[1]), pk2(c[2], c[3]), pk2(c[4], c[5]), pk2(c[6], c[7])};
}
DI void hart_table(u32x4 (&tab)[2][4], int kap, bool sp, int g) {
  const float kp = (float)kap * (1.f / 256.f), km = sp ? 0.125f : kp, cp = 2.f * cosr(4.f * kp), cm = sp ? -2.f : cp;
#pragma unroll
  for (int r = 0; r < 4; ++r) {
    const float kk = r == 1 && sp ? km : -km, fr_ = (float)(32 * g + r);
    tab[0][r] = cos8(fmaf(kp, fr_, -0.125f), kp, cp); tab[1][r] = cos8(fmaf(kk, fr_, -0.125f), kk, cm);
  }
}
template <bool INV> DI void hart256(f32x2 (&o)[8][2], u32 ap, const u32x4 (&tab)[2][4], int kap, bool sp) {
  const float sg1 = (kap & 1) ? -1.f : 1.f, al1 = sp ? 0.f : 1.f;
  const f32x2 sgn = {sg1, sg1}, al = {al1, al1};
  bf16x8 a[4][2]; f32x4 m[2][4][2];
#pragma unroll
  for (int r = 0; r < 4; ++r) { a[r][0] = cat(trr(ap + r * 8192), trr(ap + r * 8192 + 512)); a[r][1] = cat(trr(ap + r * 8192 + 4096), trr(ap + r * 8192 + 4608)); }
  SB();
#pragma unroll
  for (int r = 0; r < 4; ++r)
#pragma unroll
    for (int i = 0; i < 4; ++i) m[i >> 1][r][i & 1] = mma(a[r][i & 1], asf(tab[(i >> 1) ^ (INV & i & 1)][r]), f32x4{});
  SB();
#pragma unroll
  for (int hh = 0; hh < 2; ++hh) {
    f32x2 p[2][4];
#pragma unroll
    for (int i = 0; i < 8; ++i) { const f32x2 sg_ = INV && i >= 4 ? -sgn : sgn; p[i >> 2][i & 3] = hh ? fma2(sg_, m[i >> 2][i & 3][1].zw, m[i >> 2][i & 3][0].zw) : fma2(sg_, m[i >> 2][i & 3][1].xy, m[i >> 2][i & 3][0].xy); }
    const f32x2 s0 = p[0][1] + p[0][3], d0 = p[0][1] - p[0][3], s1 = INV ? p[1][1] + p[1][3] : fma2(al, p[1][1], p[1][3]), d1 = INV ? p[1][1] - p[1][3] : fma2(-al, p[1][3], p[1][1]);
    const f32x2 x0 = sp ? d0 : d1, x1 = sp ? d1 : d0;
#pragma unroll
    for (int sg = 0; sg < 2; ++sg) {
      const f32x2 A = p[sg][0] + p[sg][2], B = p[sg][0] - p[sg][2], S = sg ? s1 : s0, X = sg ? x1 : x0;
      o[4 * sg][hh] = A + S; o[4 * sg + 2][hh] = A - S; o[4 * sg + 1][hh] = B + X; o[4 * sg + 3][hh] = B - X;
    }
  }
  SB();
#pragma unroll
  for (int i = 0; i < 8; ++i) {
    const auto e0 = __builtin_amdgcn_permlane32_swap(__float_as_uint(o[i][0].x), __float_as_uint(o[i][1].x), false, false);
    const auto e1 = __builtin_amdgcn_permlane32_swap(__float_as_uint(o[i][0].y), __float_as_uint(o[i][1].y), false, false);
    o[i][0] = f32x2{__uint_as_float(e0[0]), __uint_as_float(e1[0])}; o[i][1] = f32x2{__uint_as_float(e0[1]), __uint_as_float(e1[1])};
  }
  SB();
#pragma unroll
  for (int i = 0; i < 8; ++i) { const f32x2 a_ = o[i][0], b_ = o[i][1]; o[i][0] = a_ + b_; o[i][1] = a_ - b_; }
}

struct TileD { int k, c, src, w; };
DI TileD tile_desc(int tau, int k1, int kd1) {
  TileD t; const int i = tau >> 1, ka = tau & 1, hi = i >= 5;
  t.c = kd1 + 2 * ka;
  t.src = k1 ? -1 : (kd1 ? ka : hi);
  t.k = 256 * i + (k1 ? k1 : (kd1 ? 128 * ka : -1152 * hi));
  t.w = k1 ? 1 : (kd1 ? 2 : (tau >= 18 ? 0 : ((i & 3) == 0 && i < 8 ? 1 : 2)));
  return t;
}
DI void m0_fwd(bf16x8& o, int n, int l15, int g, int k1, int kd1) {
  const TileD t = tile_desc(8 * n + (l15 >> 1), k1, kd1); f32x4 v[2];
  const float ak = (float)t.k * (-1.f / 2048.f), a0 = fmaf(ak, (float)(2 * g), -0.25f * (float)(l15 & 1)), ac = -0.25f * (float)t.c;
#pragma unroll
  for (int j = 0; j < 8; ++j) {
    const int rs = j & 1;
    const float c = cosr(a0 + ((j & 2) ? ak : 0.f) + ((j & 4) ? ac : 0.f) + (t.src < 0 ? (rs ? 0.125f : -0.125f) : 0.f));
    v[j >> 2][j & 3] = (t.src >= 0 && (t.w == 0 || t.src != rs)) ? 0.f : c;
  }
  o = pk8(v[0], v[1]);
}
DI void tr2(bf16x8& X, bf16x8& Y, bf16x8 A, bf16x8 B, u32 wr, u32 rd) {
  const u32x4 a = __builtin_bit_cast(u32x4, A), b = __builtin_bit_cast(u32x4, B);
  FENCE(); lw<u32x2>(wr, a.xy); lw<u32x2>(wr + 512, b.xy); lw<u32x2>(wr + 1024, a.zw); lw<u32x2>(wr + 1536, b.zw); FENCE();
  X = cat(trr(rd), trr(rd + 512)); Y = cat(trr(rd + 1024), trr(rd + 1536)); FENCE();
}

template <int O> DI void ldhi(float& v, u32 a) { asm volatile("ds_read_u16_d16_hi %0, %1 offset:%2" : "+v"(v) : "v"(a), "n"(O) : "memory"); }
#define UE 2
#define U0 0
constexpr int g1_post(int j) { return j >= U0 && j < U0 + UE ? 4 / UE : 0; }
#define LEAD 2
constexpr int g1_wait(int j) { int c = j >= LEAD ? g1_post(j - LEAD) : 0; for (int u = j - LEAD + 1; u < j; ++u) c += (u + LEAD < 16 ? 8 : 0) + (u >= 0 ? g1_post(u) : 0) + (u == 15 - 2 * LEAD ? 1 : 0); return c; }
#define TU 12
#define NE 2
#define TFIRST 1
constexpr int g2_c0(int u) { return u >= 13 ? 64 : 16 + 4 * NE + u * (48 - 4 * NE) / 13; }
constexpr bool g2_ok() { for (int u = 0; u < 13; ++u) for (int e = g2_c0(u); e < g2_c0(u + 1); ++e) if (u > e / 4 - 3 || (e / 4 >= 9 && u < e / 4 - 5)) return false; return true; }
static_assert(g2_ok(), "C tile schedule");
constexpr int g2_pre(int u) { return u < 0 ? 0 : g2_c0(u + 1) - g2_c0(u) + (u == 0 || u == 3 ? 1 : 0); }
constexpr int g2_post(int u) { return (u >= TU && u < TU + 4 ? 3 : 0) + (u == TU - 5 ? 1 : 0); }
constexpr int g2_wait(int u) { int c = g2_post(u - 3); for (int v = u - 2; v < u; ++v) c += g2_pre(v) + (v + 3 < 16 ? 4 : 0) + g2_post(v); return c; }
template <int N> struct IC { static constexpr int v = N; };
template <int I, int N, class F> DI void sfor(F&& f) { if constexpr (I < N) { f(IC<I>{}); sfor<I + 1, N>(f); } }

typedef __attribute__((ext_vector_type(8))) u64 u64x8;
typedef __attribute__((ext_vector_type(4))) u64 u64x4;
typedef __attribute__((ext_vector_type(2))) u64 u64x2;
__global__ __launch_bounds__(512, 2) void k_all(const u16*, const u16*, const u16*, const u16*, const u16*, const u16*, const u16*, const u16*,
    const u16*, const u16*, const u16*, const u16*, char*, float*) {
#define GETARGS() u64x8 ka; u64x4 kb_; u64x2 kc; \
  asm volatile("s_load_dwordx16 %0, %3, 0x0\n\ts_load_dwordx8 %1, %3, 0x40\n\ts_load_dwordx4 %2, %3, 0x60" : "=&s"(ka), "=&s"(kb_), "=&s"(kc) : "s"(__builtin_amdgcn_kernarg_segment_ptr()) : "memory")
  extern __shared__ __attribute__((aligned(16))) char lds_[];
  const u32 L = (u32)(uintptr_t)(LDSP char*)lds_;
  const int lane = threadIdx.x & 63, wv = __builtin_amdgcn_readfirstlane(threadIdx.x >> 6), w = wv & 3, rh = wv >> 2, l15 = lane & 15, g = lane >> 4;
  const int bid = blockIdx.x, xcd = bid & 7, jj = bid >> 3, ti = jj & 3, pj = jj >> 2;
  const int tt = (xcd >> 1) * 4 + ti, pt = (xcd & 1) * 8 + pj;
  const int s2 = tt >> 1, sh = tt & 1, ct = pt;
  const int lr8 = lane >> 3, lc8 = lane & 7, q = l15 >> 2, pq = lane & 3;
  int rot1 = ROT1, rot2 = ROT2;
  u32 gq[2]; u64 outp, wsp;
#define ARGS() asm volatile("s_waitcnt lgkmcnt(0)" : "+s"(ka), "+s"(kb_), "+s"(kc) :: "memory"); \
  char* ws = (char*)kc[0]; outp = kc[1]; wsp = kc[0]; \
  const rsrc_t xr = mkr((const void*)ka[0], 4u << 20), wsr = mkr(ws, WS_END), cr = mkr((const void*)((ct & PX) ? kb_[1] : kb_[0]), 2u << 20); \
  auto issue_a = [&](int rr, int hf) { const int r = (rr + r0) & 3; blds(xr, vA, (8 * r + 1024 * hf) * 2048, L + r * 8192 + hf * 4096 + w * 1024, L_RA); }; \
  u32 vC[2]; vC[0] = (u32)(32 * (ct ^ ((w & 1) * PX)) + 512 * (w >> 1) + lr8) * 2048u + 16 * (lc8 ^ (lr8 >> 1)); vC[1] = vC[0] ^ 64; \
  auto c_piece = [&](int i, int e) { blds(cr, vC[e & 1], e * 8 * 2048 + ORD2(i) * 128, L + w * 4096, (i % 6) * 16384 + e * 1024); }; \
  const u32 vGo = WS_G + tt * 512 + (u32)((w & 1) * 64 + lr8 * 4 + 2 * rh + (w >> 1)) * 4

  if (rh) {
    GETARGS();
    {
      u64 pc; u32 nb; asm volatile("s_getpc_b64 %0\n.Lcode_lo:\n\ts_mov_b32 %1, .Lcode_hi-.Lcode_lo\n\t.subsection 1\n.Lcode_hi:\n\t.subsection 0" : "=s"(pc), "=s"(nb));
      if (w == 3) touch(mkr((const void*)(pc & ~127ull), min(nb, 61440u)), (lane * 32 + jj) * 128, L);
    }
    const int ii = 8 * (w & 1) + lr8, swz = 16 * (lc8 ^ ((ii >> 1) & 7)), ac = lc8 ^ (((lr8 >> 1) & 1) | ((w & 1) << 1)), r0 = 2 * sh + (ct & 1);
    u32 vB = (u32)(64 * pt + ii) * 2048u + swz, vA = (u32)(s2 + 32 * (8 * w + lr8)) * 2048u + 64 * ct + 16 * (ac & 3) + 1024 * (ac >> 2);
    u32 vP = (64 * pt + 32 * (w & 1) + (lane & 31)) * 2;
    PIN(vA); PIN(vB); PIN(vP);
    lw<u32>(L + L_SYNC + 4 * wv, 0u);
    ARGS();
    const rsrc_t br = mkr((const void*)((w & 2) ? ka[2] : ka[1]), 2u << 20);
    auto issue_b1 = [&](int j, int i) { blds(br, vB, 16 * i * 2048 + ((j + rot1) & 15) * 128, L + w * 1024, ((j + 3) & 3) * 32768 + 16384 + i * 4096); };
    blh(mkr((const void*)ka[3], 2048), vP, L + w * 1024, L_V + 16384); blh(mkr((const void*)ka[4], 2048), vP, L + w * 1024, L_V + 16640); blh(mkr((const void*)ka[5], 2048), vP, L + w * 1024, L_V + 16896);
    issue_a(0, 0); issue_a(0, 1); issue_b1(0, 0); issue_b1(0, 1); issue_b1(0, 2); issue_b1(0, 3);
    BAR();
    SB();
    const u32 vX = (u32)(128 * tt + 16 * (ii >> 2) + 8 * (ii & 1) + 2 * (w >> 1) + ((ii >> 1) & 1)) * 2048u + swz, vl = lane * 16;
    auto issue_x1 = [&](int j, int i) { blds(xr, vX, (64 * (i >> 1) + 4 * (i & 1)) * 2048 + ((j + rot1) & 15) * 128, L + w * 1024, ((j + 3) & 3) * 32768 + i * 4096); };
    u64 tag; asm volatile("buffer_load_dwordx2 %0, off, %1, 0 sc1" : "=v"(tag) : "s"(wsr) : "memory");
    if (w == 0) st4c(u32x4{SENT, SENT, SENT, SENT}, wsr, lane < 32 ? vl : WS_END, WS_AGG + tt * 8192 + pt * 512);
    if (w == 2) touch(mkr(ws + WS_FP, WS_G + 8192 - WS_FP), (lane * 256 + bid) * 128, L);
    if (w == 1) touch(mkr((const void*)kb_[3], 2048), ((jj & 15) + 16 * lane) * 128, L);
    const int kap = 16 * sh + l15, ecol = 8 * w + 4 * (g & 1) + 2 * (g >> 1);
    u32 q_ds[2] = {bl1(mkr((const void*)kb_[2], 2048), 2 * ecol, 64 * ct), bl1(mkr((const void*)kb_[2], 2048), 2 * ecol, 64 * ct + 1024)};
    const rsrc_t w0 = mkr((const void*)ka[6], 2048 * 1026), w1 = mkr((const void*)ka[7], 2048 * 1026);
    const u32 rep = (bid & (NREP - 1)) * 2048;
    TX(16);
    const int k1 = xcd * 16 + (jj >> 1), kd1 = jj & 1, cc = kd1 + 2 * (w & 1);
    const bool sp0 = (k1 == 0 && kd1 == 0);
    bf16x8 m0[2], mi[2], m0x, mix[2], fa_r, fa_i, fb_r, fb_i, gb_r, gb_i, ga_r, ga_i; f32x4 t1c, t1s, t2c, t2s;
    u32 vW[5], wa[5], wo[5]; float fr[4][4], fi[4][4], er[4], ei[4];
    const float fl = (float)l15, fg = (float)(4 * g), fc = (float)cc;
    const u32 tw = L + L_SEG + w * 2048 + l15 * 32 + 8 * g, tr = L + L_SEG + w * 2048 + (4 * g + q) * 32 + 8 * pq;
    SB();
    {
      const float st = fmaf(fl, -4.f / 64.f, fc * (-1.f / 64.f));
#pragma unroll
      for (int e = 0; e < 4; ++e) { const float a = (fg + (float)e) * st; t2c[e] = cosr(a); t2s[e] = sinr(a); t1c[e] = cosr(a * (1.f / 16.f)); t1s[e] = sinr(a * (1.f / 16.f)); }
      fa_r = pk8(t2c, -t2s); fa_i = pk8(t2s, t2c); PIN(t1c); PIN(t1s);
      PIN(fa_r); PIN(fa_i);
      WAITV(9); gpost(L + L_SYNC + 16, w, 1);
      const float fm = (float)(l15 < 8 ? l15 : l15 ^ 3) * (-1.f / 16.f); const u32 sl = l15 < 8 ? 0u : 0x80008000u;
#pragma unroll
      for (int e = 0; e < 4; ++e) { const float a = (fg + (float)e) * fm; t2c[e] = cosr(a); t2s[e] = sinr(a); }
      fb_r = pk8(t2c, -t2s); fb_i = asf(__builtin_bit_cast(u32x4, pk8(t2s, t2c)) ^ sl);
      PIN(fb_r); PIN(fb_i);
    }
    u32 xa[2], ua, us, us0, fo, ap0, wp0, tr_, tw_;
    const u32 vo = L + L_V + w * L_CH;
    int wid, wsl; f32x4 uc, us_;
    auto fill = [&](auto k_) {
      constexpr int k = decltype(k_)::v;
      SB();
      if constexpr (k == -4) {
        const float mk = (float)(k1 + 256 * (l15 >> 2)) * (-1.f / 2048.f), mc = -0.25f * (float)(kd1 + (l15 & 2)), ma = fmaf(mk, 0.5f * fg, fmaf((float)(l15 & 1), -0.25f, -0.125f));
        u32x4 f;
#pragma unroll
        for (int e = 0; e < 4; ++e) { const float a = ma + ((e & 1) ? mk : 0.f) + ((e & 2) ? mc : 0.f); f[e] = pk2(cosr(a), -sinr(a)); }
        m0[0] = asf(f); m0[1] = asf(f ^ u32x4{0u, 0x80008000u, 0u, 0x80008000u});
        const float b = fmaf((float)(l15 >> 1), fmaf(fg, -1.f / 32.f, (float)k1 * (-1.f / 2048.f)), fmaf((float)(l15 & 1), 0.75f, fmaf((float)k1, 1.f / 512.f, -0.125f))), b1 = b - 0.25f * (float)kd1;
        const u32 p0 = pk2(cosr(b), sinr(b)), p1 = pk2(cosr(b1), sinr(b1)), sgn = (l15 & 2) ? 0x80008000u : 0u;
        mi[0] = asf(u32x4{p0, p0, p0 ^ sgn, p0 ^ sgn}); mi[1] = asf(u32x4{p1, p1 ^ 0x80008000u, p1 ^ sgn, p1 ^ sgn ^ 0x80008000u});
        if (__builtin_expect(k1 == 0, 0)) { m0_fwd(m0[0], 0, l15, g, 0, kd1); m0_fwd(m0[1], 1, l15, g, 0, kd1); m0_fwd(m0x, 2, l15, g, 0, 0); tr2(mix[0], mix[1], m0x, bf16x8{}, tw, tr); tr2(mi[0], mi[1], m0[0], m0[1], tw, tr); }
        PIN(m0[0]); PIN(m0[1]); PIN(mi[0]); PIN(mi[1]);
      }
      if constexpr (k == -2 || k == -1) { constexpr int i = k + 2; const int n = i ? 63 - kap : kap, rho = 64 * (n & 3) + (n >> 2); xa[i] = L + L_RA + rho * 128 + 16 * (w ^ (((rho >> 1) & 1) | ((rho >> 3) & 1) << 1)) + 2 * (ecol & 7); PIN(xa[i]); }
      if constexpr (k >= 0 && k < 4) {
        const float a = fl * fmaf(fg + (float)k, 4.f / 1024.f, fc * (1.f / 1024.f));
        t2c[k] = cosr(a) * (1.f / 1048576.f); t2s[k] = sinr(a) * (1.f / 1048576.f); uc[k] = cosr(16.f * a); us_[k] = sinr(16.f * a); PIN(t2c); PIN(t2s); PIN(uc); PIN(us_);
      }
      if constexpr (k == 4) { ga_r = pk8(uc, -us_); ga_i = pk8(us_, uc); PIN(ga_r); PIN(ga_i); }
      if constexpr (k >= 5 && k < 9) { const float a = (fg + (g < 2 ? (float)(k - 5) : (float)(8 - k))) * fl * (-1.f / 16.f); uc[k - 5] = cosr(a); us_[k - 5] = sinr(a); PIN(uc); PIN(us_); }
      if constexpr (k == 9) {
        const u32 sg = g < 2 ? 0u : 0x80008000u; const u32x4 t = __builtin_bit_cast(u32x4, pk8(uc, us_));
        gb_r = asf(u32x4{t.x, t.y, t.z ^ sg, t.w ^ sg}); gb_i = asf(u32x4{t.z ^ 0x80008000u, t.w ^ 0x80008000u, t.x ^ sg, t.y ^ sg}); PIN(gb_r); PIN(gb_i);
      }
      if constexpr (k == 12) { ua = L + L_V + (16 * w + g) * 256 + (lr8 & 1) * 128 + 16 * (lc8 ^ g); PIN(ua); }
      if constexpr (k == 13) { us = (u32)(64 * w + 2 * g + (lr8 & 1) + 32 * (sh ^ (w & 1))) * 16384u + lc8 * 16; PIN(us); }
      if constexpr (k == 14) { us0 = (w & 1) && g == 0 ? us ^ (32 * 16384) : us; PIN(us0); }
      if constexpr (k == 11) { fo = lane < NREP ? lane * 2048 + bid * 8 : WS_END; PIN(fo); }
      if constexpr (k == 33) { ap0 = vo + (4 * g + q) * 64 + 16 * pq; PIN(ap0); }
      if constexpr (k == 34) { wp0 = vo + l15 * L_PS + 16 * g; PIN(wp0); }
      if constexpr (k == 35) { tr_ = L + L_V + w * 2 * L_PS + g * L_CH + q * 32 + 8 * pq; PIN(tr_); }
      if constexpr (k == 36) { tw_ = L + L_V + w * 2 * L_PS + q * L_CH + pq * 32 + 8 * g; PIN(tw_); }
      if constexpr (k == 37) { PIN(vC[0]); PIN(vC[1]); }
      if constexpr (k == 40) { wid = 2 * cc + 8 * l15 + 512 * g; PIN(wid); }
      if constexpr (k == 41) { wa[0] = g < 2 ? L + L_W + (w >> 1) * 1056 + ((k1 * 1026) & 15) + wid : L + L_W + (8 + (w >> 1)) * 1056 + ((-k1 * 1026) & 15) + 1664 - wid; PIN(wa[0]); }
      if constexpr (k == 42) { wo[0] = g < 2 ? L + L_W + (8 + (w >> 1)) * 1056 + ((-k1 * 1026) & 15) : L + L_W + (w >> 1) * 1056 + ((k1 * 1026) & 15) + 1024; PIN(wo[0]); }
      if constexpr (k >= 43 && k < 46) { wa[k - 42] = wa[0] + 2112 * (k - 42); wo[k - 42] = wo[0] + 2112 * (k - 42); PIN(wa[k - 42]); PIN(wo[k - 42]); }
      if constexpr (k == 46) {
        if (__builtin_expect(k1 == 0, 0)) {
#pragma unroll
          for (int i = 0; i < 5; ++i) {
            const int kk = tile_desc(w + 4 * i, 0, kd1).k;
            const u32 wp = L + L_W + (kk >> 7) * 1056, wq = L + L_W + (((2048 - kk) & 2047) >> 7) * 1056;
            wa[i] = g < 2 ? wp + wid : wq + 1664 - wid; wo[i] = g < 2 ? wq : wp + 1024;
          }
        }
      }
      if constexpr (k >= 48 && k < 73) {
        constexpr int i = (k - 48) / 5, ph = (k - 48) % 5;
        if (ph == 0) { wid = 64 * (w + 4 * i) + lane; wsl = (wid * 993) >> 16; PIN(wid); PIN(wsl); }
        if (ph == 1) { wsl = min(wsl, 15); wid = 16 * wid - 1056 * wsl; PIN(wid); PIN(wsl); }
        if (ph == 2) { wsl = k1 + (k1 ? 256 : 128) * wsl; PIN(wsl); }
        if (ph == 3) { wsl = wsl < 2048 ? wsl : 4096 - wsl; PIN(wsl); }
        if (ph == 4) { vW[i] = ((wsl * 1026) & ~15) + wid; PIN(vW[i]); }
      }
      if constexpr (k >= 76 && k < 96) {
#pragma unroll
        for (int r = 2 * (k & 1); r < 2 * (k & 1) + 2; ++r) { constexpr int z_ = (k - 76) >> 1; float& z = z_ < 4 ? fr[z_ & 3][r] : z_ < 8 ? fi[z_ & 3][r] : z_ == 8 ? er[r] : ei[r]; z = 0.f; PIN(z); }
      }
      SB();
    };
    auto issue_t = [&](auto j_) {
      constexpr int j = decltype(j_)::v;
      sfor<0, 8>([&](auto i_) { constexpr int i = decltype(i_)::v; if (i < 4) issue_b1(j, i); else issue_x1(j, i - 4); if constexpr (j > 2) fill(IC<8 * j - 8 + i>{}); });
    };
    sfor<-4, 15>(fill);
    TX(17);
    WAITV(0); gpost(L + L_SYNC + 16, w, 2);
#pragma unroll
    for (int i = 0; i < 4; ++i) issue_x1(0, i);
    issue_t(IC<1>{}); if (LEAD == 3) issue_t(IC<2>{});
    gwait(L + L_SYNC, 1);
    u32 sk[8][2];
#pragma unroll
    for (int i = 0; i < 8; ++i) { const u32 a = xa[i >> 2] + 2048 * (i < 4 ? i : (i + 3) & 3); sk[i][0] = lr<u32>(a); sk[i][1] = lr<u32>(a + 64); }
    PIN(tag); PIN(q_ds[0]); PIN(q_ds[1]);
    tag += 0x9e3779b97f4a7c15ull;
    if (w == 0) lw<u64>(L + L_SYNC + 32, tag);
    TX(18);
    u32x4 uq[4]; u64 fu;
    const f32x4 z4 = {};
    bf16x8 a0[2][2];
    sfor<0, 17 - LEAD>([&](auto j_) {
      constexpr int j = decltype(j_)::v;
      WAITV(g1_wait(j));
      if (j == 0) LBAR(); else BAR();
      ITS(1, j);
      PINS(rot1);
      if (j == 0) {
#pragma unroll
        for (int i = 0; i < 4; ++i) uq[i] = lr<u32x4>((ua ^ (64 * (i & 1))) + 1024 * i);
      }
      if (j == JF && w == 0) st2c(tag, wsr, fo, WS_FU);
      if (j == 15 - 2 * LEAD) asm volatile("buffer_load_dwordx2 %0, %1, %2, %3 offen sc1" : "=v"(fu) : "v"(lane * 8), "s"(wsr), "s"(WS_FU + rep + w * 512) : "memory");
      SB();
      if constexpr (j + LEAD < 16) issue_t(IC<j + LEAD>{});
      SB();
      if constexpr (j >= U0 && j < U0 + UE) {
#pragma unroll
        for (int i = (j - U0) * 4 / UE; i < (j - U0 + 1) * 4 / UE; ++i) st4c(uq[i], wsr, i == 0 ? us0 : us, WS_U + ct * 1024 + s2 * 128 + i * 8 * 16384);
      }
      if (j == 15 - LEAD) { PIN(fu); if (__builtin_expect(!__all(fu == tag), 0)) pollq(wsr, WS_FU + rep + w * 512, lane, tag); }
      SB();
    });
#pragma unroll
    for (int i = 0; i < 4; ++i) bldsc(wsr, vl, WS_U + (u32)(2 * k1 + kd1) * 16384u + w * 2048 + (i >> 1) * 8192 + (i & 1) * 1024, L + w * L_CH, L_V + i * 1024);
#pragma unroll
    for (int i = 0; i < NW13; ++i) blds(w0, vW[i], 0, L + w * 1024, L_W + i * 4096);
    WAITV(4 + NW13); BAR();
    ITS(1, 17 - LEAD);
    auto wissue = [&]() {
#pragma unroll
      for (int i = 0; i < 4; ++i) { if (i >= NW13) blds(w0, vW[i], 0, L + w * 1024, L_W + i * 4096); blds(w1, vW[i], 0, L + w * 1024, L_W + L_WS + i * 4096); }
      if (w == 0) { blds(w0, vW[4], 0, L, L_W + 16384); blds(w1, vW[4], 0, L, L_W + L_WS + 16384); }
#pragma unroll
      for (int e = 0; e < 4 * NC0; ++e) c_piece(e >> 2, e & 3);
    };
    {
      WAITV(NW13);
#pragma unroll
      for (int cb = 0; cb < 2; ++cb) { a0[cb][0] = cat(trr(ap0 + 1024 * cb), trr(ap0 + 1024 * cb + 2048)); a0[cb][1] = cat(trr(ap0 + 1024 * cb + 8), trr(ap0 + 1024 * cb + 2056)); }
      FENCE();
      if (WPOS == 0) wissue();
      f32x4 d[2][2][2];
#pragma unroll
      for (int i = 0; i < 8; ++i) d[i >> 2][(i >> 1) & 1][i & 1] = mma(a0[(i >> 1) & 1][i & 1], m0[i >> 2], z4);
#pragma unroll
      for (int i = 0; i < 4; ++i) lw<bf16x8>(wp0 + 16 * (i >> 1) * L_PS + 64 * (i & 1), pk8(d[i >> 1][i & 1][0], d[i >> 1][i & 1][1]));
      if (__builtin_expect(sp0, 0)) {
#pragma unroll
        for (int cb = 0; cb < 2; ++cb) { const bf16x8 o = pk8(mma(a0[cb][0], m0x, z4), mma(a0[cb][1], m0x, z4)); if (l15 < 4) lw<bf16x8>(wp0 + 32 * L_PS + 64 * cb, o); }
      }
      gbar(L + L_SYNC + 16, w, 3);
    }
    TX(8);
    auto tiles = [&](auto nt_, auto i0_) {
      constexpr int NT = decltype(nt_)::v, i0 = decltype(i0_)::v;
      const u32 pr = tr_ + i0 * 8 * L_PS, pw_ = tw_ + i0 * 8 * L_PS;
      bf16x8 a[NT]; f32x4 yr[NT], yi[NT];
#pragma unroll
      for (int i = 0; i < NT; ++i) a[i] = cat(trr(pr + i * 8 * L_PS), trr(pr + i * 8 * L_PS + L_PS));
      SB();
      if (!i0 && WPOS == 1) wissue();
#pragma unroll
      for (int i = 0; i < NT; ++i) { yr[i] = mma(a[i], fa_r, z4); yi[i] = mma(a[i], fa_i, z4); }
      SB();
#pragma unroll
      for (int i = 0; i < NT; ++i) { a[i] = pk8(yr[i] * t1c - yi[i] * t1s, yr[i] * t1s + yi[i] * t1c); yr[i] = mma(fb_r, a[i], z4); yi[i] = mma(fb_i, a[i], z4); SB(); }
#pragma unroll
      for (int i = 0; i < NT; ++i) asm volatile("" :: "v"(yr[i]), "v"(yi[i]));
      if (!i0) { WAITV(4 * NC0); gbar(L + L_SYNC + 16, w, 4); }
      if (!i0) TX(9);
#pragma unroll
      for (int i = 0; i < NT; ++i) {
        if (i0) {
#pragma unroll
          for (int r = 0; r < 4; ++r) fr[i][r] = fi[i][r] = er[i] = ei[i] = 0.f;
        }
        ldhi<0>(fr[i][0], wa[i0 + i]); ldhi<128>(fr[i][1], wa[i0 + i]); ldhi<256>(fr[i][2], wa[i0 + i]); ldhi<384>(fr[i][3], wa[i0 + i]);
        ldhi<L_WS>(fi[i][0], wa[i0 + i]); ldhi<L_WS + 128>(fi[i][1], wa[i0 + i]); ldhi<L_WS + 256>(fi[i][2], wa[i0 + i]); ldhi<L_WS + 384>(fi[i][3], wa[i0 + i]);
        ldhi<0>(er[i], wo[i0 + i]); ldhi<L_WS>(ei[i], wo[i0 + i]);
      }
      if (!i0 && CPOS == 3) gwait(L + L_SYNC, 2); else WAITL(0);
#pragma unroll
      for (int i = 0; i < NT; ++i)
#pragma unroll
        for (int r = 0; r < 4; ++r) { PIN(fr[i][r]); PIN(fi[i][r]); if (r == 0) { PIN(er[i]); PIN(ei[i]); } }
      if (cc == 0) {
        const float b0 = lane == 0 ? 0.5f : 0.f, b3 = lane == 32 ? 0.5f : 0.f;
#pragma unroll
        for (int i = 0; i < NT; ++i) {
          float t;
          asm volatile("v_sub_f32 %4, %5, %0\n\tv_fmac_f32 %0, %7, %4\n\tv_add_f32 %4, %6, %1\n\tv_fma_f32 %1, -%7, %4, %1\n\t"
                       "v_sub_f32 %4, %5, %2\n\tv_fmac_f32 %2, %8, %4\n\tv_add_f32 %4, %6, %3\n\tv_fma_f32 %3, -%8, %4, %3"
                       : "+v"(fr[i][0]), "+v"(fi[i][0]), "+v"(fr[i][3]), "+v"(fi[i][3]), "=&v"(t) : "v"(er[i]), "v"(ei[i]), "v"(b0), "v"(b3));
        }
      }
      if (__builtin_expect(k1 == 0, 0)) {
#pragma unroll
        for (int i = 0; i < NT; ++i) { const float wt_ = (float)tile_desc(w + 4 * (i0 + i), 0, kd1).w; yr[i] *= wt_; yi[i] *= wt_; }
      }
      SB();
#pragma unroll
      for (int i = 0; i < NT; ++i) {
        const f32x4 vr = {fr[i][0], fr[i][1], fr[i][2], fr[i][3]}, vi = {fi[i][0], fi[i][1], fi[i][2], fi[i][3]};
        a[i] = pk8(yr[i] * vr - yi[i] * vi, yr[i] * vi + yi[i] * vr); yr[i] = mma(a[i], gb_r, z4); yi[i] = mma(a[i], gb_i, z4);
        SB();
        if (!i0 && CPOS == 3) { c_piece(2 + (i >> 1), 2 * (i & 1)); c_piece(2 + (i >> 1), 2 * (i & 1) + 1); }
        SB();
      }
#pragma unroll
      for (int i = 0; i < NT; ++i) {
        a[i] = pk8(yr[i] * t2c - yi[i] * t2s, yr[i] * t2s + yi[i] * t2c); yr[i] = mma(a[i], ga_r, z4); yi[i] = mma(a[i], ga_i, z4);
        SB();
      }
#pragma unroll
      for (int i = 0; i < NT; ++i) { lw<u32x2>(pw_ + i * 8 * L_PS, u32x2{pk2(yr[i].xy), pk2(yr[i].zw)}); lw<u32x2>(pw_ + i * 8 * L_PS + L_PS, u32x2{pk2(yi[i].xy), pk2(yi[i].zw)}); }
    };
    tiles(IC<4>{}, IC<0>{});
    if (__builtin_expect(sp0 && w < 2, 0)) tiles(IC<1>{}, IC<4>{});
    TX(23);
    gbar(L + L_SYNC + 16, w, 5);
    TX(24);
    u32x4 tq[4];
    {
      f32x4 o[2][2][2];
      const u32 ap = vo + (4 * g + q) * L_PS + 16 * pq;
#pragma unroll
      for (int cb = 0; cb < 2; ++cb) { a0[cb][0] = cat(trr(ap + 64 * cb), trr(ap + 64 * cb + 16 * L_PS)); a0[cb][1] = cat(trr(ap + 64 * cb + 8), trr(ap + 64 * cb + 16 * L_PS + 8)); }
#pragma unroll
      for (int i = 0; i < 8; ++i) o[i >> 2][(i >> 1) & 1][i & 1] = mma(a0[(i >> 1) & 1][i & 1], mi[i >> 2], z4);
      if (__builtin_expect(sp0, 0)) {
        const u32 ax = vo + (32 + q) * L_PS + 16 * pq;
#pragma unroll
        for (int i = 0; i < 4; ++i) { const u32x2 t = trr(ax + 64 * (i >> 1) + 8 * (i & 1)); const bf16x8 a = cat(t, t); o[0][i >> 1][i & 1] = mma(a, mix[0], o[0][i >> 1][i & 1]); o[1][i >> 1][i & 1] = mma(a, mix[1], o[1][i >> 1][i & 1]); }
      }
      FENCE();
#pragma unroll
      for (int i = 0; i < 4; ++i) lw<bf16x8>(vo + i * 1280 + l15 * 80 + 16 * g, pk8(o[i >> 1][i & 1][0], o[i >> 1][i & 1][1]));
      FENCE();
#pragma unroll
      for (int i = 0; i < 4; ++i) tq[i] = lr<u32x4>(vo + i * 1280 + (lane >> 2) * 80 + pq * 16);
#pragma unroll
      for (int i = 0; i < 4; ++i) PIN(tq[i]);
    }
    TX(10);
    u32x4 tab[2][4];
    if (__builtin_expect(bid == 0 && w == 0, 0)) st2c(tag, wsr, lane == 0 ? WS_E : WS_END, 0);
#pragma unroll
    for (int i = 0; i < 16; ++i) { const u32 xv = sk[i >> 1][i & 1], dv = q_ds[i & 1]; sk[i >> 1][i & 1] = pk2(bf2f(xv & 0xffffu) * bf2f(dv & 0xffffu), bfhi(xv) * bfhi(dv)); PIN(sk[i >> 1][i & 1]); }
    TX(11);
    const u32 hb = WS_H + (u32)(tt * 8 + 2 * w) * 65536u + ((ct & PX) ? 32768 : 0);
    u32 fa_ = lane < 32 ? (4 * w + 2 * (lane & 1) + (sh ^ (lane & 1))) * 128 + ORD2(lane >> 1) * 8 : WS_END, miss; PIN(fa_);
    u64 fh;
    auto fload = [&]() { asm volatile("buffer_load_dwordx2 %0, %1, %2, %3 offen sc1" : "=v"(fh) : "v"(fa_), "s"(wsr), "s"(WS_FH + rep) : "memory"); };
    auto fneed = [&](u32 bits) {
      if (__builtin_expect((miss & bits) != 0, 0)) {
        NOUNROLL for (u32 spn = 0; spn < SPIN_CAP; ++spn) {
          u64 a; asm volatile("buffer_load_dwordx2 %0, %1, %2, %3 offen sc1\n\ts_waitcnt vmcnt(0)" : "=&v"(a) : "v"(fa_), "s"(wsr), "s"(WS_FH + rep) : "memory");
          miss = (u32)__builtin_amdgcn_ballot_w64(a != tag);
          if (!(miss & bits)) break;
          __builtin_amdgcn_s_sleep(POLLSLP);
        }
      }
    };
    auto h_issue = [&](int u) {
      const u32 so = hb + ORD2(u) * 2048;
#pragma unroll
      for (int m = 0; m < 4; ++m) bldsc(wsr, vl, so + (m >> 1) * 65536 + (m & 1) * 1024, L + w * 4096, 98304 + (u & 3) * 16384 + m * 1024);
    };
    auto extras = [&]() {
      const u32 to = (u32)lr8 * (16 * 32768) + (u32)(2 * k1 + kd1) * 128 + lc8 * 16;
#pragma unroll
      for (int e = 0; e < 4 * NE; ++e) if (!TFIRST) c_piece(4 + (e >> 2), e & 3);
#pragma unroll
      for (int i = 0; i < 4; ++i) st4c(tq[i], wsr, to, WS_T + (8 * (i >> 1) + 2 * w + (i & 1)) * 32768);
#pragma unroll
      for (int e = 0; e < 4 * NE; ++e) if (TFIRST) c_piece(4 + (e >> 2), e & 3);
    };
    WAITV(0); BAR();
    TX(25);
    extras();
    fload();
    SB();
    {
      const float tk = (float)(2 * kap + 1) * (1.f / 512.f), tc = 2.f * cosr(4.f * tk);
#pragma unroll
      for (int i = 0; i < 8; ++i) {
        const float kk = (i & 4) ? -tk : tk;
        tab[i >> 2][i & 3] = cos8(fmaf(kk, 8.f * fg + (float)(i & 3), -0.125f), kk, tc); PIN(tab[i >> 2][i & 3]);
      }
    }
    TX(26);
    WAITV(0); PIN(fh); miss = (u32)__builtin_amdgcn_ballot_w64(fh != tag);
    fneed(FMASK); h_issue(0); h_issue(1); h_issue(2);
    SB();
    u32 ta[8];
#pragma unroll
    for (int i = 0; i < 8; ++i) {
      const int kb = 16 * i + 4 * w + g, k = (lane & 4) ? (kb ? 256 - kb : 128) : kb, rho = 64 * (k & 3) + (k >> 2);
      ta[i] = L + rho * 128 + 16 * ((((lane >> 1) & 4) | pq) ^ (((rho >> 1) & 1) | ((rho >> 3) & 1) << 1)); PIN(ta[i]);
    }
    WAITV(8);
    TX(27);
    u64 ft;
    u32x4 tv[8], gv[2];
    f32x2 o[8][2];
    sfor<0, 16>([&](auto u_) {
      constexpr int u = decltype(u_)::v;
      if (u) WAITV(g2_wait(u));
      BAR();
      ITS(2, u);
      PINS(rot2);
      if (u == FTU && w == 0) st2c(tag, wsr, fo, WS_FT);
      if (u == 3 || u == 6) { PIN(fh); miss &= (u32)__builtin_amdgcn_ballot_w64(fh != tag); }
      if constexpr (u + 3 < 16) fneed(3u << (2 * u + 6));
      if (u == 0 || u == 3) fload();
#pragma unroll
      for (int e = g2_c0(u); e < g2_c0(u + 1); ++e) c_piece(e >> 2, e & 3);
      if (u + 3 < 16) h_issue(u + 3);
      if constexpr (u >= TU && u < TU + 4) {
#pragma unroll
        for (int i = 2 * (u - TU); i < 2 * (u - TU) + 2; ++i) tv[i] = bl4c<0>(wsr, vl, WS_T + (s2 * 16 + ct) * 32768 + i * 4096 + w * 1024);
        if (u < TU + 2) gq[(u - TU) & 1] = bl1c(wsr, vGo + 128 * (u - TU), 0); else if (u == TU + 2) gv[0] = bl4c<0>(wsr, WS_G + tt * 512 + l15 * 16, 0); else gv[1] = bl4c<256>(wsr, WS_G + tt * 512 + (15 - l15) * 16, 0);
      }
      if (u == TU - 5) asm volatile("buffer_load_dwordx2 %0, %1, %2, %3 offen sc1" : "=v"(ft) : "v"(lane * 8), "s"(wsr), "s"(WS_FT + rep + w * 512) : "memory");
      if (u == TU - 1) { PIN(ft); if (__builtin_expect(!__all(ft == tag), 0)) pollq(wsr, WS_FT + rep + w * 512, lane, tag); }
      SB();
    });
    WAITV(0);
#pragma unroll
    for (int i = 0; i < 8; ++i) { PIN(tv[i]); lw<u32x4>(ta[i], tv[i]); }
    LBAR();
    hart256<true>(o, L + (8 * g + q) * 128 + 16 * ((4 * (pq >> 1) + w) ^ (((q >> 1) & 1) | ((g & 1) << 1))) + 8 * (pq & 1), tab, kap, false);
    PIN(gq[0]); PIN(gq[1]); PIN(gv[0]); PIN(gv[1]);
#pragma unroll
    for (int i = 0; i < 8; ++i) {
      const float gg = __uint_as_float(gv[i >> 2][i < 4 ? i : (i + 3) & 3]), og = rbf(1.f - gg);
      const int pos = i < 4 ? l15 : 15 - l15;
#pragma unroll
      for (int hf = 0; hf < 2; ++hf)
        lw<f32x2>(L + 65536 + pos * 256 + 16 * ((8 * hf + 2 * w + (g & 1)) ^ pos) + 8 * (g >> 1) + 4096 * (i < 4 ? 2 * i : 2 * ((i + 3) & 3) + 1), fma2(f32x2{og, og}, f32x2{bf2f(sk[i][hf] & 0xffffu), bfhi(sk[i][hf])}, gg * o[i][hf]));
    }
    TX(30);
  } else {
    GETARGS();
    lw<u32>(L + L_SYNC + 4 * wv, 0u);
    const int ac = lc8 ^ (((lr8 >> 1) & 1) | ((w & 1) << 1)), r0 = 2 * sh + (ct & 1);
    const u32 vA = (u32)(s2 + 32 * (8 * w + lr8)) * 2048u + 64 * ct + 16 * (ac & 3) + 1024 * (ac >> 2);
    ARGS();
    BAR();
    const int wn = w & 1, wm = w >> 1;
    const int hpos = 8 * wm + lc8, hsh = (tt ^ (tt >> 1)) & 1;
    u32 vH = (u32)(lr8 & 1) * (1u << 20) + hsh * (1u << 19) + (tt >> 1) * 65536 + (2 * pt + wn) * 1024 + g * 256 + hpos * 16;
    const u32 vAg = WS_AGG + (u32)((pt * 2 + wn) * 16 + l15) * 16;
    const int kap = 16 * sh + l15, np = (16 - l15) & 15; const bool sp = kap == 0;
    const int ecol = 8 * w + 4 * (g & 1) + 2 * (g >> 1);
    TS(19);
    u32x4 tab[2][4];
    hart_table(tab, kap, sp, g);
#pragma unroll
    for (int i = 0; i < 8; ++i) PIN(tab[i >> 2][i & 3]);
    u32 ua[6], hap = L + L_RA + (8 * g + q) * 128 + 16 * ((4 * (pq >> 1) + w) ^ (((q >> 1) & 1) | ((g & 1) << 1))) + 8 * (pq & 1);
#pragma unroll
    for (int i = 0; i < 2; ++i) {
      const int lo = i ? np : l15;
      ua[i] = L + L_V + lo * 256 + ((2 * ecol) ^ ((lo & 7) << 4)); PIN(ua[i]);
    }
    ua[2] = (ua[1] ^ 64) + (sp ? 0 : 48 * 256); ua[3] = (ua[1] ^ 64) + (sp ? 32 * 256 : 16 * 256); ua[4] = (ua[0] ^ 64) + (sp ? 16 * 256 : 0);
    PIN(ua[2]); PIN(ua[3]); PIN(ua[4]); PIN(hap);
    c2 lam, coef, l8, l16;
    gwait(L + L_SYNC + 16, 1);
    {
      const u32 qa = L + L_V + 16384 + w * 1024 + lane * 4, q_dt = lr<u16>(qa), q_lr = lr<u16>(qa + 256), q_li = lr<u16>(qa + 512);
      const float lre = bf2f(q_lr), lim = bf2f(q_li);
      const float dt = rbf(expf(bf2f(q_dt))), mg = expf(dt * lre);
      float sn, cs; sincosf(dt * lim, &sn, &cs);
      const float nr = mg * cs - 1.f, ni = mg * sn;
      const bool big = fabsf(lre) >= fabsf(lim);
      const float u = big ? lre : lim, v = big ? lim : lre, t = v * __builtin_amdgcn_rcpf(u), rd = 2.f * __builtin_amdgcn_rcpf(u + v * t);
      lam = {spread16(mg * cs), spread16(ni)};
      coef = {spread16(rd * (big ? nr + ni * t : nr * t + ni)), spread16(rd * (big ? ni - nr * t : ni * t - nr))};
      l8 = csq(csq(csq(lam))); l16 = csq(l8);
      pinc(lam); pinc(coef); pinc(l8); pinc(l16);
    }
    TS(22);
#pragma unroll
    for (int i = 2; i < 8; ++i) issue_a(i >> 1, i & 1);
    SB();
    SB();
    c2 pw[8], wt[4], lsa, lsb, l32 = csq(l16), l64 = csq(l32);
    u32 wa[8], ra[8], pa[4], vAs = lane < 16 ? vAg : WS_END, sa = L + L_SEG + wn * 256 + l15 * 16;
    {
      const c2 one = {{1.f, 1.f}, {0.f, 0.f}}, zero = {{0.f, 0.f}, {0.f, 0.f}};
      const c2 l128 = csq(l64), l256 = csq(l128), l512 = csq(l256);
      lsa = cmul(cmul(coef, csel(g & 1, l16, one)), cmul(csel(g & 2, l32, one), csel(wm, l64, one))); lsb = cmul(lsa, l8);
      wt[0] = cmul(csel(g & 1, l128, one), csel(g & 2, l256, one));
#pragma unroll
      for (int n = 1; n < 4; ++n) wt[n] = cmul(wt[n - 1], l512);
#pragma unroll
      for (int n = 0; n < 4; ++n) { wt[n] = csel(tt - 1 - g - 4 * n >= 0, wt[n], zero); pinc(wt[n]); pa[n] = vAg + max(tt - 1 - g - 4 * n, 0) * 8192; PIN(pa[n]); }
      pw[0] = lam; pw[1] = csq(lam); pw[2] = cmul(pw[1], lam); pw[3] = csq(pw[1]); pw[4] = cmul(pw[3], lam); pw[5] = csq(pw[2]); pw[6] = cmul(pw[3], pw[2]); pw[7] = l8;
#pragma unroll
      for (int s = 1; s < 7; ++s) pinc(pw[s]);
      pinc(lsa); pinc(lsb); pinc(l32); pinc(l64); PIN(vH); PIN(vAs); PIN(sa);
    }
      u32 aa[2][2], ab[2][2];
#pragma unroll
      for (int i = 0; i < 4; ++i) {
        const int co = 16 * ((4 * (i & 1) + g) ^ (l15 >> 1)) + (i >> 1) * 65536;
        aa[i & 1][i >> 1] = L + (64 * wm + l15) * 128 + co; ab[i & 1][i >> 1] = L + 16384 + (64 * wn + l15) * 128 + co; PIN(aa[i & 1][i >> 1]); PIN(ab[i & 1][i >> 1]);
      }
    WAITV(0);
    gwait(L + L_SYNC + 16, 2);
    gbar(L + L_SYNC, w, 1);
    TS(1);
    {
      f32x2 o[8][2];
      hart256<false>(o, hap, tab, kap, sp);
#pragma unroll
      for (int i = 0; i < 8; ++i) {
        const u32 a = i == 0 ? ua[0] : i == 1 ? ua[0] + 32 * 256 : i == 2 ? ua[2] : i == 3 ? ua[3] : i == 4 ? ua[4] : i == 5 ? ua[1] + 16 * 256 : i == 6 ? ua[1] + 48 * 256 : ua[4] + 32 * 256;
        lw<u32>(a, pk2(o[i][0])); lw<u32>(a + 128, pk2(o[i][1]));
      }
    }
    u64 tag;
    f32x4 acc[4][4];
    {
      bf16x8 fa[2][4], fb[2][4];
      auto rd1 = [&](int j, int ks, int i) {
        const int sl = (j + 3) & 3; const u32 bs = (sl & 1) * 32768 + (i >> 1) * 2048;
        if (i & 1) fb[ks][i >> 1] = lr<bf16x8>(ab[ks][sl >> 1] + bs); else fa[ks][i >> 1] = lr<bf16x8>(aa[ks][sl >> 1] + bs);
      };
      LBAR();
      TS(2);
      u64 cy0 = 0; CY0();
#pragma unroll
      for (int i = 0; i < 8; ++i) rd1(0, 0, i);
#pragma unroll
      for (int j = 0; j < 16; ++j) {
#pragma unroll
        for (int ks = 0; ks < 2; ++ks) {
          const bool nx = ks == 0 || j < 15;
          if (ks == 1 && j < 17 - LEAD) LBAR();
          SB();
#pragma unroll
          for (int qq = 0; qq < 16; ++qq) {
            const int m = qq >> 2, n = (m & 1) ? 3 - (qq & 3) : (qq & 3);
            if (j | ks) mmc(acc[m][n], fa[ks][m], fb[ks][n]); else mmz(acc[m][n], fa[ks][m], fb[ks][n]);
            SB();
            if (nx && qq < 8) rd1(j + ks, ks ^ 1, qq);
            SB();
          }
        }
      }
      NOPS();
      CY1(20);
    }
    TS(3);
    c2 h[2][8];
#pragma unroll
    for (int s = 0; s < 8; ++s) {
#pragma unroll
      for (int ps = 0; ps < 2; ++ps) {
        const f32x4 vr = acc[s >> 1][2 * ps], vi = acc[s >> 1][2 * ps + 1];
        h[ps][s] = (s & 1) ? c2{vr.zw, vi.zw} : c2{vr.xy, vi.xy};
        if (s) { const c2 a = bc(lam, ps); h[ps][s].r = fma2(-a.i, h[ps][s - 1].i, h[ps][s].r); h[ps][s].i = fma2(a.i, h[ps][s - 1].r, h[ps][s].i); }
      }
      SB();
#pragma unroll
      for (int ps = 0; ps < 2; ++ps) if (s) { const c2 a = bc(lam, ps); h[ps][s].r = fma2(a.r, h[ps][s - 1].r, h[ps][s].r); h[ps][s].i = fma2(a.r, h[ps][s - 1].i, h[ps][s].i); }
      SB();
    }
    const c2 ea = {{h[0][7].r.x, h[1][7].r.x}, {h[0][7].i.x, h[1][7].i.x}};
    { const c2 e = cfma(l8, ea, c2{{h[0][7].r.y, h[1][7].r.y}, {h[0][7].i.y, h[1][7].i.y}});
      lw<f32x4>(sa + (4 * wm + g) * 512, f32x4{e.r.x, e.r.y, e.i.x, e.i.y}); }
    gbar(L + L_SYNC, w, 2);
    u32x4 gx[4], gw[2];
    c2 cin;
    {
      c2 e[8];
#pragma unroll
      for (int j = 0; j < 8; ++j) e[j] = asc(lr<f32x4>(sa + j * 512));
      SB();
      const c2 e01 = cfma(l16, e[0], e[1]), e23 = cfma(l16, e[2], e[3]), e45 = cfma(l16, e[4], e[5]), e67 = cfma(l16, e[6], e[7]);
      const c2 e03 = cfma(l32, e01, e23), e47 = cfma(l32, e45, e67), tot = cfma(l64, e03, e47);
      st4c(__builtin_bit_cast(u32x4, f32x4{tot.r.x, tot.r.y, tot.i.x, tot.i.y}), wsr, vAs, wm ? tt * 8192 : WS_END);
      SB();
      const c2 zero = {{0.f, 0.f}, {0.f, 0.f}};
      const c2 c0 = csel(wm, e03, zero), c2_ = cfma(l32, c0, csel(wm, e45, e01)), cb = csel(g & 2, c2_, c0);
      cin = csel(g & 1, cfma(l16, cb, csel(wm, csel(g & 2, e[6], e[4]), csel(g & 2, e[2], e[0]))), cb);
    }
    pinc(cin);
    TS(4);
    SB();
      { const u32 xo = (bid * 8 + 2 * w) * 2048, vl = lane * 16; const rsrc_t gr = mkr((const void*)kb_[3], 2048);
        gx[0] = bl4<0>(xr, vl, xo); gx[1] = bl4<1024>(xr, vl, xo); gx[2] = bl4<2048>(xr, vl, xo); gx[3] = bl4<3072>(xr, vl, xo); gw[0] = bl4<0>(gr, vl, 0); gw[1] = bl4<1024>(gr, vl, 0); }
    SB();
    c2 ca, cz; u32x4 v[4];
    {
#pragma unroll
      for (int s = 0; s < 8; ++s) {
        f32x2 t[4];
#pragma unroll
        for (int ps = 0; ps < 2; ++ps) { const c2 a = bc(coef, ps); t[2 * ps] = a.r * h[ps][s].r; t[2 * ps + 1] = a.i * h[ps][s].r; }
        SB();
#pragma unroll
        for (int ps = 0; ps < 2; ++ps) { const c2 a = bc(coef, ps); h[ps][s].r = fma2(-a.i, h[ps][s].i, t[2 * ps]); h[ps][s].i = fma2(-a.r, h[ps][s].i, -t[2 * ps + 1]); }
        SB();
      }
#pragma unroll
      for (int s = 0; s < 16; ++s) pinc(h[s & 1][s >> 1]);
#pragma unroll
      for (int n = 0; n < 4; ++n) v[n] = bl4c<0>(wsr, pa[n], 0);
      if (CPOS == 2) {
#pragma unroll
        for (int e = 4 * NC0; e < 16; ++e) c_piece(e >> 2, e & 3);
      }
      SB();
      ca = cmul(coef, cin); cz = cmul(coef, cfma(l8, cin, ea));
      pinc(ca); pinc(cz);
      const u32 hs = L + 65536 + w * 8192;
#pragma unroll
      for (int i = 0; i < 8; ++i) {
        wa[i] = hs + l15 * 128 + ((4 * g + 16 * i) ^ (8 * (l15 & 1) + 4 * (l15 & 4) + 16 * (l15 & 2) + 8 * (l15 & 8))); PIN(wa[i]);
        ra[i] = hs + (8 * g + q) * 128 + ((8 * pq) ^ (8 * (q & 1) + 16 * (q & 2) + 64 * (g & 1)) ^ (16 * i)); PIN(ra[i]);
      }
    }
    {
      const u32 vG = WS_G + (u32)(((2 * w + (lane & 1)) * 2 + (((bid >> 4) ^ (bid >> 5)) & 1)) * 128 + ((bid >> 5) & 1) * 64 + (bid & 15) * 4 + (bid >> 6)) * 4;
      float sm[2] = {0.f, 0.f};
      WAITV(CPOS == 2 ? 20 - 4 * NC0 : 4); PIN(gw[0]); PIN(gw[1]);
#pragma unroll
      for (int i = 0; i < 4; ++i) {
        PIN(gx[i]);
#pragma unroll
        for (int e = 0; e < 4; ++e) { const u32 a = gx[i][e], b = gw[i & 1][e]; sm[i >> 1] = __builtin_amdgcn_fdot2_f32_bf16(__builtin_bit_cast(bf16x2, a), __builtin_bit_cast(bf16x2, b), sm[i >> 1], false); }
      }
      sm[0] = dppadd<0xB1>(sm[0]); sm[1] = dppadd<0xB1>(sm[1]);
      float t = (lane & 1) ? sm[1] : sm[0];
      t = dppadd<0x4E>(t); t = dppadd<0x124>(t); t = dppadd<0x128>(t); t = sum32(sum16(t));
      __builtin_amdgcn_raw_buffer_store_b32(__float_as_uint(rbf(1.f / (1.f + expf(-rbf(t))))), wsr, lane < 2 ? vG : WS_END, 0, 16);
    }
    SB();
    TS(5);
    WAITV(CPOS == 2 ? 17 - 4 * NC0 : 1);
    if (tt > 0) {
      auto vok = [&]() { u32 mx = 0;
#pragma unroll
        for (int n = 0; n < 4; ++n) { PIN(v[n]); mx = umax3(umax3(mx, v[n].x, v[n].y), v[n].z, v[n].w); }
        return __all(mx != SENT); };
      if (!vok()) {
        NOUNROLL for (u32 spn = 0; spn < SPIN_CAP; ++spn) {
#pragma unroll
          for (int n = 0; n < 4; ++n) v[n] = bl4c<0>(wsr, pa[n], 0);
          WAITV(0);
          if (vok()) break;
        }
      }
      c2 cb = cfma(wt[0], asc(__builtin_bit_cast(f32x4, v[0])), cmul(wt[1], asc(__builtin_bit_cast(f32x4, v[1]))));
      const c2 cd = cfma(wt[2], asc(__builtin_bit_cast(f32x4, v[2])), cmul(wt[3], asc(__builtin_bit_cast(f32x4, v[3]))));
      cb.r += cd.r; cb.i += cd.i;
      cb = c2{{sum32(sum16(cb.r.x)), sum32(sum16(cb.r.y))}, {sum32(sum16(cb.i.x)), sum32(sum16(cb.i.y))}};
      ca = cfma(lsa, cb, ca); cz = cfma(lsb, cb, cz);
    } else {
#pragma unroll
      for (int n = 0; n < 4; ++n) PIN(v[n]);
    }
    TS(6);
    {
      const c2 cc[2] = {{{ca.r.x, cz.r.x}, {ca.i.x, cz.i.x}}, {{ca.r.y, cz.r.y}, {ca.i.y, cz.i.y}}};
      u32x4 hq[8];
#pragma unroll
      for (int s = 0; s < 8; ++s) {
#pragma unroll
        for (int ps = 0; ps < 2; ++ps) { const c2 a = bc(pw[s], ps); h[ps][s].r = fma2(-a.i, cc[ps].i, h[ps][s].r); h[ps][s].i = fma2(-a.i, cc[ps].r, h[ps][s].i); }
        SB();
#pragma unroll
        for (int ps = 0; ps < 2; ++ps) { const c2 a = bc(pw[s], ps); h[ps][s].r = fma2(a.r, cc[ps].r, h[ps][s].r); h[ps][s].i = fma2(-a.r, cc[ps].i, h[ps][s].i); }
        SB();
#pragma unroll
        for (int ps = 0; ps < 2; ++ps) { lw<u32>(wa[s] + ps * 2048, pk2(h[ps][s].r)); lw<u32>(wa[s] + ps * 2048 + 4096, pk2(h[ps][s].i)); }
        FENCE();
        if (s & 1) {
          hq[s - 1] = __builtin_bit_cast(u32x4, cat(trr(ra[s - 1]), trr(ra[s] + 512))); hq[s] = __builtin_bit_cast(u32x4, cat(trr(ra[s - 1] + 4096), trr(ra[s] + 4608)));
          if (s > 1) { st4c(hq[s - 3], wsr, vH, WS_H + ((s - 3) >> 1) * (2u << 20)); st4c(hq[s - 2], wsr, vH, WS_H + ((s - 3) >> 1) * (2u << 20) + 32768); }
        }
        SB();
      }
      st4c(hq[6], wsr, vH, WS_H + 3 * (2u << 20)); st4c(hq[7], wsr, vH, WS_H + 3 * (2u << 20) + 32768);
    }
    TS(7);
    {
      bf16x8 fh[2][4], fc[2][4], cp[6][4];
      u32 ha = L + 98304 + wm * 8192 + lane * 16, ca[2][2];
#pragma unroll
      for (int i = 0; i < 4; ++i) { ca[i & 1][i >> 1] = L + (i >> 1) * 65536 + wn * 8192 + l15 * 128 + 16 * ((4 * (i & 1) + g) ^ (l15 >> 1)); PIN(ca[i & 1][i >> 1]); }
      PIN(ha);
      auto rd2 = [&](int u, int ks, int i) {
        if (i & 1) { if (u >= 3) fc[ks][i >> 1] = lr<bf16x8>(ca[ks][(u % 6) >> 2] + ((u % 6) & 3) * 16384 + (i >> 1) * 2048); } else fh[ks][i >> 1] = lr<bf16x8>(ha + (u & 3) * 16384 + (i >> 1) * 2048 + ks * 1024);
      };
      tag = lr<u64>(L + L_SYNC + 32); PIN(tag);
      if (CPOS == 0) {
#pragma unroll
        for (int e = 4 * NC0; e < 16; ++e) c_piece(e >> 2, e & 3);
        WAITV(16 - 4 * NC0);
      } else WAITV(0);
      gbar(L + L_SYNC, w, 3);
      if (w == 0) st2c(tag, wsr, lane < NREP ? lane * 2048 : WS_END, WS_FH + (tt * 16 + pt) * 8);
      TS(28);
      BAR();
      if (CPOS == 1) {
#pragma unroll
        for (int e = 4 * NC0; e < 16; ++e) c_piece(e >> 2, e & 3);
      }
      if (CPOS != 3) { WAITV(0); gbar(L + L_SYNC, w, 4); }
#pragma unroll
      for (int i = 0; i < 24; ++i) cp[i >> 2][i & 3] = lr<bf16x8>(ca[(i >> 2) & 1][0] + (i >> 3) * 16384 + (i & 3) * 2048);
      LBAR();
#pragma unroll
      for (int i = 0; i < 24; ++i) PIN(cp[i >> 2][i & 3]);
      TS(12);
      u64 cy0 = 0; CY0();
#pragma unroll
      for (int i = 0; i < 8; ++i) rd2(0, 0, i);
#pragma unroll
      for (int u = 0; u < 16; ++u) {
#pragma unroll
        for (int ks = 0; ks < 2; ++ks) {
          if (ks == 1 && u < 15) LBAR();
          SB();
#pragma unroll
          for (int qq = 0; qq < 16; ++qq) {
            const int m = qq >> 2, n = (m & 1) ? 3 - (qq & 3) : (qq & 3);
            if (u | ks) mmc(acc[m][n], u < 3 ? cp[(2 * u + ks) % 6][n] : fc[ks][n], fh[ks][m]); else mmz(acc[m][n], cp[0][n], fh[ks][m]);
            SB();
            if ((ks == 0 || u < 15) && qq < 8) rd2(u + ks, ks ^ 1, qq);
            SB();
          }
        }
      }
      NOPS();
      CY1(21);
    }
    TS(13);
    {
      const u32 vl = lane * 16;
#pragma unroll
      for (int m = 0; m < 4; ++m) st4c(__builtin_bit_cast(u32x4, pk8(acc[m][2], acc[m][3])), wsr, g * 256 + (l15 ^ (4 * g)) * 16, WS_X + (((bid ^ (PX == 8 ? 1 : 32)) * 4 + w) * 4 + m) * 1024);
      gq[0] = bl1c(wsr, vGo, 0); gq[1] = bl1c(wsr, vGo + 128, 0);
      if (!BLATE) BAR();
      WAITV(2);
      st2c(tag, wsr, lane == 0 ? 0 : WS_END, WS_FP + ((bid ^ (PX == 8 ? 1 : 32)) * 4 + w) * 128);
      if (BLATE) BAR();
      TS(29);
      const u32 pa = L + 98304 + wm * 16384 + wn * 2048 + g * 256 + (l15 ^ (4 * g)) * 16;
#pragma unroll
      for (int i = 0; i < 8; ++i) lw<f32x4>(pa + (i >> 1) * 4096 + (i & 1) * 1024, acc[i >> 1][i & 1]);
      NOUNROLL for (u32 spn = 0; spn < SPIN_CAP; ++spn) {
        u64 a; asm volatile("buffer_load_dwordx2 %0, off, %1, %2 sc1\n\ts_waitcnt vmcnt(0)" : "=&v"(a) : "s"(wsr), "s"(WS_FP + (bid * 4 + w) * 128) : "memory");
        if (__all(a == tag)) break;
      }
#pragma unroll
      for (int m = 0; m < 4; ++m) bldsc(wsr, vl, WS_X + (bid * 4 + w) * 4096 + m * 1024, L + w * 4096, 131072 + m * 1024);
      PIN(gq[0]); PIN(gq[1]);
      TS(31);
    }
  }
  if (!rh) TS(14);
  {
    const rsrc_t outr = mkr((const void*)outp, 8u << 20);
    u32 pa[2], xa[2], ba[4], oa[2]; float og[2];
#pragma unroll
    for (int hi = 0; hi < 2; ++hi) {
      const int row = lr8 + 8 * hi, s1 = 32 * wv + row + 16 * (sh ^ (w & 1));
      pa[hi] = L + 98304 + wv * 4096 + lc8 * 256 + (row ^ (4 * pq)) * 16; oa[hi] = (u32)(s2 + 8 * s1) * 4096u + lc8 * 16; og[hi] = rbf(1.f - __uint_as_float(gq[hi]));
      ba[2 * hi] = L + 65536 + (wv * 16 + row) * 256 + 16 * (lc8 ^ row); ba[2 * hi + 1] = ba[2 * hi] ^ 128;
      xa[hi] = L + 131072 + rh * 8192 + w * 1024 + pq * 256 + (row ^ (4 * pq)) * 16 + (lc8 >> 2) * 8; PIN(xa[hi]);
      PIN(pa[hi]); PIN(oa[hi]); PIN(og[hi]); PIN(ba[2 * hi]); PIN(ba[2 * hi + 1]);
    }
    WAITV(0); LBAR();
    f32x4 v[4][2]; u32x2 xv[4];
#pragma unroll
    for (int i = 0; i < 4; ++i) {
      v[i][0] = lr<f32x4>(pa[i >> 1] + (i & 1) * 2048); v[i][1] = lr<f32x4>(ba[i]); xv[i] = lr<u32x2>(xa[i >> 1] + (i & 1) * 4096);
    }
    SB();
#pragma unroll
    for (int i = 0; i < 4; ++i)
      st4(__builtin_bit_cast(u32x4, og[i >> 1] * (v[i][0] + f32x4{bf2f(xv[i].x & 0xffffu), bfhi(xv[i].x), bf2f(xv[i].y & 0xffffu), bfhi(xv[i].y)}) + v[i][1]), outr, oa[i >> 1], 128 * ct + 2048 * (i & 1));
  }
  if (!rh) TS(15);
}

extern "C" void kernel_launch(void* const* d_in, const int* in_sizes, int n_in, void* d_out, int out_size, void* d_ws, size_t ws_size, hipStream_t stream) {
  (void)in_sizes; (void)n_in; (void)out_size; (void)ws_size;
  (void)hipFuncSetAttribute((const void*)k_all, hipFuncAttributeMaxDynamicSharedMemorySize, L_TOT);
  const u16* const* in = (const u16* const*)d_in;
  k_all<<<256, 512, L_TOT, stream>>>(in[0], in[6], in[7], in[10], in[4], in[5], in[1], in[2], in[8], in[9], in[11], in[12], (char*)d_ws, (float*)d_out);
}
```

```cpp
#include <hip/hip_runtime.h>
#include <hip/hip_bf16.h>
#include <stdint.h>

typedef unsigned short u16;
typedef unsigned int u32;
typedef unsigned long long u64;
typedef __attribute__((ext_vector_type(4))) short s16x4;
typedef __attribute__((ext_vector_type(8))) __bf16 bf16x8;
typedef __attribute__((ext_vector_type(2))) __bf16 bf16x2;
typedef __attribute__((ext_vector_type(2))) float f32x2;
typedef __attribute__((ext_vector_type(4))) float f32x4;
typedef __attribute__((ext_vector_type(4))) u32 u32x4;
typedef __attribute__((ext_vector_type(2))) u32 u32x2;
#define DI __device__ __forceinline__
#define LDSP __attribute__((address_space(3)))
#define RLX __ATOMIC_RELAXED, __HIP_MEMORY_SCOPE_AGENT

#define NREP 8
constexpr u32 WS_E = 0, WS_AGG = 65536, WS_FP = 0x40000, WS_FU = 0x60000, WS_FH = WS_FU + NREP * 2048, WS_FT = WS_FH + NREP * 2048, WS_G = WS_FT + NREP * 2048;
constexpr u32 WS_U = 1u << 20, WS_T = 5u << 20, WS_H = 9u << 20, WS_X = 17u << 20, WS_END = 21u << 20;
constexpr u32 L_RA = 65536, L_W = 98304, L_WS = 17408, L_V = 133120, L_CH = 5264, L_PS = 144, L_SYNC = 154176, L_SEG = 154688, L_TOT = 163840;
constexpr u32 SPIN_CAP = 1u << 18, SENT = 0xffffffffu;

DI float bf2f(u32 h) { return __builtin_bit_cast(float, h << 16); }
DI float bfhi(u32 h) { return __builtin_bit_cast(float, h & 0xffff0000u); }
DI u32 pk2(float a, float b) { f32x2 v = {a, b}; return __builtin_bit_cast(u32, __builtin_convertvector(v, bf16x2)); }
DI u32 pk2(f32x2 v) { return __builtin_bit_cast(u32, __builtin_convertvector(v, bf16x2)); }
DI float rbf(float a) { return bf2f(pk2(a, a) & 0xffffu); }
typedef __amdgpu_buffer_rsrc_t rsrc_t;
DI rsrc_t mkr(const void* p, u32 bytes) { return __builtin_amdgcn_make_buffer_rsrc((void*)p, 0, bytes, 0x00020000); }
DI void bldsc(rsrc_t r, u32 vo, u32 so, u32 l, u32 lo) { asm volatile("s_add_u32 m0, %3, %4\n\ts_nop 0\n\tbuffer_load_dwordx4 %0, %1, %2 offen sc1 lds" :: "v"(vo), "s"(r), "s"(so), "s"(l), "i"(lo) : "memory", "scc"); }
DI void blds(rsrc_t r, u32 vo, u32 so, u32 l, u32 lo) { asm volatile("s_add_u32 m0, %3, %4\n\ts_nop 0\n\tbuffer_load_dwordx4 %0, %1, %2 offen lds" :: "v"(vo), "s"(r), "s"(so), "s"(l), "i"(lo) : "memory", "scc"); }
template <class T> DI T lr(u32 a) { return *(const LDSP T*)(uintptr_t)a; }
template <class T> DI void lw(u32 a, T v) { *(LDSP T*)(uintptr_t)a = v; }
DI u32x2 trr(u32 a) { return __builtin_bit_cast(u32x2, __builtin_amdgcn_ds_read_tr16_b64_v4i16((LDSP s16x4*)(uintptr_t)a)); }
DI bf16x8 cat(u32x2 a, u32x2 b) { u32x4 v = {a.x, a.y, b.x, b.y}; return __builtin_bit_cast(bf16x8, v); }
DI bf16x8 pk8(f32x4 a, f32x4 b) { u32x4 v = {pk2(a[0], a[1]), pk2(a[2], a[3]), pk2(b[0], b[1]), pk2(b[2], b[3])}; return __builtin_bit_cast(bf16x8, v); }
DI f32x4 mma(bf16x8 a, bf16x8 b, f32x4 c) { return __builtin_amdgcn_mfma_f32_16x16x32_bf16(a, b, c, 0, 0, 0); }
DI void mmz(f32x4& c, bf16x8 a, bf16x8 b) { asm volatile("v_mfma_f32_16x16x32_bf16 %0, %1, %2, 0" : "=&v"(c) : "v"(a), "v"(b)); }
DI void mmc(f32x4& c, bf16x8 a, bf16x8 b) { asm volatile("v_mfma_f32_16x16x32_bf16 %0, %1, %2, %0" : "+v"(c) : "v"(a), "v"(b)); }
DI bf16x8 asf(u32x4 v) { return __builtin_bit_cast(bf16x8, v); }
DI float cosr(float rev) { return __builtin_amdgcn_cosf(rev); }
DI float sinr(float rev) { return __builtin_amdgcn_sinf(rev); }
template <int O> DI u32x4 bl4(rsrc_t r, u32 vo, u32 so) { u32x4 v; asm volatile("buffer_load_dwordx4 %0, %1, %2, %3 offen offset:%4" : "=v"(v) : "v"(vo), "s"(r), "s"(so), "n"(O) : "memory"); return v; }
template <int O> DI u32x4 bl4c(rsrc_t r, u32 vo, u32 so) { u32x4 v; asm volatile("buffer_load_dwordx4 %0, %1, %2, %3 offen offset:%4 sc1" : "=v"(v) : "v"(vo), "s"(r), "s"(so), "n"(O) : "memory"); return v; }
DI u32 bl1c(rsrc_t r, u32 vo, u32 so) { u32 v; asm volatile("buffer_load_dword %0, %1, %2, %3 offen sc1" : "=v"(v) : "v"(vo), "s"(r), "s"(so) : "memory"); return v; }
DI void touch(rsrc_t r, u32 vo, u32 l) { asm volatile("s_add_u32 m0, %2, %3\n\ts_nop 0\n\tbuffer_load_dword %0, %1, 0 offen sc1 lds" :: "v"(vo), "s"(r), "s"(l), "i"(L_SYNC + 256) : "memory", "scc"); }
DI u32 bl1(rsrc_t r, u32 vo, u32 so) { u32 v; asm volatile("buffer_load_dword %0, %1, %2, %3 offen" : "=v"(v) : "v"(vo), "s"(r), "s"(so) : "memory"); return v; }
DI u32 blh(rsrc_t r, u32 vo) { u32 v; asm volatile("buffer_load_ushort %0, %1, %2, 0 offen" : "=v"(v) : "v"(vo), "s"(r) : "memory"); return v; }
DI void st4c(u32x4 v, rsrc_t r, u32 vo, u32 so) { asm volatile("buffer_store_dwordx4 %0, %1, %2, %3 offen sc1\n\ts_nop 1" :: "v"(v), "v"(vo), "s"(r), "s"(so) : "memory"); }
DI void st2c(u64 v, rsrc_t r, u32 vo, u32 so) { asm volatile("buffer_store_dwordx2 %0, %1, %2, %3 offen sc1" :: "v"(v), "v"(vo), "s"(r), "s"(so) : "memory"); }
DI void st4(u32x4 v, rsrc_t r, u32 vo, u32 so) { asm volatile("buffer_store_dwordx4 %0, %1, %2, %3 offen nt\n\ts_nop 1" :: "v"(v), "v"(vo), "s"(r), "s"(so) : "memory"); }
#define POLLSLP 2
#define NOUNROLL _Pragma("unroll 1")
#define SB() __builtin_amdgcn_sched_barrier(0)
template <class T> DI void PINS(T& v) { asm volatile("" : "+s"(v)); }
template <class T> DI void PIN(T& v) { asm volatile("" : "+v"(v)); }
#define NOPS() do { SB(); asm volatile("s_nop 7\n\ts_nop 1"); SB(); } while (0)
#define WAITV(n) asm volatile("s_waitcnt vmcnt(%0)" :: "n"(n) : "memory")
#define WAITL(n) asm volatile("s_waitcnt lgkmcnt(%0)" :: "n"(n) : "memory")
#define FENCE() asm volatile("" ::: "memory")
#define BAR() do { FENCE(); __builtin_amdgcn_s_barrier(); FENCE(); } while (0)
#define LBAR() do { WAITL(0); __builtin_amdgcn_s_barrier(); FENCE(); } while (0)
DI u32 umax3(u32 a, u32 b, u32 c) { return max(max(a, b), c); }
DI void gwait(u32 words, u32 phase) {
  NOUNROLL for (u32 s = 0; s < 16 * SPIN_CAP; ++s) {
    u32x4 v; asm volatile("ds_read_b128 %0, %1\n\ts_waitcnt lgkmcnt(0)" : "=v"(v) : "v"(words) : "memory");
    if (__builtin_amdgcn_readfirstlane(min(min(v.x, v.y), min(v.z, v.w))) >= phase) break;
  }
  FENCE();
}
DI void gpost(u32 words, int wq, u32 phase) { FENCE(); lw<u32>(words + 4 * wq, phase); FENCE(); }
DI void gbar(u32 words, int wq, u32 phase) { gpost(words, wq, phase); gwait(words, phase); }
DI void pollq(rsrc_t wr, u32 off, int lane, u64 tag) {
  NOUNROLL for (u32 s = 0; s < SPIN_CAP; ++s) {
    u64 a; asm volatile("buffer_load_dwordx2 %0, %1, %2, %3 offen sc1\n\ts_waitcnt vmcnt(0)" : "=&v"(a) : "v"(lane * 8), "s"(wr), "s"(off) : "memory");
    if (__all(a == tag)) break;
    __builtin_amdgcn_s_sleep(POLLSLP);
  }
}
#define TSW(n, t) do {} while (0)
#define CY0()
#define CY1(n)
#define ITS(p, j) do {} while (0)
#define TS(n) TSW(n, 0)
#define TX(n) TSW(n, 256)
#define NC0 1
#define NW13 4
#define FMASK 63u
#define FTU 0
#define JF 3
struct c2 { f32x2 r, i; };
DI f32x2 fma2(f32x2 a, f32x2 b, f32x2 c) { return __builtin_elementwise_fma(a, b, c); }
DI c2 cfma(c2 a, c2 b, c2 c) { return {fma2(a.r, b.r, fma2(-a.i, b.i, c.r)), fma2(a.r, b.i, fma2(a.i, b.r, c.i))}; }
DI c2 cmul(c2 a, c2 b) { return {fma2(-a.i, b.i, a.r * b.r), fma2(a.r, b.i, a.i * b.r)}; }
DI c2 csq(c2 a) { return {fma2(a.r, a.r, -a.i * a.i), (a.r + a.r) * a.i}; }
DI c2 bc(c2 a, int ps) { return ps ? c2{a.r.yy, a.i.yy} : c2{a.r.xx, a.i.xx}; }
DI c2 csel(bool c, c2 a, c2 b) { return {c ? a.r : b.r, c ? a.i : b.i}; }
DI c2 asc(f32x4 v) { return {v.xy, v.zw}; }
DI void pinc(c2& a) { PIN(a.r); PIN(a.i); }
DI f32x2 spread16(float v) { const u32 u = __float_as_uint(v); const auto e = __builtin_amdgcn_permlane16_swap(u, u, false, false); return f32x2{__uint_as_float(e[0]), __uint_as_float(e[1])}; }
DI float sum16(float v) { const f32x2 e = spread16(v); return e.x + e.y; }
DI float sum32(float v) { const u32 u = __float_as_uint(v); const auto e = __builtin_amdgcn_permlane32_swap(u, u, false, false); return __uint_as_float(e[0]) + __uint_as_float(e[1]); }
template <int CTRL> DI float dppadd(float v) { return v + __int_as_float(__builtin_amdgcn_mov_dpp(__float_as_int(v), CTRL, 0xf, 0xf, true)); }

DI u32x4 cos8(float a0, float kk, float c2d) {
  float c[8]; c[0] = cosr(a0); c[1] = cosr(fmaf(kk, 4.f, a0));
#pragma unroll
  for (int j = 2; j < 8; ++j) c[j] = fmaf(c2d, c[j - 1], -c[j - 2]);
  return u32x4{pk2(c[0], c[1]), pk2(c[2], c[3]), pk2(c[4], c[5]), pk2(c[6], c[7])};
}
DI void hart_table(u32x4 (&tab)[2][4], int kap, bool sp, int g) {
  const float kp = (float)kap * (1.f / 256.f), km = sp ? 0.125f : kp, cp = 2.f * cosr(4.f * kp), cm = sp ? -2.f : cp;
#pragma unroll
  for (int r = 0; r < 4; ++r) {
    const float kk = r == 1 && sp ? km : -km, fr_ = (float)(32 * g + r);
    tab[0][r] = cos8(fmaf(kp, fr_, -0.125f), kp, cp); tab[1][r] = cos8(fmaf(kk, fr_, -0.125f), kk, cm);
  }
}
template <bool INV> DI void hart256(f32x2 (&o)[8][2], u32 ap, const u32x4 (&tab)[2][4], int kap, bool sp) {
  const float sg1 = (kap & 1) ? -1.f : 1.f, al1 = sp ? 0.f : 1.f;
  const f32x2 sgn = {sg1, sg1}, al = {al1, al1};
  bf16x8 a[4][2]; f32x4 m[2][4][2];
#pragma unroll
  for (int r = 0; r < 4; ++r) { a[r][0] = cat(trr(ap + r * 8192), trr(ap + r * 8192 + 512)); a[r][1] = cat(trr(ap + r * 8192 + 4096), trr(ap + r * 8192 + 4608)); }
  SB();
#pragma unroll
  for (int r = 0; r < 4; ++r)
#pragma unroll
    for (int i = 0; i < 4; ++i) m[i >> 1][r][i & 1] = mma(a[r][i & 1], asf(tab[(i >> 1) ^ (INV & i & 1)][r]), f32x4{});
  SB();
#pragma unroll
  for (int hh = 0; hh < 2; ++hh) {
    f32x2 p[2][4];
#pragma unroll
    for (int i = 0; i < 8; ++i) { const f32x2 sg_ = INV && i >= 4 ? -sgn : sgn; p[i >> 2][i & 3] = hh ? fma2(sg_, m[i >> 2][i & 3][1].zw, m[i >> 2][i & 3][0].zw) : fma2(sg_, m[i >> 2][i & 3][1].xy, m[i >> 2][i & 3][0].xy); }
    const f32x2 s0 = p[0][1] + p[0][3], d0 = p[0][1] - p[0][3], s1 = INV ? p[1][1] + p[1][3] : fma2(al, p[1][1], p[1][3]), d1 = INV ? p[1][1] - p[1][3] : fma2(-al, p[1][3], p[1][1]);
    const f32x2 x0 = sp ? d0 : d1, x1 = sp ? d1 : d0;
#pragma unroll
    for (int sg = 0; sg < 2; ++sg) {
      const f32x2 A = p[sg][0] + p[sg][2], B = p[sg][0] - p[sg][2], S = sg ? s1 : s0, X = sg ? x1 : x0;
      o[4 * sg][hh] = A + S; o[4 * sg + 2][hh] = A - S; o[4 * sg + 1][hh] = B + X; o[4 * sg + 3][hh] = B - X;
    }
  }
  SB();
#pragma unroll
  for (int i = 0; i < 8; ++i) {
    const auto e0 = __builtin_amdgcn_permlane32_swap(__float_as_uint(o[i][0].x), __float_as_uint(o[i][1].x), false, false);
    const auto e1 = __builtin_amdgcn_permlane32_swap(__float_as_uint(o[i][0].y), __float_as_uint(o[i][1].y), false, false);
    o[i][0] = f32x2{__uint_as_float(e0[0]), __uint_as_float(e1[0])}; o[i][1] = f32x2{__uint_as_float(e0[1]), __uint_as_float(e1[1])};
  }
  SB();
#pragma unroll
  for (int i = 0; i < 8; ++i) { const f32x2 a_ = o[i][0], b_ = o[i][1]; o[i][0] = a_ + b_; o[i][1] = a_ - b_; }
}

struct TileD { int k, c, src, w; };
DI TileD tile_desc(int tau, int k1, int kd1) {
  TileD t; const int i = tau >> 1, ka = tau & 1, hi = i >= 5;
  t.c = kd1 + 2 * ka;
  t.src = k1 ? -1 : (kd1 ? ka : hi);
  t.k = 256 * i + (k1 ? k1 : (kd1 ? 128 * ka : -1152 * hi));
  t.w = k1 ? 1 : (kd1 ? 2 : (tau >= 18 ? 0 : ((i & 3) == 0 && i < 8 ? 1 : 2)));
  return t;
}
DI void m0_fwd(bf16x8& o, int n, int l15, int g, int k1, int kd1) {
  const TileD t = tile_desc(8 * n + (l15 >> 1), k1, kd1); f32x4 v[2];
  const float ak = (float)t.k * (-1.f / 2048.f), a0 = fmaf(ak, (float)(2 * g), -0.25f * (float)(l15 & 1)), ac = -0.25f * (float)t.c;
#pragma unroll
  for (int j = 0; j < 8; ++j) {
    const int rs = j & 1;
    const float c = cosr(a0 + ((j & 2) ? ak : 0.f) + ((j & 4) ? ac : 0.f) + (t.src < 0 ? (rs ? 0.125f : -0.125f) : 0.f));
    v[j >> 2][j & 3] = (t.src >= 0 && (t.w == 0 || t.src != rs)) ? 0.f : c;
  }
  o = pk8(v[0], v[1]);
}
DI void tr2(bf16x8& X, bf16x8& Y, bf16x8 A, bf16x8 B, u32 wr, u32 rd) {
  const u32x4 a = __builtin_bit_cast(u32x4, A), b = __builtin_bit_cast(u32x4, B);
  FENCE(); lw<u32x2>(wr, a.xy); lw<u32x2>(wr + 512, b.xy); lw<u32x2>(wr + 1024, a.zw); lw<u32x2>(wr + 1536, b.zw); FENCE();
  X = cat(trr(rd), trr(rd + 512)); Y = cat(trr(rd + 1024), trr(rd + 1536)); FENCE();
}

template <int O> DI void ldhi(float& v, u32 a) { asm volatile("ds_read_u16_d16_hi %0, %1 offset:%2" : "+v"(v) : "v"(a), "n"(O) : "memory"); }
#define BQ 12
#define LEAD 2
constexpr int g1_wait(int j) { int c = j <= 1 ? 4 : 0; for (int u = j - LEAD + 1; u < j; ++u) c += (u + LEAD < 16 ? 8 : 0) + (u == 15 - 2 * LEAD ? 1 : 0); return c; }
#define TU 12
#define NE 2
constexpr int g2_c0(int u) { return u >= 13 ? 64 : 16 + 4 * NE + u * (48 - 4 * NE) / 13; }
constexpr bool g2_ok() { for (int u = 0; u < 13; ++u) for (int e = g2_c0(u); e < g2_c0(u + 1); ++e) if (u > e / 4 - 3 || (e / 4 >= 9 && u < e / 4 - 5)) return false; return true; }
static_assert(g2_ok(), "C tile schedule");
constexpr int g2_pre(int u) { return u < 0 ? 0 : g2_c0(u + 1) - g2_c0(u) + (u == 0 || u == 3 ? 1 : 0); }
constexpr int g2_post(int u) { return (u >= TU && u < TU + 4 ? 3 : 0) + (u == TU - 5 ? 1 : 0); }
constexpr int g2_wait(int u) { int c = g2_post(u - 3); for (int v = u - 2; v < u; ++v) c += g2_pre(v) + (v + 3 < 16 ? 4 : 0) + g2_post(v); return c; }
template <int N> struct IC { static constexpr int v = N; };
template <int I, int N, class F> DI void sfor(F&& f) { if constexpr (I < N) { f(IC<I>{}); sfor<I + 1, N>(f); } }

typedef __attribute__((ext_vector_type(8))) u64 u64x8;
typedef __attribute__((ext_vector_type(4))) u64 u64x4;
typedef __attribute__((ext_vector_type(2))) u64 u64x2;
__global__ __launch_bounds__(512, 2) void k_all(const u16*, const u16*, const u16*, const u16*, const u16*, const u16*, const u16*, const u16*,
    const u16*, const u16*, const u16*, const u16*, char*, float*) {
#define GETARGS() u64x8 ka; u64x4 kb_; u64x2 kc; \
  asm volatile("s_load_dwordx16 %0, %3, 0x0\n\ts_load_dwordx8 %1, %3, 0x40\n\ts_load_dwordx4 %2, %3, 0x60" : "=&s"(ka), "=&s"(kb_), "=&s"(kc) : "s"(__builtin_amdgcn_kernarg_segment_ptr()) : "memory")
  extern __shared__ __attribute__((aligned(16))) char lds_[];
  const u32 L = (u32)(uintptr_t)(LDSP char*)lds_;
  const int lane = threadIdx.x & 63, wv = __builtin_amdgcn_readfirstlane(threadIdx.x >> 6), w = wv & 3, rh = wv >> 2, l15 = lane & 15, g = lane >> 4;
  const int bid = blockIdx.x, xcd = bid & 7, jj = bid >> 3, ti = jj & 3, pj = jj >> 2;
  const int tt = (xcd >> 1) * 4 + ti, pt = (xcd & 1) * 8 + pj;
  const int s2 = tt >> 1, sh = tt & 1, ct = pt;
  const int lr8 = lane >> 3, lc8 = lane & 7, q = l15 >> 2, pq = lane & 3;
  int rot = tt;
  u32 gq[2]; u64 outp, wsp;
#define ARGS() asm volatile("s_waitcnt lgkmcnt(0)" : "+s"(ka), "+s"(kb_), "+s"(kc) :: "memory"); \
  char* ws = (char*)kc[0]; outp = kc[1]; wsp = kc[0]; \
  const rsrc_t xr = mkr((const void*)ka[0], 4u << 20), wsr = mkr(ws, WS_END), cr = mkr((const void*)((ct & 8) ? kb_[1] : kb_[0]), 2u << 20); \
  auto issue_a = [&](int rr, int hf) { const int r = (rr + r0) & 3; blds(xr, vA, (8 * r + 1024 * hf) * 2048, L + r * 8192 + hf * 4096 + w * 1024, L_RA); }; \
  u32 vC[2]; vC[0] = (u32)(32 * (ct ^ ((w & 1) * 8)) + 512 * (w >> 1) + lr8) * 2048u + 16 * (lc8 ^ (lr8 >> 1)); vC[1] = vC[0] ^ 64; \
  auto c_piece = [&](int i, int e) { blds(cr, vC[e & 1], e * 8 * 2048 + ((i + rot) & 15) * 128, L + w * 4096, (i % 6) * 16384 + e * 1024); }; \
  const u32 vGo = WS_G + tt * 512 + (u32)((w & 1) * 64 + lr8 * 4 + 2 * rh + (w >> 1)) * 4

  if (rh) {
    GETARGS();
    {
      u64 pc; u32 nb; asm volatile("s_getpc_b64 %0\n.Lcode_lo:\n\ts_mov_b32 %1, .Lcode_hi-.Lcode_lo\n\t.subsection 1\n.Lcode_hi:\n\t.subsection 0" : "=s"(pc), "=s"(nb));
      if (w == 3) touch(mkr((const void*)(pc & ~127ull), min(nb, 61440u)), (lane * 32 + jj) * 128, L);
    }
    const int ii = 8 * (w & 1) + lr8, swz = 16 * (lc8 ^ ((ii >> 1) & 7)), ac = lc8 ^ (((lr8 >> 1) & 1) | ((w & 1) << 1)), r0 = 2 * sh + (ct & 1);
    u32 vB = (u32)(64 * pt + ii) * 2048u + swz, vA = (u32)(s2 + 32 * (8 * w + lr8)) * 2048u + 64 * ct + 16 * (ac & 3) + 1024 * (ac >> 2);
    PIN(vA); PIN(vB);
    lw<u32>(L + L_SYNC + 4 * wv, 0u);
    ARGS();
    const rsrc_t br = mkr((const void*)((w & 2) ? ka[2] : ka[1]), 2u << 20);
    auto issue_b1 = [&](int j, int i) { blds(br, vB, 16 * i * 2048 + ((j + rot) & 15) * 128, L + w * 1024, ((j + 3) & 3) * 32768 + 16384 + i * 4096); };
    issue_a(0, 0); issue_a(0, 1); issue_a(1, 0); issue_a(1, 1);
    SB();
    const u32 vX = (u32)(128 * tt + 16 * (ii >> 2) + 8 * (ii & 1) + 2 * (w >> 1) + ((ii >> 1) & 1)) * 2048u + swz, vl = lane * 16;
    auto issue_x1 = [&](int j, int i) { blds(xr, vX, (64 * (i >> 1) + 4 * (i & 1)) * 2048 + ((j + rot) & 15) * 128, L + w * 1024, ((j + 3) & 3) * 32768 + i * 4096); };
    u64 tag; asm volatile("buffer_load_dwordx2 %0, off, %1, 0 sc1" : "=v"(tag) : "s"(wsr) : "memory");
    if (w == 0) st4c(u32x4{SENT, SENT, SENT, SENT}, wsr, lane < 32 ? vl : WS_END, WS_AGG + tt * 8192 + pt * 512);
    if (w == 2) touch(mkr(ws + WS_FP, WS_G + 8192 - WS_FP), (lane * 256 + bid) * 128, L);
    if (w == 1) touch(mkr((const void*)kb_[3], 2048), ((jj & 15) + 16 * lane) * 128, L);
    const int kap = 16 * sh + l15, ecol = 8 * w + 4 * (g & 1) + 2 * (g >> 1);
    u32 q_ds[2] = {bl1(mkr((const void*)kb_[2], 2048), 2 * ecol, 64 * ct), bl1(mkr((const void*)kb_[2], 2048), 2 * ecol, 64 * ct + 1024)};
    const rsrc_t w0 = mkr((const void*)ka[6], 2048 * 1026), w1 = mkr((const void*)ka[7], 2048 * 1026);
    const u32 rep = (bid & (NREP - 1)) * 2048;
    TX(16);
    const int k1 = xcd * 16 + (jj >> 1), kd1 = jj & 1, cc = kd1 + 2 * (w & 1);
    const bool sp0 = (k1 == 0 && kd1 == 0);
    bf16x8 m0[2], mi[2], m0x, mix[2], fa_r, fa_i, fb_r, fb_i, gb_r, gb_i, ga_r, ga_i; f32x4 t1c, t1s, t2c, t2s;
    u32 vW[5], wa[5], wo[5]; float fr[4][4], fi[4][4], er[4], ei[4];
    const float fl = (float)l15, fg = (float)(4 * g), fc = (float)cc;
    const u32 tw = L + L_SEG + w * 2048 + l15 * 32 + 8 * g, tr = L + L_SEG + w * 2048 + (4 * g + q) * 32 + 8 * pq;
    SB();
    {
      const float st = fmaf(fl, -4.f / 64.f, fc * (-1.f / 64.f));
#pragma unroll
      for (int e = 0; e < 4; ++e) { const float a = (fg + (float)e) * st; t2c[e] = cosr(a); t2s[e] = sinr(a); t1c[e] = cosr(a * (1.f / 16.f)); t1s[e] = sinr(a * (1.f / 16.f)); }
      fa_r = pk8(t2c, -t2s); fa_i = pk8(t2s, t2c); PIN(t1c); PIN(t1s);
      PIN(fa_r); PIN(fa_i);
      const float fm = (float)(l15 < 8 ? l15 : l15 ^ 3) * (-1.f / 16.f); const u32 sl = l15 < 8 ? 0u : 0x80008000u;
#pragma unroll
      for (int e = 0; e < 4; ++e) { const float a = (fg + (float)e) * fm; t2c[e] = cosr(a); t2s[e] = sinr(a); }
      fb_r = pk8(t2c, -t2s); fb_i = asf(__builtin_bit_cast(u32x4, pk8(t2s, t2c)) ^ sl);
      PIN(fb_r); PIN(fb_i);
    }
    u32 xa[2], ua, us, us0, fo, ap0, wp0, tr_, tw_;
    const u32 vo = L + L_V + w * L_CH;
    int wid, wsl; f32x4 uc, us_;
    auto fill = [&](auto k_) {
      constexpr int k = decltype(k_)::v;
      SB();
      if constexpr (k == -4) {
        const float mk = (float)(k1 + 256 * (l15 >> 2)) * (-1.f / 2048.f), mc = -0.25f * (float)(kd1 + (l15 & 2)), ma = fmaf(mk, 0.5f * fg, fmaf((float)(l15 & 1), -0.25f, -0.125f));
        u32x4 f;
#pragma unroll
        for (int e = 0; e < 4; ++e) { const float a = ma + ((e & 1) ? mk : 0.f) + ((e & 2) ? mc : 0.f); f[e] = pk2(cosr(a), -sinr(a)); }
        m0[0] = asf(f); m0[1] = asf(f ^ u32x4{0u, 0x80008000u, 0u, 0x80008000u});
        const float b = fmaf((float)(l15 >> 1), fmaf(fg, -1.f / 32.f, (float)k1 * (-1.f / 2048.f)), fmaf((float)(l15 & 1), 0.75f, fmaf((float)k1, 1.f / 512.f, -0.125f))), b1 = b - 0.25f * (float)kd1;
        const u32 p0 = pk2(cosr(b), sinr(b)), p1 = pk2(cosr(b1), sinr(b1)), sgn = (l15 & 2) ? 0x80008000u : 0u;
        mi[0] = asf(u32x4{p0, p0, p0 ^ sgn, p0 ^ sgn}); mi[1] = asf(u32x4{p1, p1 ^ 0x80008000u, p1 ^ sgn, p1 ^ sgn ^ 0x80008000u});
        if (__builtin_expect(k1 == 0, 0)) { m0_fwd(m0[0], 0, l15, g, 0, kd1); m0_fwd(m0[1], 1, l15, g, 0, kd1); m0_fwd(m0x, 2, l15, g, 0, 0); tr2(mix[0], mix[1], m0x, bf16x8{}, tw, tr); tr2(mi[0], mi[1], m0[0], m0[1], tw, tr); }
        PIN(m0[0]); PIN(m0[1]); PIN(mi[0]); PIN(mi[1]);
      }
      if constexpr (k == -2 || k == -1) { constexpr int i = k + 2; const int n = i ? 63 - kap : kap, rho = 64 * (n & 3) + (n >> 2); xa[i] = L + L_RA + rho * 128 + 16 * (w ^ (((rho >> 1) & 1) | ((rho >> 3) & 1) << 1)) + 2 * (ecol & 7); PIN(xa[i]); }
      if constexpr (k >= 0 && k < 4) {
        const float a = fl * fmaf(fg + (float)k, 4.f / 1024.f, fc * (1.f / 1024.f));
        t2c[k] = cosr(a) * (1.f / 1048576.f); t2s[k] = sinr(a) * (1.f / 1048576.f); uc[k] = cosr(16.f * a); us_[k] = sinr(16.f * a); PIN(t2c); PIN(t2s); PIN(uc); PIN(us_);
      }
      if constexpr (k == 4) { ga_r = pk8(uc, -us_); ga_i = pk8(us_, uc); PIN(ga_r); PIN(ga_i); }
      if constexpr (k >= 5 && k < 9) { const float a = (fg + (g < 2 ? (float)(k - 5) : (float)(8 - k))) * fl * (-1.f / 16.f); uc[k - 5] = cosr(a); us_[k - 5] = sinr(a); PIN(uc); PIN(us_); }
      if constexpr (k == 9) {
        const u32 sg = g < 2 ? 0u : 0x80008000u; const u32x4 t = __builtin_bit_cast(u32x4, pk8(uc, us_));
        gb_r = asf(u32x4{t.x, t.y, t.z ^ sg, t.w ^ sg}); gb_i = asf(u32x4{t.z ^ 0x80008000u, t.w ^ 0x80008000u, t.x ^ sg, t.y ^ sg}); PIN(gb_r); PIN(gb_i);
      }
      if constexpr (k == 12) { ua = L + L_V + (16 * w + g) * 256 + (lr8 & 1) * 128 + 16 * (lc8 ^ g); PIN(ua); }
      if constexpr (k == 13) { us = (u32)(64 * w + 2 * g + (lr8 & 1) + 32 * (sh ^ (w & 1))) * 16384u + lc8 * 16; PIN(us); }
      if constexpr (k == 14) { us0 = (w & 1) && g == 0 ? us ^ (32 * 16384) : us; PIN(us0); }
      if constexpr (k == 11) { fo = lane < NREP ? lane * 2048 + bid * 8 : WS_END; PIN(fo); }
      if constexpr (k == 33) { ap0 = vo + (4 * g + q) * 64 + 16 * pq; PIN(ap0); }
      if constexpr (k == 34) { wp0 = vo + l15 * L_PS + 16 * g; PIN(wp0); }
      if constexpr (k == 35) { tr_ = L + L_V + w * 2 * L_PS + g * L_CH + q * 32 + 8 * pq; PIN(tr_); }
      if constexpr (k == 36) { tw_ = L + L_V + w * 2 * L_PS + q * L_CH + pq * 32 + 8 * g; PIN(tw_); }
      if constexpr (k == 37) { PIN(vC[0]); PIN(vC[1]); }
      if constexpr (k == 40) { wid = 2 * cc + 8 * l15 + 512 * g; PIN(wid); }
      if constexpr (k == 41) { wa[0] = g < 2 ? L + L_W + (w >> 1) * 1056 + ((k1 * 1026) & 15) + wid : L + L_W + (8 + (w >> 1)) * 1056 + ((-k1 * 1026) & 15) + 1664 - wid; PIN(wa[0]); }
      if constexpr (k == 42) { wo[0] = g < 2 ? L + L_W + (8 + (w >> 1)) * 1056 + ((-k1 * 1026) & 15) : L + L_W + (w >> 1) * 1056 + ((k1 * 1026) & 15) + 1024; PIN(wo[0]); }
      if constexpr (k >= 43 && k < 46) { wa[k - 42] = wa[0] + 2112 * (k - 42); wo[k - 42] = wo[0] + 2112 * (k - 42); PIN(wa[k - 42]); PIN(wo[k - 42]); }
      if constexpr (k == 46) {
        if (__builtin_expect(k1 == 0, 0)) {
#pragma unroll
          for (int i = 0; i < 5; ++i) {
            const int kk = tile_desc(w + 4 * i, 0, kd1).k;
            const u32 wp = L + L_W + (kk >> 7) * 1056, wq = L + L_W + (((2048 - kk) & 2047) >> 7) * 1056;
            wa[i] = g < 2 ? wp + wid : wq + 1664 - wid; wo[i] = g < 2 ? wq : wp + 1024;
          }
        }
      }
      if constexpr (k >= 48 && k < 73) {
        constexpr int i = (k - 48) / 5, ph = (k - 48) % 5;
        if (ph == 0) { wid = 64 * (w + 4 * i) + lane; wsl = (wid * 993) >> 16; PIN(wid); PIN(wsl); }
        if (ph == 1) { wsl = min(wsl, 15); wid = 16 * wid - 1056 * wsl; PIN(wid); PIN(wsl); }
        if (ph == 2) { wsl = k1 + (k1 ? 256 : 128) * wsl; PIN(wsl); }
        if (ph == 3) { wsl = wsl < 2048 ? wsl : 4096 - wsl; PIN(wsl); }
        if (ph == 4) { vW[i] = ((wsl * 1026) & ~15) + wid; PIN(vW[i]); }
      }
      if constexpr (k >= 76 && k < 96) {
#pragma unroll
        for (int r = 2 * (k & 1); r < 2 * (k & 1) + 2; ++r) { constexpr int z_ = (k - 76) >> 1; float& z = z_ < 4 ? fr[z_ & 3][r] : z_ < 8 ? fi[z_ & 3][r] : z_ == 8 ? er[r] : ei[r]; z = 0.f; PIN(z); }
      }
      SB();
    };
    auto issue_t = [&](auto j_) {
      constexpr int j = decltype(j_)::v;
      sfor<0, 8>([&](auto i_) { constexpr int i = decltype(i_)::v; if (i < 4) issue_b1(j, i); else issue_x1(j, i - 4); if constexpr (j > 2) fill(IC<8 * j - 8 + i>{}); });
    };
    sfor<-4, 0>(fill);
    TX(17);
    issue_a(2, 0); issue_a(2, 1); issue_a(3, 0); issue_a(3, 1); issue_b1(0, 0); issue_b1(0, 1); issue_b1(0, 2); issue_b1(0, 3);
    WAITV(4); LBAR();
    u32 sk[8][2];
#pragma unroll
    for (int i = 0; i < 8; ++i) { const u32 a = xa[i >> 2] + 2048 * (i < 4 ? i : (i + 3) & 3); sk[i][0] = lr<u32>(a); sk[i][1] = lr<u32>(a + 64); }
    PIN(tag); PIN(q_ds[0]); PIN(q_ds[1]);
    tag += 0x9e3779b97f4a7c15ull;
    if (w == 0) lw<u64>(L + L_SYNC + 32, tag);
    SB();
#pragma unroll
    for (int i = 0; i < 4; ++i) issue_x1(0, i);
    sfor<0, 15>(fill);
    issue_t(IC<1>{});
    {
      u32x4 uq[4];
      gwait(L + L_SYNC, 1);
#pragma unroll
      for (int i = 0; i < 4; ++i) uq[i] = lr<u32x4>((ua ^ (64 * (i & 1))) + 1024 * i);
#pragma unroll
      for (int i = 0; i < 4; ++i) st4c(uq[i], wsr, i == 0 ? us0 : us, WS_U + ct * 1024 + s2 * 128 + i * 8 * 16384);
    }
    if (LEAD == 3) issue_t(IC<2>{});
    TX(18);
    u64 fu;
    const f32x4 z4 = {};
    bf16x8 a0[2][2];
    sfor<0, 17 - LEAD>([&](auto j_) {
      constexpr int j = decltype(j_)::v;
      WAITV(g1_wait(j));
      if (j == 0) LBAR(); else BAR();
      ITS(1, j);
      PINS(rot);
      if (j == JF && w == 0) st2c(tag, wsr, fo, WS_FU);
      if (j == 15 - 2 * LEAD) asm volatile("buffer_load_dwordx2 %0, %1, %2, %3 offen sc1" : "=v"(fu) : "v"(lane * 8), "s"(wsr), "s"(WS_FU + rep + w * 512) : "memory");
      SB();
      if constexpr (j + LEAD < 16) issue_t(IC<j + LEAD>{});
      SB();
      if (j == 15 - LEAD) { PIN(fu); if (__builtin_expect(!__all(fu == tag), 0)) pollq(wsr, WS_FU + rep + w * 512, lane, tag); }
      SB();
    });
#pragma unroll
    for (int i = 0; i < 4; ++i) bldsc(wsr, vl, WS_U + (u32)(2 * k1 + kd1) * 16384u + w * 2048 + (i >> 1) * 8192 + (i & 1) * 1024, L + w * L_CH, L_V + i * 1024);
#pragma unroll
    for (int i = 0; i < NW13; ++i) blds(w0, vW[i], 0, L + w * 1024, L_W + i * 4096);
    WAITV(4 + NW13); BAR();
    ITS(1, 17 - LEAD);
    auto wissue = [&]() {
#pragma unroll
      for (int i = 0; i < 4; ++i) { if (i >= NW13) blds(w0, vW[i], 0, L + w * 1024, L_W + i * 4096); blds(w1, vW[i], 0, L + w * 1024, L_W + L_WS + i * 4096); }
      if (w == 0) { blds(w0, vW[4], 0, L, L_W + 16384); blds(w1, vW[4], 0, L, L_W + L_WS + 16384); }
#pragma unroll
      for (int e = 0; e < 4 * NC0; ++e) c_piece(e >> 2, e & 3);
    };
    {
      WAITV(NW13);
#pragma unroll
      for (int cb = 0; cb < 2; ++cb) { a0[cb][0] = cat(trr(ap0 + 1024 * cb), trr(ap0 + 1024 * cb + 2048)); a0[cb][1] = cat(trr(ap0 + 1024 * cb + 8), trr(ap0 + 1024 * cb + 2056)); }
      FENCE();
      wissue();
      f32x4 d[2][2][2];
#pragma unroll
      for (int i = 0; i < 8; ++i) d[i >> 2][(i >> 1) & 1][i & 1] = mma(a0[(i >> 1) & 1][i & 1], m0[i >> 2], z4);
#pragma unroll
      for (int i = 0; i < 4; ++i) lw<bf16x8>(wp0 + 16 * (i >> 1) * L_PS + 64 * (i & 1), pk8(d[i >> 1][i & 1][0], d[i >> 1][i & 1][1]));
      if (__builtin_expect(sp0, 0)) {
#pragma unroll
        for (int cb = 0; cb < 2; ++cb) { const bf16x8 o = pk8(mma(a0[cb][0], m0x, z4), mma(a0[cb][1], m0x, z4)); if (l15 < 4) lw<bf16x8>(wp0 + 32 * L_PS + 64 * cb, o); }
      }
      gbar(L + L_SYNC + 16, w, 3);
    }
    TX(8);
    auto tiles = [&](auto nt_, auto i0_) {
      constexpr int NT = decltype(nt_)::v, i0 = decltype(i0_)::v;
      const u32 pr = tr_ + i0 * 8 * L_PS, pw_ = tw_ + i0 * 8 * L_PS;
      bf16x8 a[NT]; f32x4 yr[NT], yi[NT];
#pragma unroll
      for (int i = 0; i < NT; ++i) a[i] = cat(trr(pr + i * 8 * L_PS), trr(pr + i * 8 * L_PS + L_PS));
      SB();
#pragma unroll
      for (int i = 0; i < NT; ++i) { yr[i] = mma(a[i], fa_r, z4); yi[i] = mma(a[i], fa_i, z4); }
      SB();
#pragma unroll
      for (int i = 0; i < NT; ++i) { a[i] = pk8(yr[i] * t1c - yi[i] * t1s, yr[i] * t1s + yi[i] * t1c); yr[i] = mma(fb_r, a[i], z4); yi[i] = mma(fb_i, a[i], z4); SB(); }
#pragma unroll
      for (int i = 0; i < NT; ++i) asm volatile("" :: "v"(yr[i]), "v"(yi[i]));
      if (!i0) { WAITV(4 * NC0); gbar(L + L_SYNC + 16, w, 4); }
      if (!i0) TX(9);
#pragma unroll
      for (int i = 0; i < NT; ++i) {
        if (i0) {
#pragma unroll
          for (int r = 0; r < 4; ++r) fr[i][r] = fi[i][r] = er[i] = ei[i] = 0.f;
        }
        ldhi<0>(fr[i][0], wa[i0 + i]); ldhi<128>(fr[i][1], wa[i0 + i]); ldhi<256>(fr[i][2], wa[i0 + i]); ldhi<384>(fr[i][3], wa[i0 + i]);
        ldhi<L_WS>(fi[i][0], wa[i0 + i]); ldhi<L_WS + 128>(fi[i][1], wa[i0 + i]); ldhi<L_WS + 256>(fi[i][2], wa[i0 + i]); ldhi<L_WS + 384>(fi[i][3], wa[i0 + i]);
        ldhi<0>(er[i], wo[i0 + i]); ldhi<L_WS>(ei[i], wo[i0 + i]);
      }
      if (!i0) gwait(L + L_SYNC, 2); else WAITL(0);
#pragma unroll
      for (int i = 0; i < NT; ++i)
#pragma unroll
        for (int r = 0; r < 4; ++r) { PIN(fr[i][r]); PIN(fi[i][r]); if (r == 0) { PIN(er[i]); PIN(ei[i]); } }
      if (cc == 0) {
        const float b0 = lane == 0 ? 0.5f : 0.f, b3 = lane == 32 ? 0.5f : 0.f;
#pragma unroll
        for (int i = 0; i < NT; ++i) {
          float t;
          asm volatile("v_sub_f32 %4, %5, %0\n\tv_fmac_f32 %0, %7, %4\n\tv_add_f32 %4, %6, %1\n\tv_fma_f32 %1, -%7, %4, %1\n\t"
                       "v_sub_f32 %4, %5, %2\n\tv_fmac_f32 %2, %8, %4\n\tv_add_f32 %4, %6, %3\n\tv_fma_f32 %3, -%8, %4, %3"
                       : "+v"(fr[i][0]), "+v"(fi[i][0]), "+v"(fr[i][3]), "+v"(fi[i][3]), "=&v"(t) : "v"(er[i]), "v"(ei[i]), "v"(b0), "v"(b3));
        }
      }
      if (__builtin_expect(k1 == 0, 0)) {
#pragma unroll
        for (int i = 0; i < NT; ++i) { const float wt_ = (float)tile_desc(w + 4 * (i0 + i), 0, kd1).w; yr[i] *= wt_; yi[i] *= wt_; }
      }
      SB();
#pragma unroll
      for (int i = 0; i < NT; ++i) {
        const f32x4 vr = {fr[i][0], fr[i][1], fr[i][2], fr[i][3]}, vi = {fi[i][0], fi[i][1], fi[i][2], fi[i][3]};
        a[i] = pk8(yr[i] * vr - yi[i] * vi, yr[i] * vi + yi[i] * vr); yr[i] = mma(a[i], gb_r, z4); yi[i] = mma(a[i], gb_i, z4);
        SB();
        if (!i0) {
#pragma unroll
          for (int e = 4 * NC0 + (4 - NC0) * i; e < 4 * NC0 + (4 - NC0) * (i + 1); ++e) c_piece(e >> 2, e & 3);
        }
        SB();
      }
#pragma unroll
      for (int i = 0; i < NT; ++i) {
        a[i] = pk8(yr[i] * t2c - yi[i] * t2s, yr[i] * t2s + yi[i] * t2c); yr[i] = mma(a[i], ga_r, z4); yi[i] = mma(a[i], ga_i, z4);
        SB();
      }
#pragma unroll
      for (int i = 0; i < NT; ++i) { lw<u32x2>(pw_ + i * 8 * L_PS, u32x2{pk2(yr[i].xy), pk2(yr[i].zw)}); lw<u32x2>(pw_ + i * 8 * L_PS + L_PS, u32x2{pk2(yi[i].xy), pk2(yi[i].zw)}); }
    };
    tiles(IC<4>{}, IC<0>{});
    if (__builtin_expect(sp0 && w < 2, 0)) tiles(IC<1>{}, IC<4>{});
    TX(23);
    gbar(L + L_SYNC + 16, w, 5);
    TX(24);
    u32x4 tq[4];
    {
      f32x4 o[2][2][2];
      const u32 ap = vo + (4 * g + q) * L_PS + 16 * pq;
#pragma unroll
      for (int cb = 0; cb < 2; ++cb) { a0[cb][0] = cat(trr(ap + 64 * cb), trr(ap + 64 * cb + 16 * L_PS)); a0[cb][1] = cat(trr(ap + 64 * cb + 8), trr(ap + 64 * cb + 16 * L_PS + 8)); }
#pragma unroll
      for (int i = 0; i < 8; ++i) o[i >> 2][(i >> 1) & 1][i & 1] = mma(a0[(i >> 1) & 1][i & 1], mi[i >> 2], z4);
      if (__builtin_expect(sp0, 0)) {
        const u32 ax = vo + (32 + q) * L_PS + 16 * pq;
#pragma unroll
        for (int i = 0; i < 4; ++i) { const u32x2 t = trr(ax + 64 * (i >> 1) + 8 * (i & 1)); const bf16x8 a = cat(t, t); o[0][i >> 1][i & 1] = mma(a, mix[0], o[0][i >> 1][i & 1]); o[1][i >> 1][i & 1] = mma(a, mix[1], o[1][i >> 1][i & 1]); }
      }
      FENCE();
#pragma unroll
      for (int i = 0; i < 4; ++i) lw<bf16x8>(vo + i * 1280 + l15 * 80 + 16 * g, pk8(o[i >> 1][i & 1][0], o[i >> 1][i & 1][1]));
      FENCE();
#pragma unroll
      for (int i = 0; i < 4; ++i) tq[i] = lr<u32x4>(vo + i * 1280 + (lane >> 2) * 80 + pq * 16);
#pragma unroll
      for (int i = 0; i < 4; ++i) PIN(tq[i]);
    }
    TX(10);
    u32x4 tab[2][4];
    if (__builtin_expect(bid == 0 && w == 0, 0)) st2c(tag, wsr, lane == 0 ? WS_E : WS_END, 0);
#pragma unroll
    for (int i = 0; i < 16; ++i) { const u32 xv = sk[i >> 1][i & 1], dv = q_ds[i & 1]; sk[i >> 1][i & 1] = pk2(bf2f(xv & 0xffffu) * bf2f(dv & 0xffffu), bfhi(xv) * bfhi(dv)); PIN(sk[i >> 1][i & 1]); }
    TX(11);
    const u32 hb = WS_H + (u32)(tt * 8 + 2 * w) * 65536u + ((ct & 8) ? 32768 : 0);
    u32 fa_ = lane < 32 ? (4 * w + 2 * (lane & 1) + (sh ^ (lane & 1))) * 128 + (((lane >> 1) + rot) & 15) * 8 : WS_END, miss; PIN(fa_);
    u64 fh;
    auto fload = [&]() { asm volatile("buffer_load_dwordx2 %0, %1, %2, %3 offen sc1" : "=v"(fh) : "v"(fa_), "s"(wsr), "s"(WS_FH + rep) : "memory"); };
    auto fneed = [&](u32 bits) {
      if (__builtin_expect((miss & bits) != 0, 0)) {
        NOUNROLL for (u32 spn = 0; spn < SPIN_CAP; ++spn) {
          u64 a; asm volatile("buffer_load_dwordx2 %0, %1, %2, %3 offen sc1\n\ts_waitcnt vmcnt(0)" : "=&v"(a) : "v"(fa_), "s"(wsr), "s"(WS_FH + rep) : "memory");
          miss = (u32)__builtin_amdgcn_ballot_w64(a != tag);
          if (!(miss & bits)) break;
          __builtin_amdgcn_s_sleep(POLLSLP);
        }
      }
    };
    auto h_issue = [&](int u) {
      const u32 so = hb + ((u + rot) & 15) * 2048;
#pragma unroll
      for (int m = 0; m < 4; ++m) bldsc(wsr, vl, so + (m >> 1) * 65536 + (m & 1) * 1024, L + w * 4096, 98304 + (u & 3) * 16384 + m * 1024);
    };
    auto extras = [&]() {
      const u32 to = (u32)lr8 * (16 * 32768) + (u32)(2 * k1 + kd1) * 128 + lc8 * 16;
#pragma unroll
      for (int i = 0; i < 4; ++i) st4c(tq[i], wsr, to, WS_T + (8 * (i >> 1) + 2 * w + (i & 1)) * 32768);
#pragma unroll
      for (int e = 0; e < 4 * NE; ++e) c_piece(4 + (e >> 2), e & 3);
    };
    WAITV(0); BAR();
    TX(25);
    extras();
    fload();
    SB();
    {
      const float tk = (float)(2 * kap + 1) * (1.f / 512.f), tc = 2.f * cosr(4.f * tk);
#pragma unroll
      for (int i = 0; i < 8; ++i) {
        const float kk = (i & 4) ? -tk : tk;
        tab[i >> 2][i & 3] = cos8(fmaf(kk, 8.f * fg + (float)(i & 3), -0.125f), kk, tc); PIN(tab[i >> 2][i & 3]);
      }
    }
    TX(26);
    WAITV(0); PIN(fh); miss = (u32)__builtin_amdgcn_ballot_w64(fh != tag);
    fneed(FMASK); h_issue(0); h_issue(1); h_issue(2);
    SB();
    u32 ta[8];
    auto taf = [&]() {
#pragma unroll
    for (int i = 0; i < 8; ++i) {
      const int kb = 16 * i + 4 * w + g, k = (lane & 4) ? (kb ? 256 - kb : 128) : kb, rho = 64 * (k & 3) + (k >> 2);
      ta[i] = L + rho * 128 + 16 * ((((lane >> 1) & 4) | pq) ^ (((rho >> 1) & 1) | ((rho >> 3) & 1) << 1)); PIN(ta[i]);
    }
    };
    WAITV(8);
    TX(27);
    u64 ft;
    u32x4 tv[8], gv[2];
    f32x2 o[8][2];
    sfor<0, 16>([&](auto u_) {
      constexpr int u = decltype(u_)::v;
      if (u) WAITV(g2_wait(u));
      BAR();
      ITS(2, u);
      PINS(rot);
      if (u == FTU && w == 0) st2c(tag, wsr, fo, WS_FT);
      if (u == 3 || u == 6) { PIN(fh); miss &= (u32)__builtin_amdgcn_ballot_w64(fh != tag); }
      if constexpr (u + 3 < 16) fneed(3u << (2 * u + 6));
      if (u == 0 || u == 3) fload();
#pragma unroll
      for (int e = g2_c0(u); e < g2_c0(u + 1); ++e) c_piece(e >> 2, e & 3);
      if (u + 3 < 16) h_issue(u + 3);
      if constexpr (u >= TU && u < TU + 4) {
#pragma unroll
        for (int i = 2 * (u - TU); i < 2 * (u - TU) + 2; ++i) tv[i] = bl4c<0>(wsr, vl, WS_T + (s2 * 16 + ct) * 32768 + i * 4096 + w * 1024);
        if (u < TU + 2) gq[(u - TU) & 1] = bl1c(wsr, vGo + 128 * (u - TU), 0); else if (u == TU + 2) gv[0] = bl4c<0>(wsr, WS_G + tt * 512 + l15 * 16, 0); else gv[1] = bl4c<256>(wsr, WS_G + tt * 512 + (15 - l15) * 16, 0);
      }
      if (u == TU - 5) asm volatile("buffer_load_dwordx2 %0, %1, %2, %3 offen sc1" : "=v"(ft) : "v"(lane * 8), "s"(wsr), "s"(WS_FT + rep + w * 512) : "memory");
      if (u == TU - 1) { PIN(ft); if (__builtin_expect(!__all(ft == tag), 0)) pollq(wsr, WS_FT + rep + w * 512, lane, tag); }
      SB();
    });
    taf();
    WAITV(0);
#pragma unroll
    for (int i = 0; i < 8; ++i) { PIN(tv[i]); lw<u32x4>(ta[i], tv[i]); }
    LBAR();
    hart256<true>(o, L + (8 * g + q) * 128 + 16 * ((4 * (pq >> 1) + w) ^ (((q >> 1) & 1) | ((g & 1) << 1))) + 8 * (pq & 1), tab, kap, false);
    PIN(gq[0]); PIN(gq[1]); PIN(gv[0]); PIN(gv[1]);
#pragma unroll
    for (int i = 0; i < 8; ++i) {
      const float gg = __uint_as_float(gv[i >> 2][i < 4 ? i : (i + 3) & 3]), og = rbf(1.f - gg);
      const int pos = i < 4 ? l15 : 15 - l15;
#pragma unroll
      for (int hf = 0; hf < 2; ++hf)
        lw<f32x2>(L + 65536 + pos * 256 + 16 * ((8 * hf + 2 * w + (g & 1)) ^ pos) + 8 * (g >> 1) + 4096 * (i < 4 ? 2 * i : 2 * ((i + 3) & 3) + 1), fma2(f32x2{og, og}, f32x2{bf2f(sk[i][hf] & 0xffffu), bfhi(sk[i][hf])}, gg * o[i][hf]));
    }
    TX(30);
  } else {
    GETARGS();
    lw<u32>(L + L_SYNC + 4 * wv, 0u);
    const int r0 = 0; const u32 vA = 0, vP = (64 * pt + 32 * (w & 1) + (lane & 31)) * 2;
    ARGS();
    u32 q_dt = blh(mkr((const void*)ka[3], 2048), vP), q_lr = blh(mkr((const void*)ka[4], 2048), vP), q_li = blh(mkr((const void*)ka[5], 2048), vP);
    const int wn = w & 1, wm = w >> 1;
    const int hpos = 8 * wm + lc8, hsh = (tt ^ (tt >> 1)) & 1;
    u32 vH = (u32)(lr8 & 1) * (1u << 20) + hsh * (1u << 19) + (tt >> 1) * 65536 + (2 * pt + wn) * 1024 + g * 256 + hpos * 16;
    const u32 vAg = WS_AGG + (u32)((pt * 2 + wn) * 16 + l15) * 16;
    const int kap = 16 * sh + l15, np = (16 - l15) & 15; const bool sp = kap == 0;
    const int ecol = 8 * w + 4 * (g & 1) + 2 * (g >> 1);
    TS(19);
    u32x4 tab[2][4];
    hart_table(tab, kap, sp, g);
#pragma unroll
    for (int i = 0; i < 8; ++i) PIN(tab[i >> 2][i & 3]);
    u32 ua[6], hap = L + L_RA + (8 * g + q) * 128 + 16 * ((4 * (pq >> 1) + w) ^ (((q >> 1) & 1) | ((g & 1) << 1))) + 8 * (pq & 1);
#pragma unroll
    for (int i = 0; i < 2; ++i) {
      const int lo = i ? np : l15;
      ua[i] = L + L_V + lo * 256 + ((2 * ecol) ^ ((lo & 7) << 4)); PIN(ua[i]);
    }
    ua[2] = (ua[1] ^ 64) + (sp ? 0 : 48 * 256); ua[3] = (ua[1] ^ 64) + (sp ? 32 * 256 : 16 * 256); ua[4] = (ua[0] ^ 64) + (sp ? 16 * 256 : 0);
    PIN(ua[2]); PIN(ua[3]); PIN(ua[4]); PIN(hap);
    c2 lam, coef, l8, l16;
    WAITV(0); PIN(q_dt); PIN(q_lr); PIN(q_li);
    {
      const float lre = bf2f(q_lr), lim = bf2f(q_li);
      const float dt = rbf(expf(bf2f(q_dt))), mg = expf(dt * lre);
      float sn, cs; sincosf(dt * lim, &sn, &cs);
      const float nr = mg * cs - 1.f, ni = mg * sn;
      const bool big = fabsf(lre) >= fabsf(lim);
      const float u = big ? lre : lim, v = big ? lim : lre, t = v * __builtin_amdgcn_rcpf(u), rd = 2.f * __builtin_amdgcn_rcpf(u + v * t);
      lam = {spread16(mg * cs), spread16(ni)};
      coef = {spread16(rd * (big ? nr + ni * t : nr * t + ni)), spread16(rd * (big ? ni - nr * t : ni * t - nr))};
      l8 = csq(csq(csq(lam))); l16 = csq(l8);
      pinc(lam); pinc(coef); pinc(l8); pinc(l16);
    }
    TS(22);
    SB();
      u32 aa[2][2], ab[2][2];
#pragma unroll
      for (int i = 0; i < 4; ++i) {
        const int co = 16 * ((4 * (i & 1) + g) ^ (l15 >> 1)) + (i >> 1) * 65536;
        aa[i & 1][i >> 1] = L + (64 * wm + l15) * 128 + co; ab[i & 1][i >> 1] = L + 16384 + (64 * wn + l15) * 128 + co; PIN(aa[i & 1][i >> 1]); PIN(ab[i & 1][i >> 1]);
      }
    LBAR();
    TS(1);
    {
      f32x2 o[8][2];
      hart256<false>(o, hap, tab, kap, sp);
#pragma unroll
      for (int i = 0; i < 8; ++i) {
        const u32 a = i == 0 ? ua[0] : i == 1 ? ua[0] + 32 * 256 : i == 2 ? ua[2] : i == 3 ? ua[3] : i == 4 ? ua[4] : i == 5 ? ua[1] + 16 * 256 : i == 6 ? ua[1] + 48 * 256 : ua[4] + 32 * 256;
        lw<u32>(a, pk2(o[i][0])); lw<u32>(a + 128, pk2(o[i][1]));
      }
    }
    gpost(L + L_SYNC, w, 1);
    SB();
    c2 pw[8], wt[4], lsa, lsb, l32 = csq(l16), l64 = csq(l32);
    u32 wa[8], ra[8], pa[4], vAs = lane < 16 ? vAg : WS_END, sa = L + L_SEG + wn * 256 + l15 * 16;
#pragma unroll
    for (int n = 0; n < 4; ++n) { pa[n] = vAg + max(tt - 1 - g - 4 * n, 0) * 8192; PIN(pa[n]); }
    u32 sw = sa + (4 * wm + g) * 512;
    pinc(l32); pinc(l64); PIN(vAs); PIN(sa); PIN(sw); PIN(vH);
    auto pwf = [&]() {
      const c2 one = {{1.f, 1.f}, {0.f, 0.f}}, zero = {{0.f, 0.f}, {0.f, 0.f}};
      const c2 l128 = csq(l64), l256 = csq(l128), l512 = csq(l256);
      lsa = cmul(cmul(coef, csel(g & 1, l16, one)), cmul(csel(g & 2, l32, one), csel(wm, l64, one))); lsb = cmul(lsa, l8);
      wt[0] = cmul(csel(g & 1, l128, one), csel(g & 2, l256, one));
#pragma unroll
      for (int n = 1; n < 4; ++n) wt[n] = cmul(wt[n - 1], l512);
#pragma unroll
      for (int n = 0; n < 4; ++n) { wt[n] = csel(tt - 1 - g - 4 * n >= 0, wt[n], zero); pinc(wt[n]); }
      pinc(lsa); pinc(lsb);
    };
    auto pwg = [&]() {
      pw[0] = lam; pw[1] = csq(lam); pw[2] = cmul(pw[1], lam); pw[3] = csq(pw[1]); pw[4] = cmul(pw[3], lam); pw[5] = csq(pw[2]); pw[6] = cmul(pw[3], pw[2]); pw[7] = l8;
#pragma unroll
      for (int s = 1; s < 7; ++s) pinc(pw[s]);
    };
    u64 tag;
    f32x4 acc[4][4];
    {
      LBAR();
      TS(2);
      u64 cy0 = 0; CY0();
      {
        bf16x8 A[2][2][4], B[2][2][4];
        auto rd = [&](int j, int i) { const int sl = (j + 3) & 3, ks = i & 1, f = (i >> 1) & 3; const u32 bs = (sl & 1) * 32768 + f * 2048;
          if (i < 8) B[j & 1][ks][f] = lr<bf16x8>(ab[ks][sl >> 1] + bs); else A[j & 1][ks][f] = lr<bf16x8>(aa[ks][sl >> 1] + bs); };
#pragma unroll
        for (int i = 0; i < 16; ++i) rd(0, i);
#pragma unroll
        for (int j = 0; j < 16; ++j) {
#pragma unroll
          for (int qq = 0; qq < 32; ++qq) {
            const int p = qq >> 1, m = p >> 2, n = (m & 1) ? 3 - (p & 3) : (p & 3), ks = (qq ^ p) & 1;
            if (qq == BQ && j < 17 - LEAD) LBAR();
            SB();
            if (j || (qq & 1)) mmc(acc[m][n], A[j & 1][ks][m], B[j & 1][ks][n]); else mmz(acc[m][n], A[j & 1][ks][m], B[j & 1][ks][n]);
            SB();
            if (j < 15 && qq >= BQ && qq < BQ + 16) rd(j + 1, qq - BQ);
          }
        }
      }
      NOPS();
      CY1(20);
    }
    TS(3);
    c2 h[2][8];
#pragma unroll
    for (int s = 0; s < 8; ++s) {
#pragma unroll
      for (int ps = 0; ps < 2; ++ps) {
        const f32x4 vr = acc[s >> 1][2 * ps], vi = acc[s >> 1][2 * ps + 1];
        h[ps][s] = (s & 1) ? c2{vr.zw, vi.zw} : c2{vr.xy, vi.xy};
        if (s) { const c2 a = bc(lam, ps); h[ps][s].r = fma2(-a.i, h[ps][s - 1].i, h[ps][s].r); h[ps][s].i = fma2(a.i, h[ps][s - 1].r, h[ps][s].i); }
      }
      SB();
#pragma unroll
      for (int ps = 0; ps < 2; ++ps) if (s) { const c2 a = bc(lam, ps); h[ps][s].r = fma2(a.r, h[ps][s - 1].r, h[ps][s].r); h[ps][s].i = fma2(a.r, h[ps][s - 1].i, h[ps][s].i); }
      SB();
    }
    const c2 ea = {{h[0][7].r.x, h[1][7].r.x}, {h[0][7].i.x, h[1][7].i.x}};
    { const c2 e = cfma(l8, ea, c2{{h[0][7].r.y, h[1][7].r.y}, {h[0][7].i.y, h[1][7].i.y}});
      lw<f32x4>(sw, f32x4{e.r.x, e.r.y, e.i.x, e.i.y}); }
    gbar(L + L_SYNC, w, 2);
    u32x4 gx[4], gw[2];
    c2 cin;
    {
      c2 e[8];
#pragma unroll
      for (int j = 0; j < 8; ++j) e[j] = asc(lr<f32x4>(sa + j * 512));
      SB();
      const c2 e01 = cfma(l16, e[0], e[1]), e23 = cfma(l16, e[2], e[3]), e45 = cfma(l16, e[4], e[5]), e67 = cfma(l16, e[6], e[7]);
      const c2 e03 = cfma(l32, e01, e23), e47 = cfma(l32, e45, e67), tot = cfma(l64, e03, e47);
      st4c(__builtin_bit_cast(u32x4, f32x4{tot.r.x, tot.r.y, tot.i.x, tot.i.y}), wsr, vAs, wm ? tt * 8192 : WS_END);
      SB();
      const c2 zero = {{0.f, 0.f}, {0.f, 0.f}};
      const c2 c0 = csel(wm, e03, zero), c2_ = cfma(l32, c0, csel(wm, e45, e01)), cb = csel(g & 2, c2_, c0);
      cin = csel(g & 1, cfma(l16, cb, csel(wm, csel(g & 2, e[6], e[4]), csel(g & 2, e[2], e[0]))), cb);
    }
    pinc(cin);
    TS(4);
    SB();
    auto gload = [&]()
      { const u32 xo = (bid * 8 + 2 * w) * 2048, vl = lane * 16; const rsrc_t gr = mkr((const void*)kb_[3], 2048);
        gx[0] = bl4<0>(xr, vl, xo); gx[1] = bl4<1024>(xr, vl, xo); gx[2] = bl4<2048>(xr, vl, xo); gx[3] = bl4<3072>(xr, vl, xo); gw[0] = bl4<0>(gr, vl, 0); gw[1] = bl4<1024>(gr, vl, 0); };
    gload();
    SB();
    c2 ca, cz; u32x4 v[4];
    auto poll = [&]() {
#pragma unroll
      for (int n = 0; n < 4; ++n) v[n] = bl4c<0>(wsr, pa[n], 0);
    };
    {
#pragma unroll
      for (int s = 0; s < 8; ++s) {
        f32x2 t[4];
#pragma unroll
        for (int ps = 0; ps < 2; ++ps) { const c2 a = bc(coef, ps); t[2 * ps] = a.r * h[ps][s].r; t[2 * ps + 1] = a.i * h[ps][s].r; }
        SB();
#pragma unroll
        for (int ps = 0; ps < 2; ++ps) { const c2 a = bc(coef, ps); h[ps][s].r = fma2(-a.i, h[ps][s].i, t[2 * ps]); h[ps][s].i = fma2(-a.r, h[ps][s].i, -t[2 * ps + 1]); }
        SB();
      }
#pragma unroll
      for (int s = 0; s < 16; ++s) pinc(h[s & 1][s >> 1]);
      SB();
      poll();
      SB();
      ca = cmul(coef, cin); cz = cmul(coef, cfma(l8, cin, ea));
      pinc(ca); pinc(cz);
      const u32 hs = L + 65536 + w * 8192;
#pragma unroll
      for (int i = 0; i < 8; ++i) {
        wa[i] = hs + l15 * 128 + ((4 * g + 16 * i) ^ (8 * (l15 & 1) + 4 * (l15 & 4) + 16 * (l15 & 2) + 8 * (l15 & 8))); PIN(wa[i]);
        ra[i] = hs + (8 * g + q) * 128 + ((8 * pq) ^ (8 * (q & 1) + 16 * (q & 2) + 64 * (g & 1)) ^ (16 * i)); PIN(ra[i]);
      }
    }
    SB();
    pwf();
    SB();
    pwg();
    SB();
    auto gate = [&]()
    {
      const u32 vG = WS_G + (u32)(((2 * w + (lane & 1)) * 2 + (((bid >> 4) ^ (bid >> 5)) & 1)) * 128 + ((bid >> 5) & 1) * 64 + (bid & 15) * 4 + (bid >> 6)) * 4;
      float sm[2] = {0.f, 0.f};
      PIN(gw[0]); PIN(gw[1]);
#pragma unroll
      for (int i = 0; i < 4; ++i) {
        PIN(gx[i]);
#pragma unroll
        for (int e = 0; e < 4; ++e) { const u32 a = gx[i][e], b = gw[i & 1][e]; sm[i >> 1] = __builtin_amdgcn_fdot2_f32_bf16(__builtin_bit_cast(bf16x2, a), __builtin_bit_cast(bf16x2, b), sm[i >> 1], false); }
      }
      sm[0] = dppadd<0xB1>(sm[0]); sm[1] = dppadd<0xB1>(sm[1]);
      float t = (lane & 1) ? sm[1] : sm[0];
      t = dppadd<0x4E>(t); t = dppadd<0x124>(t); t = dppadd<0x128>(t); t = sum32(sum16(t));
      __builtin_amdgcn_raw_buffer_store_b32(__float_as_uint(rbf(1.f / (1.f + expf(-rbf(t))))), wsr, lane < 2 ? vG : WS_END, 0, 16);
    };
    WAITV(4); gate();
    SB();
    TS(5);
    WAITV(1);
    if (tt > 0) {
      auto vok = [&]() { u32 mx = 0;
#pragma unroll
        for (int n = 0; n < 4; ++n) { PIN(v[n]); mx = umax3(umax3(mx, v[n].x, v[n].y), v[n].z, v[n].w); }
        return __all(mx != SENT); };
      if (!vok()) {
        NOUNROLL for (u32 spn = 0; spn < SPIN_CAP; ++spn) {
#pragma unroll
          for (int n = 0; n < 4; ++n) v[n] = bl4c<0>(wsr, pa[n], 0);
          WAITV(0);
          if (vok()) break;
        }
      }
      c2 cb = cfma(wt[0], asc(__builtin_bit_cast(f32x4, v[0])), cmul(wt[1], asc(__builtin_bit_cast(f32x4, v[1]))));
      const c2 cd = cfma(wt[2], asc(__builtin_bit_cast(f32x4, v[2])), cmul(wt[3], asc(__builtin_bit_cast(f32x4, v[3]))));
      cb.r += cd.r; cb.i += cd.i;
      cb = c2{{sum32(sum16(cb.r.x)), sum32(sum16(cb.r.y))}, {sum32(sum16(cb.i.x)), sum32(sum16(cb.i.y))}};
      ca = cfma(lsa, cb, ca); cz = cfma(lsb, cb, cz);
    } else {
#pragma unroll
      for (int n = 0; n < 4; ++n) PIN(v[n]);
    }
    TS(6);
    {
      const c2 cc[2] = {{{ca.r.x, cz.r.x}, {ca.i.x, cz.i.x}}, {{ca.r.y, cz.r.y}, {ca.i.y, cz.i.y}}};
      u32x4 hq[8];
#pragma unroll
      for (int s = 0; s < 8; ++s) {
#pragma unroll
        for (int ps = 0; ps < 2; ++ps) { const c2 a = bc(pw[s], ps); h[ps][s].r = fma2(-a.i, cc[ps].i, h[ps][s].r); h[ps][s].i = fma2(-a.i, cc[ps].r, h[ps][s].i); }
        SB();
#pragma unroll
        for (int ps = 0; ps < 2; ++ps) { const c2 a = bc(pw[s], ps); h[ps][s].r = fma2(a.r, cc[ps].r, h[ps][s].r); h[ps][s].i = fma2(-a.r, cc[ps].i, h[ps][s].i); }
        SB();
#pragma unroll
        for (int ps = 0; ps < 2; ++ps) { lw<u32>(wa[s] + ps * 2048, pk2(h[ps][s].r)); lw<u32>(wa[s] + ps * 2048 + 4096, pk2(h[ps][s].i)); }
        FENCE();
        if (s & 1) {
          hq[s - 1] = __builtin_bit_cast(u32x4, cat(trr(ra[s - 1]), trr(ra[s] + 512))); hq[s] = __builtin_bit_cast(u32x4, cat(trr(ra[s - 1] + 4096), trr(ra[s] + 4608)));
          if (s > 1) { st4c(hq[s - 3], wsr, vH, WS_H + ((s - 3) >> 1) * (2u << 20)); st4c(hq[s - 2], wsr, vH, WS_H + ((s - 3) >> 1) * (2u << 20) + 32768); }
        }
        SB();
      }
      st4c(hq[6], wsr, vH, WS_H + 3 * (2u << 20)); st4c(hq[7], wsr, vH, WS_H + 3 * (2u << 20) + 32768);
    }
    TS(7);
    {
      bf16x8 cp[6][4];
      u32 ha = L + 98304 + wm * 8192 + lane * 16, ca[2][2];
#pragma unroll
      for (int i = 0; i < 4; ++i) { ca[i & 1][i >> 1] = L + (i >> 1) * 65536 + wn * 8192 + l15 * 128 + 16 * ((4 * (i & 1) + g) ^ (l15 >> 1)); PIN(ca[i & 1][i >> 1]); }
      PIN(ha);
      tag = lr<u64>(L + L_SYNC + 32); PIN(tag);
      WAITV(0);
      gbar(L + L_SYNC, w, 3);
      if (w == 0) st2c(tag, wsr, lane < NREP ? lane * 2048 : WS_END, WS_FH + (tt * 16 + pt) * 8);
      { u64 pc; u32 bk, nb; asm volatile("s_getpc_b64 %0\n.Lcode_m%=:\n\ts_mov_b32 %1, .Lcode_m%=-.Lcode_lo\n\ts_mov_b32 %2, .Lcode_hi-.Lcode_lo" : "=s"(pc), "=s"(bk), "=s"(nb));
        if (w == 1) touch(mkr((const void*)((pc - bk) & ~127ull), min(nb, 61440u)), (lane * 32 + jj) * 128, L); }
      TS(28);
      BAR();
#pragma unroll
      for (int i = 0; i < 24; ++i) cp[i >> 2][i & 3] = lr<bf16x8>(ca[(i >> 2) & 1][0] + (i >> 3) * 16384 + (i & 3) * 2048);
      WAITV(0); LBAR();
#pragma unroll
      for (int i = 0; i < 24; ++i) PIN(cp[i >> 2][i & 3]);
      TS(12);
      u64 cy0 = 0; CY0();
      {
        bf16x8 H[2][2][4], Cf[2][2][4];
        auto rd = [&](int u, int i) { const int ks = i & 1, f = (i >> 1) & 3;
          if (i < 8) { if (u >= 3) Cf[u & 1][ks][f] = lr<bf16x8>(ca[ks][(u % 6) >> 2] + ((u % 6) & 3) * 16384 + f * 2048); } else H[u & 1][ks][f] = lr<bf16x8>(ha + (u & 3) * 16384 + f * 2048 + ks * 1024); };
#pragma unroll
        for (int i = 8; i < 16; ++i) rd(0, i);
#pragma unroll
        for (int u = 0; u < 16; ++u) {
#pragma unroll
          for (int qq = 0; qq < 32; ++qq) {
            const int p = qq >> 1, m = p >> 2, n = (m & 1) ? 3 - (p & 3) : (p & 3), ks = (qq ^ p) & 1;
            if (qq == BQ && u < 15) LBAR();
            SB();
            const bf16x8 cf = u < 3 ? cp[(2 * u + ks) % 6][n] : Cf[u & 1][ks][n];
            if (u || (qq & 1)) mmc(acc[m][n], cf, H[u & 1][ks][m]); else mmz(acc[m][n], cf, H[u & 1][ks][m]);
            SB();
            if (u < 15 && qq >= BQ && qq < BQ + 16) rd(u + 1, qq - BQ);
          }
        }
      }
      NOPS();
      CY1(21);
    }
    TS(13);
    {
      const u32 vl = lane * 16;
#pragma unroll
      for (int m = 0; m < 4; ++m) st4c(__builtin_bit_cast(u32x4, pk8(acc[m][2], acc[m][3])), wsr, g * 256 + (l15 ^ (4 * g)) * 16, WS_X + (((bid ^ 1) * 4 + w) * 4 + m) * 1024);
      gq[0] = bl1c(wsr, vGo, 0); gq[1] = bl1c(wsr, vGo + 128, 0);
      BAR();
      WAITV(2);
      st2c(tag, wsr, lane == 0 ? 0 : WS_END, WS_FP + ((bid ^ 1) * 4 + w) * 128);
      TS(29);
      const u32 pa = L + 98304 + wm * 16384 + wn * 2048 + g * 256 + (l15 ^ (4 * g)) * 16;
#pragma unroll
      for (int i = 0; i < 8; ++i) lw<f32x4>(pa + (i >> 1) * 4096 + (i & 1) * 1024, acc[i >> 1][i & 1]);
      NOUNROLL for (u32 spn = 0; spn < SPIN_CAP; ++spn) {
        u64 a; asm volatile("buffer_load_dwordx2 %0, off, %1, %2 sc1\n\ts_waitcnt vmcnt(0)" : "=&v"(a) : "s"(wsr), "s"(WS_FP + (bid * 4 + w) * 128) : "memory");
        if (__all(a == tag)) break;
      }
#pragma unroll
      for (int m = 0; m < 4; ++m) bldsc(wsr, vl, WS_X + (bid * 4 + w) * 4096 + m * 1024, L + w * 4096, 131072 + m * 1024);
      PIN(gq[0]); PIN(gq[1]);
      TS(31);
    }
  }
  if (!rh) TS(14);
  {
    const rsrc_t outr = mkr((const void*)outp, 8u << 20);
    u32 pa[2], xa[2], ba[4], oa[2]; float og[2];
#pragma unroll
    for (int hi = 0; hi < 2; ++hi) {
      const int row = lr8 + 8 * hi, s1 = 32 * wv + row + 16 * (sh ^ (w & 1));
      pa[hi] = L + 98304 + wv * 4096 + lc8 * 256 + (row ^ (4 * pq)) * 16; oa[hi] = (u32)(s2 + 8 * s1) * 4096u + lc8 * 16; og[hi] = rbf(1.f - __uint_as_float(gq[hi]));
      ba[2 * hi] = L + 65536 + (wv * 16 + row) * 256 + 16 * (lc8 ^ row); ba[2 * hi + 1] = ba[2 * hi] ^ 128;
      xa[hi] = L + 131072 + rh * 8192 + w * 1024 + pq * 256 + (row ^ (4 * pq)) * 16 + (lc8 >> 2) * 8; PIN(xa[hi]);
      PIN(pa[hi]); PIN(oa[hi]); PIN(og[hi]); PIN(ba[2 * hi]); PIN(ba[2 * hi + 1]);
    }
    WAITV(0); LBAR();
    f32x4 v[4][2]; u32x2 xv[4];
#pragma unroll
    for (int i = 0; i < 4; ++i) {
      v[i][0] = lr<f32x4>(pa[i >> 1] + (i & 1) * 2048); v[i][1] = lr<f32x4>(ba[i]); xv[i] = lr<u32x2>(xa[i >> 1] + (i & 1) * 4096);
    }
    SB();
#pragma unroll
    for (int i = 0; i < 4; ++i)
      st4(__builtin_bit_cast(u32x4, og[i >> 1] * (v[i][0] + f32x4{bf2f(xv[i].x & 0xffffu), bfhi(xv[i].x), bf2f(xv[i].y & 0xffffu), bfhi(xv[i].y)}) + v[i][1]), outr, oa[i >> 1], 128 * ct + 2048 * (i & 1));
  }
  if (!rh) TS(15);
}

extern "C" void kernel_launch(void* const* d_in, const int* in_sizes, int n_in, void* d_out, int out_size, void* d_ws, size_t ws_size, hipStream_t stream) {
  (void)in_sizes; (void)n_in; (void)out_size; (void)ws_size;
  (void)hipFuncSetAttribute((const void*)k_all, hipFuncAttributeMaxDynamicSharedMemorySize, L_TOT);
  const u16* const* in = (const u16* const*)d_in;
  k_all<<<256, 512, L_TOT, stream>>>(in[0], in[6], in[7], in[10], in[4], in[5], in[1], in[2], in[8], in[9], in[11], in[12], (char*)d_ws, (float*)d_out);
}
```

```cpp
#include <hip/hip_runtime.h>
#include <hip/hip_bf16.h>
#include <stdint.h>

typedef unsigned short u16;
typedef unsigned int u32;
typedef unsigned long long u64;
typedef __attribute__((ext_vector_type(4))) short s16x4;
typedef __attribute__((ext_vector_type(8))) __bf16 bf16x8;
typedef __attribute__((ext_vector_type(2))) __bf16 bf16x2;
typedef __attribute__((ext_vector_type(2))) float f32x2;
typedef __attribute__((ext_vector_type(4))) float f32x4;
typedef __attribute__((ext_vector_type(4))) u32 u32x4;
typedef __attribute__((ext_vector_type(2))) u32 u32x2;
#define DI __device__ __forceinline__
#define LDSP __attribute__((address_space(3)))
#define RLX __ATOMIC_RELAXED, __HIP_MEMORY_SCOPE_AGENT

#define NREP 8
constexpr u32 WS_E = 0, WS_AGG = 65536, WS_FP = 0x40000, WS_FU = 0x60000, WS_FH = WS_FU + NREP * 2048, WS_FT = WS_FH + NREP * 2048, WS_G = WS_FT + NREP * 2048;
constexpr u32 WS_U = 1u << 20, WS_T = 5u << 20, WS_H = 9u << 20, WS_X = 17u << 20, WS_END = 21u << 20;
constexpr u32 L_RA = 65536, L_W = 98304, L_WS = 17408, L_V = 133120, L_CH = 5264, L_PS = 144, L_SYNC = 154176, L_SEG = 154688, L_TOT = 163840;
constexpr u32 SPIN_CAP = 1u << 18, SENT = 0xffffffffu;

DI float bf2f(u32 h) { return __builtin_bit_cast(float, h << 16); }
DI float bfhi(u32 h) { return __builtin_bit_cast(float, h & 0xffff0000u); }
DI u32 pk2(float a, float b) { f32x2 v = {a, b}; return __builtin_bit_cast(u32, __builtin_convertvector(v, bf16x2)); }
DI u32 pk2(f32x2 v) { return __builtin_bit_cast(u32, __builtin_convertvector(v, bf16x2)); }
DI float rbf(float a) { return bf2f(pk2(a, a) & 0xffffu); }
typedef __amdgpu_buffer_rsrc_t rsrc_t;
DI rsrc_t mkr(const void* p, u32 bytes) { return __builtin_amdgcn_make_buffer_rsrc((void*)p, 0, bytes, 0x00020000); }
DI void bldsc(rsrc_t r, u32 vo, u32 so, u32 l, u32 lo) { asm volatile("s_add_u32 m0, %3, %4\n\ts_nop 0\n\tbuffer_load_dwordx4 %0, %1, %2 offen sc1 lds" :: "v"(vo), "s"(r), "s"(so), "s"(l), "i"(lo) : "memory", "scc"); }
DI void blds(rsrc_t r, u32 vo, u32 so, u32 l, u32 lo) { asm volatile("s_add_u32 m0, %3, %4\n\ts_nop 0\n\tbuffer_load_dwordx4 %0, %1, %2 offen lds" :: "v"(vo), "s"(r), "s"(so), "s"(l), "i"(lo) : "memory", "scc"); }
template <class T> DI T lr(u32 a) { return *(const LDSP T*)(uintptr_t)a; }
template <class T> DI void lw(u32 a, T v) { *(LDSP T*)(uintptr_t)a = v; }
DI u32x2 trr(u32 a) { return __builtin_bit_cast(u32x2, __builtin_amdgcn_ds_read_tr16_b64_v4i16((LDSP s16x4*)(uintptr_t)a)); }
DI bf16x8 cat(u32x2 a, u32x2 b) { u32x4 v = {a.x, a.y, b.x, b.y}; return __builtin_bit_cast(bf16x8, v); }
DI bf16x8 pk8(f32x4 a, f32x4 b) { u32x4 v = {pk2(a[0], a[1]), pk2(a[2], a[3]), pk2(b[0], b[1]), pk2(b[2], b[3])}; return __builtin_bit_cast(bf16x8, v); }
DI f32x4 mma(bf16x8 a, bf16x8 b, f32x4 c) { return __builtin_amdgcn_mfma_f32_16x16x32_bf16(a, b, c, 0, 0, 0); }
DI void mmz(f32x4& c, bf16x8 a, bf16x8 b) { asm volatile("v_mfma_f32_16x16x32_bf16 %0, %1, %2, 0" : "=&v"(c) : "v"(a), "v"(b)); }
DI void mmc(f32x4& c, bf16x8 a, bf16x8 b) { asm volatile("v_mfma_f32_16x16x32_bf16 %0, %1, %2, %0" : "+v"(c) : "v"(a), "v"(b)); }
DI bf16x8 asf(u32x4 v) { return __builtin_bit_cast(bf16x8, v); }
DI float cosr(float rev) { return __builtin_amdgcn_cosf(rev); }
DI float sinr(float rev) { return __builtin_amdgcn_sinf(rev); }
template <int O> DI u32x4 bl4(rsrc_t r, u32 vo, u32 so) { u32x4 v; asm volatile("buffer_load_dwordx4 %0, %1, %2, %3 offen offset:%4" : "=v"(v) : "v"(vo), "s"(r), "s"(so), "n"(O) : "memory"); return v; }
template <int O> DI u32x4 bl4c(rsrc_t r, u32 vo, u32 so) { u32x4 v; asm volatile("buffer_load_dwordx4 %0, %1, %2, %3 offen offset:%4 sc1" : "=v"(v) : "v"(vo), "s"(r), "s"(so), "n"(O) : "memory"); return v; }
DI u32 bl1c(rsrc_t r, u32 vo, u32 so) { u32 v; asm volatile("buffer_load_dword %0, %1, %2, %3 offen sc1" : "=v"(v) : "v"(vo), "s"(r), "s"(so) : "memory"); return v; }
DI void touch(rsrc_t r, u32 vo, u32 l) { asm volatile("s_add_u32 m0, %2, %3\n\ts_nop 0\n\tbuffer_load_dword %0, %1, 0 offen sc1 lds" :: "v"(vo), "s"(r), "s"(l), "i"(L_SYNC + 256) : "memory", "scc"); }
DI u32 bl1(rsrc_t r, u32 vo, u32 so) { u32 v; asm volatile("buffer_load_dword %0, %1, %2, %3 offen" : "=v"(v) : "v"(vo), "s"(r), "s"(so) : "memory"); return v; }
DI u32 blh(rsrc_t r, u32 vo) { u32 v; asm volatile("buffer_load_ushort %0, %1, %2, 0 offen" : "=v"(v) : "v"(vo), "s"(r) : "memory"); return v; }
DI void st4c(u32x4 v, rsrc_t r, u32 vo, u32 so) { asm volatile("buffer_store_dwordx4 %0, %1, %2, %3 offen sc1\n\ts_nop 1" :: "v"(v), "v"(vo), "s"(r), "s"(so) : "memory"); }
DI void st2c(u64 v, rsrc_t r, u32 vo, u32 so) { asm volatile("buffer_store_dwordx2 %0, %1, %2, %3 offen sc1" :: "v"(v), "v"(vo), "s"(r), "s"(so) : "memory"); }
DI void st4(u32x4 v, rsrc_t r, u32 vo, u32 so) { asm volatile("buffer_store_dwordx4 %0, %1, %2, %3 offen nt\n\ts_nop 1" :: "v"(v), "v"(vo), "s"(r), "s"(so) : "memory"); }
#define POLLSLP 2
#define NOUNROLL _Pragma("unroll 1")
#define SB() __builtin_amdgcn_sched_barrier(0)
template <class T> DI void PINS(T& v) { asm volatile("" : "+s"(v)); }
template <class T> DI void PIN(T& v) { asm volatile("" : "+v"(v)); }
#define NOPS() do { SB(); asm volatile("s_nop 7\n\ts_nop 1"); SB(); } while (0)
#define WAITV(n) asm volatile("s_waitcnt vmcnt(%0)" :: "n"(n) : "memory")
#define WAITL(n) asm volatile("s_waitcnt lgkmcnt(%0)" :: "n"(n) : "memory")
#define FENCE() asm volatile("" ::: "memory")
#define BAR() do { FENCE(); __builtin_amdgcn_s_barrier(); FENCE(); } while (0)
#define LBAR() do { WAITL(0); __builtin_amdgcn_s_barrier(); FENCE(); } while (0)
DI u32 umax3(u32 a, u32 b, u32 c) { return max(max(a, b), c); }
DI void gwait(u32 words, u32 phase) {
  NOUNROLL for (u32 s = 0; s < 16 * SPIN_CAP; ++s) {
    u32x4 v; asm volatile("ds_read_b128 %0, %1\n\ts_waitcnt lgkmcnt(0)" : "=v"(v) : "v"(words) : "memory");
    if (__builtin_amdgcn_readfirstlane(min(min(v.x, v.y), min(v.z, v.w))) >= phase) break;
  }
  FENCE();
}
DI void gpost(u32 words, int wq, u32 phase) { FENCE(); lw<u32>(words + 4 * wq, phase); FENCE(); }
DI void gbar(u32 words, int wq, u32 phase) { gpost(words, wq, phase); gwait(words, phase); }
DI void pollq(rsrc_t wr, u32 off, int lane, u64 tag) {
  NOUNROLL for (u32 s = 0; s < SPIN_CAP; ++s) {
    u64 a; asm volatile("buffer_load_dwordx2 %0, %1, %2, %3 offen sc1\n\ts_waitcnt vmcnt(0)" : "=&v"(a) : "v"(lane * 8), "s"(wr), "s"(off) : "memory");
    if (__all(a == tag)) break;
    __builtin_amdgcn_s_sleep(POLLSLP);
  }
}
#define TSW(n, t) do {} while (0)
#define CY0()
#define CY1(n)
#define ITS(p, j) do {} while (0)
#define TS(n) TSW(n, 0)
#define TX(n) TSW(n, 256)
#define NC0 1
#define NW13 4
#define FMASK 63u
#define FTU 0
#define JF 3
struct c2 { f32x2 r, i; };
DI f32x2 fma2(f32x2 a, f32x2 b, f32x2 c) { return __builtin_elementwise_fma(a, b, c); }
DI c2 cfma(c2 a, c2 b, c2 c) { return {fma2(a.r, b.r, fma2(-a.i, b.i, c.r)), fma2(a.r, b.i, fma2(a.i, b.r, c.i))}; }
DI c2 cmul(c2 a, c2 b) { return {fma2(-a.i, b.i, a.r * b.r), fma2(a.r, b.i, a.i * b.r)}; }
DI c2 csq(c2 a) { return {fma2(a.r, a.r, -a.i * a.i), (a.r + a.r) * a.i}; }
DI c2 bc(c2 a, int ps) { return ps ? c2{a.r.yy, a.i.yy} : c2{a.r.xx, a.i.xx}; }
DI c2 csel(bool c, c2 a, c2 b) { return {c ? a.r : b.r, c ? a.i : b.i}; }
DI c2 asc(f32x4 v) { return {v.xy, v.zw}; }
DI void pinc(c2& a) { PIN(a.r); PIN(a.i); }
DI f32x2 spread16(float v) { const u32 u = __float_as_uint(v); const auto e = __builtin_amdgcn_permlane16_swap(u, u, false, false); return f32x2{__uint_as_float(e[0]), __uint_as_float(e[1])}; }
DI float sum16(float v) { const f32x2 e = spread16(v); return e.x + e.y; }
DI float sum32(float v) { const u32 u = __float_as_uint(v); const auto e = __builtin_amdgcn_permlane32_swap(u, u, false, false); return __uint_as_float(e[0]) + __uint_as_float(e[1]); }
template <int CTRL> DI float dppadd(float v) { return v + __int_as_float(__builtin_amdgcn_mov_dpp(__float_as_int(v), CTRL, 0xf, 0xf, true)); }

DI u32x4 cos8(float a0, float kk, float c2d) {
  float c[8]; c[0] = cosr(a0); c[1] = cosr(fmaf(kk, 4.f, a0));
#pragma unroll
  for (int j = 2; j < 8; ++j) c[j] = fmaf(c2d, c[j - 1], -c[j - 2]);
  return u32x4{pk2(c[0], c[1]), pk2(c[2], c[3]), pk2(c[4], c[5]), pk2(c[6], c[7])};
}
DI void hart_table(u32x4 (&tab)[2][4], int kap, bool sp, int g) {
  const float kp = (float)kap * (1.f / 256.f), km = sp ? 0.125f : kp, cp = 2.f * cosr(4.f * kp), cm = sp ? -2.f : cp;
#pragma unroll
  for (int r = 0; r < 4; ++r) {
    const float kk = r == 1 && sp ? km : -km, fr_ = (float)(32 * g + r);
    tab[0][r] = cos8(fmaf(kp, fr_, -0.125f), kp, cp); tab[1][r] = cos8(fmaf(kk, fr_, -0.125f), kk, cm);
  }
}
template <bool INV> DI void hart256(f32x2 (&o)[8][2], u32 ap, const u32x4 (&tab)[2][4], int kap, bool sp) {
  const float sg1 = (kap & 1) ? -1.f : 1.f, al1 = sp ? 0.f : 1.f;
  const f32x2 sgn = {sg1, sg1}, al = {al1, al1};
  bf16x8 a[4][2]; f32x4 m[2][4][2];
#pragma unroll
  for (int r = 0; r < 4; ++r) { a[r][0] = cat(trr(ap + r * 8192), trr(ap + r * 8192 + 512)); a[r][1] = cat(trr(ap + r * 8192 + 4096), trr(ap + r * 8192 + 4608)); }
  SB();
#pragma unroll
  for (int r = 0; r < 4; ++r)
#pragma unroll
    for (int i = 0; i < 4; ++i) m[i >> 1][r][i & 1] = mma(a[r][i & 1], asf(tab[(i >> 1) ^ (INV & i & 1)][r]), f32x4{});
  SB();
#pragma unroll
  for (int hh = 0; hh < 2; ++hh) {
    f32x2 p[2][4];
#pragma unroll
    for (int i = 0; i < 8; ++i) { const f32x2 sg_ = INV && i >= 4 ? -sgn : sgn; p[i >> 2][i & 3] = hh ? fma2(sg_, m[i >> 2][i & 3][1].zw, m[i >> 2][i & 3][0].zw) : fma2(sg_, m[i >> 2][i & 3][1].xy, m[i >> 2][i & 3][0].xy); }
    const f32x2 s0 = p[0][1] + p[0][3], d0 = p[0][1] - p[0][3], s1 = INV ? p[1][1] + p[1][3] : fma2(al, p[1][1], p[1][3]), d1 = INV ? p[1][1] - p[1][3] : fma2(-al, p[1][3], p[1][1]);
    const f32x2 x0 = sp ? d0 : d1, x1 = sp ? d1 : d0;
#pragma unroll
    for (int sg = 0; sg < 2; ++sg) {
      const f32x2 A = p[sg][0] + p[sg][2], B = p[sg][0] - p[sg][2], S = sg ? s1 : s0, X = sg ? x1 : x0;
      o[4 * sg][hh] = A + S; o[4 * sg + 2][hh] = A - S; o[4 * sg + 1][hh] = B + X; o[4 * sg + 3][hh] = B - X;
    }
  }
  SB();
#pragma unroll
  for (int i = 0; i < 8; ++i) {
    const auto e0 = __builtin_amdgcn_permlane32_swap(__float_as_uint(o[i][0].x), __float_as_uint(o[i][1].x), false, false);
    const auto e1 = __builtin_amdgcn_permlane32_swap(__float_as_uint(o[i][0].y), __float_as_uint(o[i][1].y), false, false);
    o[i][0] = f32x2{__uint_as_float(e0[0]), __uint_as_float(e1[0])}; o[i][1] = f32x2{__uint_as_float(e0[1]), __uint_as_float(e1[1])};
  }
  SB();
#pragma unroll
  for (int i = 0; i < 8; ++i) { const f32x2 a_ = o[i][0], b_ = o[i][1]; o[i][0] = a_ + b_; o[i][1] = a_ - b_; }
}

struct TileD { int k, c, src, w; };
DI TileD tile_desc(int tau, int k1, int kd1) {
  TileD t; const int i = tau >> 1, ka = tau & 1, hi = i >= 5;
  t.c = kd1 + 2 * ka;
  t.src = k1 ? -1 : (kd1 ? ka : hi);
  t.k = 256 * i + (k1 ? k1 : (kd1 ? 128 * ka : -1152 * hi));
  t.w = k1 ? 1 : (kd1 ? 2 : (tau >= 18 ? 0 : ((i & 3) == 0 && i < 8 ? 1 : 2)));
  return t;
}
DI void m0_fwd(bf16x8& o, int n, int l15, int g, int k1, int kd1) {
  const TileD t = tile_desc(8 * n + (l15 >> 1), k1, kd1); f32x4 v[2];
  const float ak = (float)t.k * (-1.f / 2048.f), a0 = fmaf(ak, (float)(2 * g), -0.25f * (float)(l15 & 1)), ac = -0.25f * (float)t.c;
#pragma unroll
  for (int j = 0; j < 8; ++j) {
    const int rs = j & 1;
    const float c = cosr(a0 + ((j & 2) ? ak : 0.f) + ((j & 4) ? ac : 0.f) + (t.src < 0 ? (rs ? 0.125f : -0.125f) : 0.f));
    v[j >> 2][j & 3] = (t.src >= 0 && (t.w == 0 || t.src != rs)) ? 0.f : c;
  }
  o = pk8(v[0], v[1]);
}
DI void tr2(bf16x8& X, bf16x8& Y, bf16x8 A, bf16x8 B, u32 wr, u32 rd) {
  const u32x4 a = __builtin_bit_cast(u32x4, A), b = __builtin_bit_cast(u32x4, B);
  FENCE(); lw<u32x2>(wr, a.xy); lw<u32x2>(wr + 512, b.xy); lw<u32x2>(wr + 1024, a.zw); lw<u32x2>(wr + 1536, b.zw); FENCE();
  X = cat(trr(rd), trr(rd + 512)); Y = cat(trr(rd + 1024), trr(rd + 1536)); FENCE();
}

template <int O> DI void ldhi(float& v, u32 a) { asm volatile("ds_read_u16_d16_hi %0, %1 offset:%2" : "+v"(v) : "v"(a), "n"(O) : "memory"); }
#define BQ 12
#define LEAD 2
constexpr int g1_wait(int j) { int c = j <= 1 ? 4 : 0; for (int u = j - LEAD + 1; u < j; ++u) c += (u + LEAD < 16 ? 8 : 0) + (u == 15 - 2 * LEAD ? 1 : 0); return c; }
#define SE 2
#define TU 12
#define NE 2
constexpr int g2_c0(int u) { return u >= 13 ? 64 : 16 + 4 * NE + u * (48 - 4 * NE) / 13; }
constexpr bool g2_ok() { for (int u = 0; u < 13; ++u) for (int e = g2_c0(u); e < g2_c0(u + 1); ++e) if (u > e / 4 - 3 || (e / 4 >= 9 && u < e / 4 - 5)) return false; return true; }
static_assert(g2_ok(), "C tile schedule");
constexpr int g2_pre(int u) { return u < 0 ? 0 : g2_c0(u + 1) - g2_c0(u) + (u == 0 || u == 3 ? 1 : 0); }
constexpr int g2_post(int u) { return (u >= TU && u < TU + 4 ? 3 : 0) + (u == TU - 5 ? 1 : 0); }
constexpr int g2_wait(int u) { int c = g2_post(u - 3); for (int v = u - 2; v < u; ++v) c += g2_pre(v) + (v + 3 < 16 ? 4 : 0) + g2_post(v); return c; }
template <int N> struct IC { static constexpr int v = N; };
template <int I, int N, class F> DI void sfor(F&& f) { if constexpr (I < N) { f(IC<I>{}); sfor<I + 1, N>(f); } }

typedef __attribute__((ext_vector_type(8))) u64 u64x8;
typedef __attribute__((ext_vector_type(4))) u64 u64x4;
typedef __attribute__((ext_vector_type(2))) u64 u64x2;
__global__ __launch_bounds__(512, 2) void k_all(const u16*, const u16*, const u16*, const u16*, const u16*, const u16*, const u16*, const u16*,
    const u16*, const u16*, const u16*, const u16*, char*, float*) {
#define GETARGS() u64x8 ka; u64x4 kb_; u64x2 kc; \
  asm volatile("s_load_dwordx16 %0, %3, 0x0\n\ts_load_dwordx8 %1, %3, 0x40\n\ts_load_dwordx4 %2, %3, 0x60" : "=&s"(ka), "=&s"(kb_), "=&s"(kc) : "s"(__builtin_amdgcn_kernarg_segment_ptr()) : "memory")
  extern __shared__ __attribute__((aligned(16))) char lds_[];
  const u32 L = (u32)(uintptr_t)(LDSP char*)lds_;
  const int lane = threadIdx.x & 63, wv = __builtin_amdgcn_readfirstlane(threadIdx.x >> 6), w = wv & 3, rh = wv >> 2, l15 = lane & 15, g = lane >> 4;
  const int bid = blockIdx.x, xcd = bid & 7, jj = bid >> 3, ti = jj & 3, pj = jj >> 2;
  const int tt = (xcd >> 1) * 4 + ti, pt = (xcd & 1) * 8 + pj;
  const int s2 = tt >> 1, sh = tt & 1, ct = pt;
  const int lr8 = lane >> 3, lc8 = lane & 7, q = l15 >> 2, pq = lane & 3;
  int rot = tt;
  u32 gq[2]; u64 outp, wsp;
#define ARGS() asm volatile("s_waitcnt lgkmcnt(0)" : "+s"(ka), "+s"(kb_), "+s"(kc) :: "memory"); \
  char* ws = (char*)kc[0]; outp = kc[1]; wsp = kc[0]; \
  const rsrc_t xr = mkr((const void*)ka[0], 4u << 20), wsr = mkr(ws, WS_END), cr = mkr((const void*)((ct & 8) ? kb_[1] : kb_[0]), 2u << 20); \
  auto issue_a = [&](int rr, int hf) { const int r = (rr + r0) & 3; blds(xr, vA, (8 * r + 1024 * hf) * 2048, L + r * 8192 + hf * 4096 + w * 1024, L_RA); }; \
  u32 vC[2]; vC[0] = (u32)(32 * (ct ^ ((w & 1) * 8)) + 512 * (w >> 1) + lr8) * 2048u + 16 * (lc8 ^ (lr8 >> 1)); vC[1] = vC[0] ^ 64; \
  auto c_piece = [&](int i, int e) { blds(cr, vC[e & 1], e * 8 * 2048 + ((i + rot) & 15) * 128, L + w * 4096, (i % 6) * 16384 + e * 1024); }; \
  const u32 vGo = WS_G + tt * 512 + (u32)((w & 1) * 64 + lr8 * 4 + 2 * rh + (w >> 1)) * 4

  if (rh) {
    GETARGS();
    {
      u64 pc; u32 nb; asm volatile("s_getpc_b64 %0\n.Lcode_lo:\n\ts_mov_b32 %1, .Lcode_hi-.Lcode_lo\n\t.subsection 1\n.Lcode_hi:\n\t.subsection 0" : "=s"(pc), "=s"(nb));
      if (w == 3) touch(mkr((const void*)(pc & ~127ull), min(nb, 61440u)), (lane * 32 + jj) * 128, L);
    }
    const int ii = 8 * (w & 1) + lr8, swz = 16 * (lc8 ^ ((ii >> 1) & 7)), ac = lc8 ^ (((lr8 >> 1) & 1) | ((w & 1) << 1)), r0 = 2 * sh + (ct & 1);
    u32 vB = (u32)(64 * pt + ii) * 2048u + swz, vA = (u32)(s2 + 32 * (8 * w + lr8)) * 2048u + 64 * ct + 16 * (ac & 3) + 1024 * (ac >> 2);
    PIN(vA); PIN(vB);
    lw<u32>(L + L_SYNC + 4 * wv, 0u);
    ARGS();
    const rsrc_t br = mkr((const void*)((w & 2) ? ka[2] : ka[1]), 2u << 20);
    auto issue_b1 = [&](int j, int i) { blds(br, vB, 16 * i * 2048 + ((j + rot) & 15) * 128, L + w * 1024, ((j + 3) & 3) * 32768 + 16384 + i * 4096); };
    issue_a(0, 0); issue_a(0, 1); issue_a(1, 0); issue_a(1, 1);
    SB();
    const u32 vX = (u32)(128 * tt + 16 * (ii >> 2) + 8 * (ii & 1) + 2 * (w >> 1) + ((ii >> 1) & 1)) * 2048u + swz, vl = lane * 16;
    auto issue_x1 = [&](int j, int i) { blds(xr, vX, (64 * (i >> 1) + 4 * (i & 1)) * 2048 + ((j + rot) & 15) * 128, L + w * 1024, ((j + 3) & 3) * 32768 + i * 4096); };
    u64 tag; asm volatile("buffer_load_dwordx2 %0, off, %1, 0 sc1" : "=v"(tag) : "s"(wsr) : "memory");
    if (w == 0) st4c(u32x4{SENT, SENT, SENT, SENT}, wsr, lane < 32 ? vl : WS_END, WS_AGG + tt * 8192 + pt * 512);
    if (w == 2) touch(mkr(ws + WS_FP, WS_G + 8192 - WS_FP), (lane * 256 + bid) * 128, L);
    if (w == 1) touch(mkr((const void*)kb_[3], 2048), ((jj & 15) + 16 * lane) * 128, L);
    const int kap = 16 * sh + l15, ecol = 8 * w + 4 * (g & 1) + 2 * (g >> 1);
    u32 q_ds[2] = {bl1(mkr((const void*)kb_[2], 2048), 2 * ecol, 64 * ct), bl1(mkr((const void*)kb_[2], 2048), 2 * ecol, 64 * ct + 1024)};
    const rsrc_t w0 = mkr((const void*)ka[6], 2048 * 1026), w1 = mkr((const void*)ka[7], 2048 * 1026);
    const u32 rep = (bid & (NREP - 1)) * 2048;
    TX(16);
    const int k1 = xcd * 16 + (jj >> 1), kd1 = jj & 1, cc = kd1 + 2 * (w & 1);
    const bool sp0 = (k1 == 0 && kd1 == 0);
    bf16x8 m0[2], mi[2], m0x, mix[2], fa_r, fa_i, fb_r, fb_i, gb_r, gb_i, ga_r, ga_i; f32x4 t1c, t1s, t2c, t2s;
    u32 vW[5], wa[5], wo[5]; float fr[4][4], fi[4][4], er[4], ei[4];
    const float fl = (float)l15, fg = (float)(4 * g), fc = (float)cc;
    const u32 tw = L + L_SEG + w * 2048 + l15 * 32 + 8 * g, tr = L + L_SEG + w * 2048 + (4 * g + q) * 32 + 8 * pq;
    SB();
    {
      const float st = fmaf(fl, -4.f / 64.f, fc * (-1.f / 64.f));
#pragma unroll
      for (int e = 0; e < 4; ++e) { const float a = (fg + (float)e) * st; t2c[e] = cosr(a); t2s[e] = sinr(a); t1c[e] = cosr(a * (1.f / 16.f)); t1s[e] = sinr(a * (1.f / 16.f)); }
      fa_r = pk8(t2c, -t2s); fa_i = pk8(t2s, t2c); PIN(t1c); PIN(t1s);
      PIN(fa_r); PIN(fa_i);
      const float fm = (float)(l15 < 8 ? l15 : l15 ^ 3) * (-1.f / 16.f); const u32 sl = l15 < 8 ? 0u : 0x80008000u;
#pragma unroll
      for (int e = 0; e < 4; ++e) { const float a = (fg + (float)e) * fm; t2c[e] = cosr(a); t2s[e] = sinr(a); }
      fb_r = pk8(t2c, -t2s); fb_i = asf(__builtin_bit_cast(u32x4, pk8(t2s, t2c)) ^ sl);
      PIN(fb_r); PIN(fb_i);
    }
    u32 xa[2], ua, us, us0, fo, ap0, wp0, tr_, tw_;
    const u32 vo = L + L_V + w * L_CH;
    int wid, wsl; f32x4 uc, us_;
    auto fill = [&](auto k_) {
      constexpr int k = decltype(k_)::v;
      SB();
      if constexpr (k == -4) {
        const float mk = (float)(k1 + 256 * (l15 >> 2)) * (-1.f / 2048.f), mc = -0.25f * (float)(kd1 + (l15 & 2)), ma = fmaf(mk, 0.5f * fg, fmaf((float)(l15 & 1), -0.25f, -0.125f));
        u32x4 f;
#pragma unroll
        for (int e = 0; e < 4; ++e) { const float a = ma + ((e & 1) ? mk : 0.f) + ((e & 2) ? mc : 0.f); f[e] = pk2(cosr(a), -sinr(a)); }
        m0[0] = asf(f); m0[1] = asf(f ^ u32x4{0u, 0x80008000u, 0u, 0x80008000u});
        const float b = fmaf((float)(l15 >> 1), fmaf(fg, -1.f / 32.f, (float)k1 * (-1.f / 2048.f)), fmaf((float)(l15 & 1), 0.75f, fmaf((float)k1, 1.f / 512.f, -0.125f))), b1 = b - 0.25f * (float)kd1;
        const u32 p0 = pk2(cosr(b), sinr(b)), p1 = pk2(cosr(b1), sinr(b1)), sgn = (l15 & 2) ? 0x80008000u : 0u;
        mi[0] = asf(u32x4{p0, p0, p0 ^ sgn, p0 ^ sgn}); mi[1] = asf(u32x4{p1, p1 ^ 0x80008000u, p1 ^ sgn, p1 ^ sgn ^ 0x80008000u});
        if (__builtin_expect(k1 == 0, 0)) { m0_fwd(m0[0], 0, l15, g, 0, kd1); m0_fwd(m0[1], 1, l15, g, 0, kd1); m0_fwd(m0x, 2, l15, g, 0, 0); tr2(mix[0], mix[1], m0x, bf16x8{}, tw, tr); tr2(mi[0], mi[1], m0[0], m0[1], tw, tr); }
        PIN(m0[0]); PIN(m0[1]); PIN(mi[0]); PIN(mi[1]);
      }
      if constexpr (k == -2 || k == -1) { constexpr int i = k + 2; const int n = i ? 63 - kap : kap, rho = 64 * (n & 3) + (n >> 2); xa[i] = L + L_RA + rho * 128 + 16 * (w ^ (((rho >> 1) & 1) | ((rho >> 3) & 1) << 1)) + 2 * (ecol & 7); PIN(xa[i]); }
      if constexpr (k >= 0 && k < 4) {
        const float a = fl * fmaf(fg + (float)k, 4.f / 1024.f, fc * (1.f / 1024.f));
        t2c[k] = cosr(a) * (1.f / 1048576.f); t2s[k] = sinr(a) * (1.f / 1048576.f); uc[k] = cosr(16.f * a); us_[k] = sinr(16.f * a); PIN(t2c); PIN(t2s); PIN(uc); PIN(us_);
      }
      if constexpr (k == 4) { ga_r = pk8(uc, -us_); ga_i = pk8(us_, uc); PIN(ga_r); PIN(ga_i); }
      if constexpr (k >= 5 && k < 9) { const float a = (fg + (g < 2 ? (float)(k - 5) : (float)(8 - k))) * fl * (-1.f / 16.f); uc[k - 5] = cosr(a); us_[k - 5] = sinr(a); PIN(uc); PIN(us_); }
      if constexpr (k == 9) {
        const u32 sg = g < 2 ? 0u : 0x80008000u; const u32x4 t = __builtin_bit_cast(u32x4, pk8(uc, us_));
        gb_r = asf(u32x4{t.x, t.y, t.z ^ sg, t.w ^ sg}); gb_i = asf(u32x4{t.z ^ 0x80008000u, t.w ^ 0x80008000u, t.x ^ sg, t.y ^ sg}); PIN(gb_r); PIN(gb_i);
      }
      if constexpr (k == 12) { ua = L + L_V + (16 * w + g) * 256 + (lr8 & 1) * 128 + 16 * (lc8 ^ g); PIN(ua); }
      if constexpr (k == 13) { us = (u32)(64 * w + 2 * g + (lr8 & 1) + 32 * (sh ^ (w & 1))) * 16384u + lc8 * 16; PIN(us); }
      if constexpr (k == 14) { us0 = (w & 1) && g == 0 ? us ^ (32 * 16384) : us; PIN(us0); }
      if constexpr (k == 11) { fo = lane < NREP ? lane * 2048 + bid * 8 : WS_END; PIN(fo); }
      if constexpr (k == 33) { ap0 = vo + (4 * g + q) * 64 + 16 * pq; PIN(ap0); }
      if constexpr (k == 34) { wp0 = vo + l15 * L_PS + 16 * g; PIN(wp0); }
      if constexpr (k == 35) { tr_ = L + L_V + w * 2 * L_PS + g * L_CH + q * 32 + 8 * pq; PIN(tr_); }
      if constexpr (k == 36) { tw_ = L + L_V + w * 2 * L_PS + q * L_CH + pq * 32 + 8 * g; PIN(tw_); }
      if constexpr (k == 37) { PIN(vC[0]); PIN(vC[1]); }
      if constexpr (k == 40) { wid = 2 * cc + 8 * l15 + 512 * g; PIN(wid); }
      if constexpr (k == 41) { wa[0] = g < 2 ? L + L_W + (w >> 1) * 1056 + ((k1 * 1026) & 15) + wid : L + L_W + (8 + (w >> 1)) * 1056 + ((-k1 * 1026) & 15) + 1664 - wid; PIN(wa[0]); }
      if constexpr (k == 42) { wo[0] = g < 2 ? L + L_W + (8 + (w >> 1)) * 1056 + ((-k1 * 1026) & 15) : L + L_W + (w >> 1) * 1056 + ((k1 * 1026) & 15) + 1024; PIN(wo[0]); }
      if constexpr (k >= 43 && k < 46) { wa[k - 42] = wa[0] + 2112 * (k - 42); wo[k - 42] = wo[0] + 2112 * (k - 42); PIN(wa[k - 42]); PIN(wo[k - 42]); }
      if constexpr (k == 46) {
        if (__builtin_expect(k1 == 0, 0)) {
#pragma unroll
          for (int i = 0; i < 5; ++i) {
            const int kk = tile_desc(w + 4 * i, 0, kd1).k;
            const u32 wp = L + L_W + (kk >> 7) * 1056, wq = L + L_W + (((2048 - kk) & 2047) >> 7) * 1056;
            wa[i] = g < 2 ? wp + wid : wq + 1664 - wid; wo[i] = g < 2 ? wq : wp + 1024;
          }
        }
      }
      if constexpr (k >= 48 && k < 73) {
        constexpr int i = (k - 48) / 5, ph = (k - 48) % 5;
        if (ph == 0) { wid = 64 * (w + 4 * i) + lane; wsl = (wid * 993) >> 16; PIN(wid); PIN(wsl); }
        if (ph == 1) { wsl = min(wsl, 15); wid = 16 * wid - 1056 * wsl; PIN(wid); PIN(wsl); }
        if (ph == 2) { wsl = k1 + (k1 ? 256 : 128) * wsl; PIN(wsl); }
        if (ph == 3) { wsl = wsl < 2048 ? wsl : 4096 - wsl; PIN(wsl); }
        if (ph == 4) { vW[i] = ((wsl * 1026) & ~15) + wid; PIN(vW[i]); }
      }
      if constexpr (k >= 76 && k < 96) {
#pragma unroll
        for (int r = 2 * (k & 1); r < 2 * (k & 1) + 2; ++r) { constexpr int z_ = (k - 76) >> 1; float& z = z_ < 4 ? fr[z_ & 3][r] : z_ < 8 ? fi[z_ & 3][r] : z_ == 8 ? er[r] : ei[r]; z = 0.f; PIN(z); }
      }
      SB();
    };
    auto issue_t = [&](auto j_) {
      constexpr int j = decltype(j_)::v;
      sfor<0, 8>([&](auto i_) { constexpr int i = decltype(i_)::v; if (i < 4) issue_b1(j, i); else issue_x1(j, i - 4); if constexpr (j > 2) fill(IC<8 * j - 8 + i>{}); });
    };
    sfor<-4, 0>(fill);
    TX(17);
    issue_a(2, 0); issue_a(2, 1); issue_a(3, 0); issue_a(3, 1); issue_b1(0, 0); issue_b1(0, 1); issue_b1(0, 2); issue_b1(0, 3);
    WAITV(4); LBAR();
    u32 sk[8][2];
#pragma unroll
    for (int i = 0; i < 8; ++i) { const u32 a = xa[i >> 2] + 2048 * (i < 4 ? i : (i + 3) & 3); sk[i][0] = lr<u32>(a); sk[i][1] = lr<u32>(a + 64); }
    PIN(tag); PIN(q_ds[0]); PIN(q_ds[1]);
    tag += 0x9e3779b97f4a7c15ull;
    if (w == 0) lw<u64>(L + L_SYNC + 32, tag);
    SB();
#pragma unroll
    for (int i = 0; i < 4; ++i) issue_x1(0, i);
    sfor<0, 15>(fill);
    issue_t(IC<1>{});
    {
      u32x4 uq[4];
      gwait(L + L_SYNC, 1);
#pragma unroll
      for (int i = 0; i < 4; ++i) uq[i] = lr<u32x4>((ua ^ (64 * (i & 1))) + 1024 * i);
#pragma unroll
      for (int i = 0; i < 4; ++i) st4c(uq[i], wsr, i == 0 ? us0 : us, WS_U + ct * 1024 + s2 * 128 + i * 8 * 16384);
    }
    if (LEAD == 3) issue_t(IC<2>{});
    TX(18);
    u64 fu;
    const f32x4 z4 = {};
    bf16x8 a0[2][2];
    sfor<0, 17 - LEAD>([&](auto j_) {
      constexpr int j = decltype(j_)::v;
      WAITV(g1_wait(j));
      if (j == 0) LBAR(); else BAR();
      ITS(1, j);
      PINS(rot);
      if (j == JF && w == 0) st2c(tag, wsr, fo, WS_FU);
      if (j == 15 - 2 * LEAD) asm volatile("buffer_load_dwordx2 %0, %1, %2, %3 offen sc1" : "=v"(fu) : "v"(lane * 8), "s"(wsr), "s"(WS_FU + rep + w * 512) : "memory");
      SB();
      if constexpr (j + LEAD < 16) issue_t(IC<j + LEAD>{});
      SB();
      if (j == 15 - LEAD) { PIN(fu); if (__builtin_expect(!__all(fu == tag), 0)) pollq(wsr, WS_FU + rep + w * 512, lane, tag); }
      SB();
    });
#pragma unroll
    for (int i = 0; i < 4; ++i) bldsc(wsr, vl, WS_U + (u32)(2 * k1 + kd1) * 16384u + w * 2048 + (i >> 1) * 8192 + (i & 1) * 1024, L + w * L_CH, L_V + i * 1024);
#pragma unroll
    for (int i = 0; i < NW13; ++i) blds(w0, vW[i], 0, L + w * 1024, L_W + i * 4096);
    WAITV(4 + NW13); BAR();
    ITS(1, 17 - LEAD);
    auto wissue = [&]() {
#pragma unroll
      for (int i = 0; i < 4; ++i) { if (i >= NW13) blds(w0, vW[i], 0, L + w * 1024, L_W + i * 4096); blds(w1, vW[i], 0, L + w * 1024, L_W + L_WS + i * 4096); }
      if (w == 0) { blds(w0, vW[4], 0, L, L_W + 16384); blds(w1, vW[4], 0, L, L_W + L_WS + 16384); }
#pragma unroll
      for (int e = 0; e < 4 * NC0; ++e) c_piece(e >> 2, e & 3);
    };
    {
      WAITV(NW13);
#pragma unroll
      for (int cb = 0; cb < 2; ++cb) { a0[cb][0] = cat(trr(ap0 + 1024 * cb), trr(ap0 + 1024 * cb + 2048)); a0[cb][1] = cat(trr(ap0 + 1024 * cb + 8), trr(ap0 + 1024 * cb + 2056)); }
      FENCE();
      wissue();
      f32x4 d[2][2][2];
#pragma unroll
      for (int i = 0; i < 8; ++i) d[i >> 2][(i >> 1) & 1][i & 1] = mma(a0[(i >> 1) & 1][i & 1], m0[i >> 2], z4);
#pragma unroll
      for (int i = 0; i < 4; ++i) lw<bf16x8>(wp0 + 16 * (i >> 1) * L_PS + 64 * (i & 1), pk8(d[i >> 1][i & 1][0], d[i >> 1][i & 1][1]));
      if (__builtin_expect(sp0, 0)) {
#pragma unroll
        for (int cb = 0; cb < 2; ++cb) { const bf16x8 o = pk8(mma(a0[cb][0], m0x, z4), mma(a0[cb][1], m0x, z4)); if (l15 < 4) lw<bf16x8>(wp0 + 32 * L_PS + 64 * cb, o); }
      }
      gbar(L + L_SYNC + 16, w, 3);
    }
    TX(8);
    auto tiles = [&](auto nt_, auto i0_) {
      constexpr int NT = decltype(nt_)::v, i0 = decltype(i0_)::v;
      const u32 pr = tr_ + i0 * 8 * L_PS, pw_ = tw_ + i0 * 8 * L_PS;
      bf16x8 a[NT]; f32x4 yr[NT], yi[NT];
#pragma unroll
      for (int i = 0; i < NT; ++i) a[i] = cat(trr(pr + i * 8 * L_PS), trr(pr + i * 8 * L_PS + L_PS));
      SB();
#pragma unroll
      for (int i = 0; i < NT; ++i) { yr[i] = mma(a[i], fa_r, z4); yi[i] = mma(a[i], fa_i, z4); }
      SB();
#pragma unroll
      for (int i = 0; i < NT; ++i) { a[i] = pk8(yr[i] * t1c - yi[i] * t1s, yr[i] * t1s + yi[i] * t1c); yr[i] = mma(fb_r, a[i], z4); yi[i] = mma(fb_i, a[i], z4); SB(); }
#pragma unroll
      for (int i = 0; i < NT; ++i) asm volatile("" :: "v"(yr[i]), "v"(yi[i]));
      if (!i0) { WAITV(4 * NC0); gbar(L + L_SYNC + 16, w, 4); }
      if (!i0) TX(9);
#pragma unroll
      for (int i = 0; i < NT; ++i) {
        if (i0) {
#pragma unroll
          for (int r = 0; r < 4; ++r) fr[i][r] = fi[i][r] = er[i] = ei[i] = 0.f;
        }
        ldhi<0>(fr[i][0], wa[i0 + i]); ldhi<128>(fr[i][1], wa[i0 + i]); ldhi<256>(fr[i][2], wa[i0 + i]); ldhi<384>(fr[i][3], wa[i0 + i]);
        ldhi<L_WS>(fi[i][0], wa[i0 + i]); ldhi<L_WS + 128>(fi[i][1], wa[i0 + i]); ldhi<L_WS + 256>(fi[i][2], wa[i0 + i]); ldhi<L_WS + 384>(fi[i][3], wa[i0 + i]);
        ldhi<0>(er[i], wo[i0 + i]); ldhi<L_WS>(ei[i], wo[i0 + i]);
      }
      if (!i0) gwait(L + L_SYNC, 2); else WAITL(0);
#pragma unroll
      for (int i = 0; i < NT; ++i)
#pragma unroll
        for (int r = 0; r < 4; ++r) { PIN(fr[i][r]); PIN(fi[i][r]); if (r == 0) { PIN(er[i]); PIN(ei[i]); } }
      if (cc == 0) {
        const float b0 = lane == 0 ? 0.5f : 0.f, b3 = lane == 32 ? 0.5f : 0.f;
#pragma unroll
        for (int i = 0; i < NT; ++i) {
          float t;
          asm volatile("v_sub_f32 %4, %5, %0\n\tv_fmac_f32 %0, %7, %4\n\tv_add_f32 %4, %6, %1\n\tv_fma_f32 %1, -%7, %4, %1\n\t"
                       "v_sub_f32 %4, %5, %2\n\tv_fmac_f32 %2, %8, %4\n\tv_add_f32 %4, %6, %3\n\tv_fma_f32 %3, -%8, %4, %3"
                       : "+v"(fr[i][0]), "+v"(fi[i][0]), "+v"(fr[i][3]), "+v"(fi[i][3]), "=&v"(t) : "v"(er[i]), "v"(ei[i]), "v"(b0), "v"(b3));
        }
      }
      if (__builtin_expect(k1 == 0, 0)) {
#pragma unroll
        for (int i = 0; i < NT; ++i) { const float wt_ = (float)tile_desc(w + 4 * (i0 + i), 0, kd1).w; yr[i] *= wt_; yi[i] *= wt_; }
      }
      SB();
#pragma unroll
      for (int i = 0; i < NT; ++i) {
        const f32x4 vr = {fr[i][0], fr[i][1], fr[i][2], fr[i][3]}, vi = {fi[i][0], fi[i][1], fi[i][2], fi[i][3]};
        a[i] = pk8(yr[i] * vr - yi[i] * vi, yr[i] * vi + yi[i] * vr); yr[i] = mma(a[i], gb_r, z4); yi[i] = mma(a[i], gb_i, z4);
        SB();
        if (!i0) {
#pragma unroll
          for (int e = 4 * NC0 + (4 - NC0) * i; e < 4 * NC0 + (4 - NC0) * (i + 1); ++e) c_piece(e >> 2, e & 3);
        }
        SB();
      }
#pragma unroll
      for (int i = 0; i < NT; ++i) {
        a[i] = pk8(yr[i] * t2c - yi[i] * t2s, yr[i] * t2s + yi[i] * t2c); yr[i] = mma(a[i], ga_r, z4); yi[i] = mma(a[i], ga_i, z4);
        SB();
      }
#pragma unroll
      for (int i = 0; i < NT; ++i) { lw<u32x2>(pw_ + i * 8 * L_PS, u32x2{pk2(yr[i].xy), pk2(yr[i].zw)}); lw<u32x2>(pw_ + i * 8 * L_PS + L_PS, u32x2{pk2(yi[i].xy), pk2(yi[i].zw)}); }
    };
    tiles(IC<4>{}, IC<0>{});
    if (__builtin_expect(sp0 && w < 2, 0)) tiles(IC<1>{}, IC<4>{});
    TX(23);
    gbar(L + L_SYNC + 16, w, 5);
    TX(24);
    u32x4 tq[4];
    {
      f32x4 o[2][2][2];
      const u32 ap = vo + (4 * g + q) * L_PS + 16 * pq;
#pragma unroll
      for (int cb = 0; cb < 2; ++cb) { a0[cb][0] = cat(trr(ap + 64 * cb), trr(ap + 64 * cb + 16 * L_PS)); a0[cb][1] = cat(trr(ap + 64 * cb + 8), trr(ap + 64 * cb + 16 * L_PS + 8)); }
#pragma unroll
      for (int i = 0; i < 8; ++i) o[i >> 2][(i >> 1) & 1][i & 1] = mma(a0[(i >> 1) & 1][i & 1], mi[i >> 2], z4);
      if (__builtin_expect(sp0, 0)) {
        const u32 ax = vo + (32 + q) * L_PS + 16 * pq;
#pragma unroll
        for (int i = 0; i < 4; ++i) { const u32x2 t = trr(ax + 64 * (i >> 1) + 8 * (i & 1)); const bf16x8 a = cat(t, t); o[0][i >> 1][i & 1] = mma(a, mix[0], o[0][i >> 1][i & 1]); o[1][i >> 1][i & 1] = mma(a, mix[1], o[1][i >> 1][i & 1]); }
      }
      FENCE();
#pragma unroll
      for (int i = 0; i < 4; ++i) lw<bf16x8>(vo + i * 1280 + l15 * 80 + 16 * g, pk8(o[i >> 1][i & 1][0], o[i >> 1][i & 1][1]));
      FENCE();
#pragma unroll
      for (int i = 0; i < 4; ++i) tq[i] = lr<u32x4>(vo + i * 1280 + (lane >> 2) * 80 + pq * 16);
#pragma unroll
      for (int i = 0; i < 4; ++i) PIN(tq[i]);
    }
    TX(10);
    u32x4 tab[2][4];
    if (__builtin_expect(bid == 0 && w == 0, 0)) st2c(tag, wsr, lane == 0 ? WS_E : WS_END, 0);
#pragma unroll
    for (int i = 0; i < 16; ++i) { const u32 xv = sk[i >> 1][i & 1], dv = q_ds[i & 1]; sk[i >> 1][i & 1] = pk2(bf2f(xv & 0xffffu) * bf2f(dv & 0xffffu), bfhi(xv) * bfhi(dv)); PIN(sk[i >> 1][i & 1]); }
    TX(11);
    const u32 hb = WS_H + (u32)(tt * 8 + 2 * w) * 65536u + ((ct & 8) ? 32768 : 0);
    u32 fa_ = lane < 32 ? (4 * w + 2 * (lane & 1) + (sh ^ (lane & 1))) * 128 + (((lane >> 1) + rot) & 15) * 8 : WS_END, miss; PIN(fa_);
    u64 fh;
    auto fload = [&]() { asm volatile("buffer_load_dwordx2 %0, %1, %2, %3 offen sc1" : "=v"(fh) : "v"(fa_), "s"(wsr), "s"(WS_FH + rep) : "memory"); };
    auto fneed = [&](u32 bits) {
      if (__builtin_expect((miss & bits) != 0, 0)) {
        NOUNROLL for (u32 spn = 0; spn < SPIN_CAP; ++spn) {
          u64 a; asm volatile("buffer_load_dwordx2 %0, %1, %2, %3 offen sc1\n\ts_waitcnt vmcnt(0)" : "=&v"(a) : "v"(fa_), "s"(wsr), "s"(WS_FH + rep) : "memory");
          miss = (u32)__builtin_amdgcn_ballot_w64(a != tag);
          if (!(miss & bits)) break;
          __builtin_amdgcn_s_sleep(POLLSLP);
        }
      }
    };
    auto h_issue = [&](int u) {
      const u32 so = hb + ((u + rot) & 15) * 2048;
#pragma unroll
      for (int m = 0; m < 4; ++m) bldsc(wsr, vl, so + (m >> 1) * 65536 + (m & 1) * 1024, L + w * 4096, 98304 + (u & 3) * 16384 + m * 1024);
    };
    auto extras = [&]() {
      const u32 to = (u32)lr8 * (16 * 32768) + (u32)(2 * k1 + kd1) * 128 + lc8 * 16;
#pragma unroll
      for (int i = 0; i < 4; ++i) st4c(tq[i], wsr, to, WS_T + (8 * (i >> 1) + 2 * w + (i & 1)) * 32768);
#pragma unroll
      for (int e = 0; e < 4 * NE; ++e) c_piece(4 + (e >> 2), e & 3);
    };
    WAITV(0); BAR();
    TX(25);
    extras();
    fload();
    SB();
    {
      const float tk = (float)(2 * kap + 1) * (1.f / 512.f), tc = 2.f * cosr(4.f * tk);
#pragma unroll
      for (int i = 0; i < 8; ++i) {
        const float kk = (i & 4) ? -tk : tk;
        tab[i >> 2][i & 3] = cos8(fmaf(kk, 8.f * fg + (float)(i & 3), -0.125f), kk, tc); PIN(tab[i >> 2][i & 3]);
      }
    }
    TX(26);
    WAITV(0); PIN(fh); miss = (u32)__builtin_amdgcn_ballot_w64(fh != tag);
    fneed(FMASK); h_issue(0); h_issue(1); h_issue(2);
    SB();
    u32 ta[8];
    auto taf = [&]() {
#pragma unroll
    for (int i = 0; i < 8; ++i) {
      const int kb = 16 * i + 4 * w + g, k = (lane & 4) ? (kb ? 256 - kb : 128) : kb, rho = 64 * (k & 3) + (k >> 2);
      ta[i] = L + rho * 128 + 16 * ((((lane >> 1) & 4) | pq) ^ (((rho >> 1) & 1) | ((rho >> 3) & 1) << 1)); PIN(ta[i]);
    }
    };
    WAITV(8);
    TX(27);
    u64 ft;
    u32x4 tv[8], gv[2];
    f32x2 o[8][2];
    sfor<0, 16>([&](auto u_) {
      constexpr int u = decltype(u_)::v;
      if (u) WAITV(g2_wait(u));
      BAR();
      ITS(2, u);
      PINS(rot);
      if (u == FTU && w == 0) st2c(tag, wsr, fo, WS_FT);
      if (u == 3 || u == 6) { PIN(fh); miss &= (u32)__builtin_amdgcn_ballot_w64(fh != tag); }
      if constexpr (u + 3 < 16) fneed(3u << (2 * u + 6));
      if (u == 0 || u == 3) fload();
#pragma unroll
      for (int e = g2_c0(u); e < g2_c0(u + 1); ++e) c_piece(e >> 2, e & 3);
      if (u + 3 < 16) h_issue(u + 3);
      if constexpr (u >= TU && u < TU + 4) {
#pragma unroll
        for (int i = 2 * (u - TU); i < 2 * (u - TU) + 2; ++i) tv[i] = bl4c<0>(wsr, vl, WS_T + (s2 * 16 + ct) * 32768 + i * 4096 + w * 1024);
        if (u < TU + 2) gq[(u - TU) & 1] = bl1c(wsr, vGo + 128 * (u - TU), 0); else if (u == TU + 2) gv[0] = bl4c<0>(wsr, WS_G + tt * 512 + l15 * 16, 0); else gv[1] = bl4c<256>(wsr, WS_G + tt * 512 + (15 - l15) * 16, 0);
      }
      if (u == TU - 5) asm volatile("buffer_load_dwordx2 %0, %1, %2, %3 offen sc1" : "=v"(ft) : "v"(lane * 8), "s"(wsr), "s"(WS_FT + rep + w * 512) : "memory");
      if (u == TU - 1) { PIN(ft); if (__builtin_expect(!__all(ft == tag), 0)) pollq(wsr, WS_FT + rep + w * 512, lane, tag); }
      SB();
    });
    taf();
    WAITV(0);
#pragma unroll
    for (int i = 0; i < 8; ++i) { PIN(tv[i]); lw<u32x4>(ta[i], tv[i]); }
    LBAR();
    hart256<true>(o, L + (8 * g + q) * 128 + 16 * ((4 * (pq >> 1) + w) ^ (((q >> 1) & 1) | ((g & 1) << 1))) + 8 * (pq & 1), tab, kap, false);
    PIN(gq[0]); PIN(gq[1]); PIN(gv[0]); PIN(gv[1]);
#pragma unroll
    for (int i = 0; i < 8; ++i) {
      const float gg = __uint_as_float(gv[i >> 2][i < 4 ? i : (i + 3) & 3]), og = rbf(1.f - gg);
      const int pos = i < 4 ? l15 : 15 - l15;
#pragma unroll
      for (int hf = 0; hf < 2; ++hf)
        lw<f32x2>(L + 65536 + pos * 256 + 16 * ((8 * hf + 2 * w + (g & 1)) ^ pos) + 8 * (g >> 1) + 4096 * (i < 4 ? 2 * i : 2 * ((i + 3) & 3) + 1), fma2(f32x2{og, og}, f32x2{bf2f(sk[i][hf] & 0xffffu), bfhi(sk[i][hf])}, gg * o[i][hf]));
    }
    TX(30);
  } else {
    GETARGS();
    lw<u32>(L + L_SYNC + 4 * wv, 0u);
    const int r0 = 0; const u32 vA = 0, vP = (64 * pt + 32 * (w & 1) + (lane & 31)) * 2;
    ARGS();
    u32 q_dt = blh(mkr((const void*)ka[3], 2048), vP), q_lr = blh(mkr((const void*)ka[4], 2048), vP), q_li = blh(mkr((const void*)ka[5], 2048), vP);
    const int wn = w & 1, wm = w >> 1;
    const int hpos = 8 * wm + lc8, hsh = (tt ^ (tt >> 1)) & 1;
    u32 vH = (u32)(lr8 & 1) * (1u << 20) + hsh * (1u << 19) + (tt >> 1) * 65536 + (2 * pt + wn) * 1024 + g * 256 + hpos * 16;
    const u32 vAg = WS_AGG + (u32)((pt * 2 + wn) * 16 + l15) * 16;
    const int kap = 16 * sh + l15, np = (16 - l15) & 15; const bool sp = kap == 0;
    const int ecol = 8 * w + 4 * (g & 1) + 2 * (g >> 1);
    TS(19);
    u32x4 tab[2][4];
    hart_table(tab, kap, sp, g);
#pragma unroll
    for (int i = 0; i < 8; ++i) PIN(tab[i >> 2][i & 3]);
    u32 ua[6], hap = L + L_RA + (8 * g + q) * 128 + 16 * ((4 * (pq >> 1) + w) ^ (((q >> 1) & 1) | ((g & 1) << 1))) + 8 * (pq & 1);
#pragma unroll
    for (int i = 0; i < 2; ++i) {
      const int lo = i ? np : l15;
      ua[i] = L + L_V + lo * 256 + ((2 * ecol) ^ ((lo & 7) << 4)); PIN(ua[i]);
    }
    ua[2] = (ua[1] ^ 64) + (sp ? 0 : 48 * 256); ua[3] = (ua[1] ^ 64) + (sp ? 32 * 256 : 16 * 256); ua[4] = (ua[0] ^ 64) + (sp ? 16 * 256 : 0);
    PIN(ua[2]); PIN(ua[3]); PIN(ua[4]); PIN(hap);
    c2 lam, coef, l8, l16;
    WAITV(0); PIN(q_dt); PIN(q_lr); PIN(q_li);
    {
      const float lre = bf2f(q_lr), lim = bf2f(q_li);
      const float dt = rbf(expf(bf2f(q_dt))), mg = expf(dt * lre);
      float sn, cs; sincosf(dt * lim, &sn, &cs);
      const float nr = mg * cs - 1.f, ni = mg * sn;
      const bool big = fabsf(lre) >= fabsf(lim);
      const float u = big ? lre : lim, v = big ? lim : lre, t = v * __builtin_amdgcn_rcpf(u), rd = 2.f * __builtin_amdgcn_rcpf(u + v * t);
      lam = {spread16(mg * cs), spread16(ni)};
      coef = {spread16(rd * (big ? nr + ni * t : nr * t + ni)), spread16(rd * (big ? ni - nr * t : ni * t - nr))};
      l8 = csq(csq(csq(lam))); l16 = csq(l8);
      pinc(lam); pinc(coef); pinc(l8); pinc(l16);
    }
    TS(22);
    SB();
      u32 aa[2][2], ab[2][2];
#pragma unroll
      for (int i = 0; i < 4; ++i) {
        const int co = 16 * ((4 * (i & 1) + g) ^ (l15 >> 1)) + (i >> 1) * 65536;
        aa[i & 1][i >> 1] = L + (64 * wm + l15) * 128 + co; ab[i & 1][i >> 1] = L + 16384 + (64 * wn + l15) * 128 + co; PIN(aa[i & 1][i >> 1]); PIN(ab[i & 1][i >> 1]);
      }
    LBAR();
    TS(1);
    {
      f32x2 o[8][2];
      hart256<false>(o, hap, tab, kap, sp);
#pragma unroll
      for (int i = 0; i < 8; ++i) {
        const u32 a = i == 0 ? ua[0] : i == 1 ? ua[0] + 32 * 256 : i == 2 ? ua[2] : i == 3 ? ua[3] : i == 4 ? ua[4] : i == 5 ? ua[1] + 16 * 256 : i == 6 ? ua[1] + 48 * 256 : ua[4] + 32 * 256;
        lw<u32>(a, pk2(o[i][0])); lw<u32>(a + 128, pk2(o[i][1]));
      }
    }
    gpost(L + L_SYNC, w, 1);
    SB();
    c2 pw[8], wt[4], lsa, lsb, l32 = csq(l16), l64 = csq(l32);
    u32 wa[8], ra[8], pa[4], vAs = lane < 16 ? vAg : WS_END, sa = L + L_SEG + wn * 256 + l15 * 16;
#pragma unroll
    for (int n = 0; n < 4; ++n) { pa[n] = vAg + max(tt - 1 - g - 4 * n, 0) * 8192; PIN(pa[n]); }
    u32 sw = sa + (4 * wm + g) * 512;
    pinc(l32); pinc(l64); PIN(vAs); PIN(sa); PIN(sw); PIN(vH);
    auto pwf = [&]() {
      const c2 one = {{1.f, 1.f}, {0.f, 0.f}}, zero = {{0.f, 0.f}, {0.f, 0.f}};
      const c2 l128 = csq(l64), l256 = csq(l128), l512 = csq(l256);
      lsa = cmul(cmul(coef, csel(g & 1, l16, one)), cmul(csel(g & 2, l32, one), csel(wm, l64, one))); lsb = cmul(lsa, l8);
      wt[0] = cmul(csel(g & 1, l128, one), csel(g & 2, l256, one));
#pragma unroll
      for (int n = 1; n < 4; ++n) wt[n] = cmul(wt[n - 1], l512);
#pragma unroll
      for (int n = 0; n < 4; ++n) { wt[n] = csel(tt - 1 - g - 4 * n >= 0, wt[n], zero); pinc(wt[n]); }
      pinc(lsa); pinc(lsb);
    };
    auto pwg = [&]() {
      pw[0] = lam; pw[1] = csq(lam); pw[2] = cmul(pw[1], lam); pw[3] = csq(pw[1]); pw[4] = cmul(pw[3], lam); pw[5] = csq(pw[2]); pw[6] = cmul(pw[3], pw[2]); pw[7] = l8;
#pragma unroll
      for (int s = 1; s < 7; ++s) pinc(pw[s]);
    };
    u64 tag;
    f32x4 acc[4][4];
    {
      LBAR();
      TS(2);
      u64 cy0 = 0; CY0();
      {
        bf16x8 A[2][2][4], B[2][2][4];
        auto rd = [&](int j, int i) { const int sl = (j + 3) & 3, ks = i & 1, f = (i >> 1) & 3; const u32 bs = (sl & 1) * 32768 + f * 2048;
          if (i < 8) B[j & 1][ks][f] = lr<bf16x8>(ab[ks][sl >> 1] + bs); else A[j & 1][ks][f] = lr<bf16x8>(aa[ks][sl >> 1] + bs); };
#pragma unroll
        for (int i = 0; i < 16; ++i) rd(0, i);
#pragma unroll
        for (int j = 0; j < 16; ++j) {
#pragma unroll
          for (int qq = 0; qq < 32; ++qq) {
            const int p = qq >> 1, m = p >> 2, n = (m & 1) ? 3 - (p & 3) : (p & 3), ks = (qq ^ p) & 1;
            if (qq == BQ && j < 17 - LEAD) LBAR();
            SB();
            if (j || (qq & 1)) mmc(acc[m][n], A[j & 1][ks][m], B[j & 1][ks][n]); else mmz(acc[m][n], A[j & 1][ks][m], B[j & 1][ks][n]);
            SB();
            if (j < 15 && qq >= BQ && qq < BQ + 16) rd(j + 1, qq - BQ);
          }
        }
      }
      NOPS();
      CY1(20);
    }
    TS(3);
    c2 h[2][8];
#pragma unroll
    for (int s = 0; s < 8; ++s) {
#pragma unroll
      for (int ps = 0; ps < 2; ++ps) {
        const f32x4 vr = acc[s >> 1][2 * ps], vi = acc[s >> 1][2 * ps + 1];
        h[ps][s] = (s & 1) ? c2{vr.zw, vi.zw} : c2{vr.xy, vi.xy};
        if (s) { const c2 a = bc(lam, ps); h[ps][s].r = fma2(-a.i, h[ps][s - 1].i, h[ps][s].r); h[ps][s].i = fma2(a.i, h[ps][s - 1].r, h[ps][s].i); }
      }
      SB();
#pragma unroll
      for (int ps = 0; ps < 2; ++ps) if (s) { const c2 a = bc(lam, ps); h[ps][s].r = fma2(a.r, h[ps][s - 1].r, h[ps][s].r); h[ps][s].i = fma2(a.r, h[ps][s - 1].i, h[ps][s].i); }
      SB();
    }
    const c2 ea = {{h[0][7].r.x, h[1][7].r.x}, {h[0][7].i.x, h[1][7].i.x}};
    { const c2 e = cfma(l8, ea, c2{{h[0][7].r.y, h[1][7].r.y}, {h[0][7].i.y, h[1][7].i.y}});
      lw<f32x4>(sw, f32x4{e.r.x, e.r.y, e.i.x, e.i.y}); }
    gbar(L + L_SYNC, w, 2);
    u32x4 gx[4], gw[2];
    c2 cin;
    {
      c2 e[8];
#pragma unroll
      for (int j = 0; j < 8; ++j) e[j] = asc(lr<f32x4>(sa + j * 512));
      SB();
      const c2 e01 = cfma(l16, e[0], e[1]), e23 = cfma(l16, e[2], e[3]), e45 = cfma(l16, e[4], e[5]), e67 = cfma(l16, e[6], e[7]);
      const c2 e03 = cfma(l32, e01, e23), e47 = cfma(l32, e45, e67), tot = cfma(l64, e03, e47);
      st4c(__builtin_bit_cast(u32x4, f32x4{tot.r.x, tot.r.y, tot.i.x, tot.i.y}), wsr, vAs, wm ? tt * 8192 : WS_END);
      SB();
      const c2 zero = {{0.f, 0.f}, {0.f, 0.f}};
      const c2 c0 = csel(wm, e03, zero), c2_ = cfma(l32, c0, csel(wm, e45, e01)), cb = csel(g & 2, c2_, c0);
      cin = csel(g & 1, cfma(l16, cb, csel(wm, csel(g & 2, e[6], e[4]), csel(g & 2, e[2], e[0]))), cb);
    }
    pinc(cin);
    TS(4);
    SB();
    auto gload = [&]()
      { const u32 xo = (bid * 8 + 2 * w) * 2048, vl = lane * 16; const rsrc_t gr = mkr((const void*)kb_[3], 2048);
        gx[0] = bl4<0>(xr, vl, xo); gx[1] = bl4<1024>(xr, vl, xo); gx[2] = bl4<2048>(xr, vl, xo); gx[3] = bl4<3072>(xr, vl, xo); gw[0] = bl4<0>(gr, vl, 0); gw[1] = bl4<1024>(gr, vl, 0); };
    gload();
    SB();
    c2 ca, cz; u32x4 v[4];
    auto poll = [&]() {
#pragma unroll
      for (int n = 0; n < 4; ++n) v[n] = bl4c<0>(wsr, pa[n], 0);
    };
    {
#pragma unroll
      for (int s = 0; s < 8; ++s) {
        f32x2 t[4];
#pragma unroll
        for (int ps = 0; ps < 2; ++ps) { const c2 a = bc(coef, ps); t[2 * ps] = a.r * h[ps][s].r; t[2 * ps + 1] = a.i * h[ps][s].r; }
        SB();
#pragma unroll
        for (int ps = 0; ps < 2; ++ps) { const c2 a = bc(coef, ps); h[ps][s].r = fma2(-a.i, h[ps][s].i, t[2 * ps]); h[ps][s].i = fma2(-a.r, h[ps][s].i, -t[2 * ps + 1]); }
        SB();
      }
#pragma unroll
      for (int s = 0; s < 16; ++s) pinc(h[s & 1][s >> 1]);
      SB();
      poll();
      SB();
      ca = cmul(coef, cin); cz = cmul(coef, cfma(l8, cin, ea));
      pinc(ca); pinc(cz);
      const u32 hs = L + 65536 + w * 8192;
#pragma unroll
      for (int i = 0; i < 8; ++i) {
        wa[i] = hs + l15 * 128 + ((4 * g + 16 * i) ^ (8 * (l15 & 1) + 4 * (l15 & 4) + 16 * (l15 & 2) + 8 * (l15 & 8))); PIN(wa[i]);
        ra[i] = hs + (8 * g + q) * 128 + ((8 * pq) ^ (8 * (q & 1) + 16 * (q & 2) + 64 * (g & 1)) ^ (16 * i)); PIN(ra[i]);
      }
    }
    SB();
    pwf();
    SB();
    pwg();
    SB();
    auto gate = [&]()
    {
      const u32 vG = WS_G + (u32)(((2 * w + (lane & 1)) * 2 + (((bid >> 4) ^ (bid >> 5)) & 1)) * 128 + ((bid >> 5) & 1) * 64 + (bid & 15) * 4 + (bid >> 6)) * 4;
      float sm[2] = {0.f, 0.f};
      PIN(gw[0]); PIN(gw[1]);
#pragma unroll
      for (int i = 0; i < 4; ++i) {
        PIN(gx[i]);
#pragma unroll
        for (int e = 0; e < 4; ++e) { const u32 a = gx[i][e], b = gw[i & 1][e]; sm[i >> 1] = __builtin_amdgcn_fdot2_f32_bf16(__builtin_bit_cast(bf16x2, a), __builtin_bit_cast(bf16x2, b), sm[i >> 1], false); }
      }
      sm[0] = dppadd<0xB1>(sm[0]); sm[1] = dppadd<0xB1>(sm[1]);
      float t = (lane & 1) ? sm[1] : sm[0];
      t = dppadd<0x4E>(t); t = dppadd<0x124>(t); t = dppadd<0x128>(t); t = sum32(sum16(t));
      __builtin_amdgcn_raw_buffer_store_b32(__float_as_uint(rbf(1.f / (1.f + expf(-rbf(t))))), wsr, lane < 2 ? vG : WS_END, 0, 16);
    };
    WAITV(4); gate();
    SB();
    TS(5);
    WAITV(1);
    if (tt > 0) {
      auto vok = [&]() { u32 mx = 0;
#pragma unroll
        for (int n = 0; n < 4; ++n) { PIN(v[n]); mx = umax3(umax3(mx, v[n].x, v[n].y), v[n].z, v[n].w); }
        return __all(mx != SENT); };
      if (!vok()) {
        NOUNROLL for (u32 spn = 0; spn < SPIN_CAP; ++spn) {
#pragma unroll
          for (int n = 0; n < 4; ++n) v[n] = bl4c<0>(wsr, pa[n], 0);
          WAITV(0);
          if (vok()) break;
        }
      }
      c2 cb = cfma(wt[0], asc(__builtin_bit_cast(f32x4, v[0])), cmul(wt[1], asc(__builtin_bit_cast(f32x4, v[1]))));
      const c2 cd = cfma(wt[2], asc(__builtin_bit_cast(f32x4, v[2])), cmul(wt[3], asc(__builtin_bit_cast(f32x4, v[3]))));
      cb.r += cd.r; cb.i += cd.i;
      cb = c2{{sum32(sum16(cb.r.x)), sum32(sum16(cb.r.y))}, {sum32(sum16(cb.i.x)), sum32(sum16(cb.i.y))}};
      ca = cfma(lsa, cb, ca); cz = cfma(lsb, cb, cz);
    } else {
#pragma unroll
      for (int n = 0; n < 4; ++n) PIN(v[n]);
    }
    TS(6);
    {
      const c2 cc[2] = {{{ca.r.x, cz.r.x}, {ca.i.x, cz.i.x}}, {{ca.r.y, cz.r.y}, {ca.i.y, cz.i.y}}};
      u32x4 hq[8];
#pragma unroll
      for (int s = 0; s < 8; ++s) {
#pragma unroll
        for (int ps = 0; ps < 2; ++ps) { const c2 a = bc(pw[s], ps); h[ps][s].r = fma2(-a.i, cc[ps].i, h[ps][s].r); h[ps][s].i = fma2(-a.i, cc[ps].r, h[ps][s].i); }
        SB();
#pragma unroll
        for (int ps = 0; ps < 2; ++ps) { const c2 a = bc(pw[s], ps); h[ps][s].r = fma2(a.r, cc[ps].r, h[ps][s].r); h[ps][s].i = fma2(-a.r, cc[ps].i, h[ps][s].i); }
        SB();
#pragma unroll
        for (int ps = 0; ps < 2; ++ps) { lw<u32>(wa[s] + ps * 2048, pk2(h[ps][s].r)); lw<u32>(wa[s] + ps * 2048 + 4096, pk2(h[ps][s].i)); }
        FENCE();
        if (s & 1) {
          hq[s - 1] = __builtin_bit_cast(u32x4, cat(trr(ra[s - 1]), trr(ra[s] + 512))); hq[s] = __builtin_bit_cast(u32x4, cat(trr(ra[s - 1] + 4096), trr(ra[s] + 4608)));
          if (s > 1) { st4c(hq[s - 3], wsr, vH, WS_H + ((s - 3) >> 1) * (2u << 20)); st4c(hq[s - 2], wsr, vH, WS_H + ((s - 3) >> 1) * (2u << 20) + 32768); }
        }
        SB();
      }
      st4c(hq[6], wsr, vH, WS_H + 3 * (2u << 20)); st4c(hq[7], wsr, vH, WS_H + 3 * (2u << 20) + 32768);
    }
    TS(7);
    {
      bf16x8 cp[6][4];
      u32 ha = L + 98304 + wm * 8192 + lane * 16, ca[2][2];
#pragma unroll
      for (int i = 0; i < 4; ++i) { ca[i & 1][i >> 1] = L + (i >> 1) * 65536 + wn * 8192 + l15 * 128 + 16 * ((4 * (i & 1) + g) ^ (l15 >> 1)); PIN(ca[i & 1][i >> 1]); }
      PIN(ha);
      tag = lr<u64>(L + L_SYNC + 32); PIN(tag);
      WAITV(0);
      gbar(L + L_SYNC, w, 3);
      if (w == 0) st2c(tag, wsr, lane < NREP ? lane * 2048 : WS_END, WS_FH + (tt * 16 + pt) * 8);
      { u64 pc; u32 bk, nb; asm volatile("s_getpc_b64 %0\n.Lcode_m%=:\n\ts_mov_b32 %1, .Lcode_m%=-.Lcode_lo\n\ts_mov_b32 %2, .Lcode_hi-.Lcode_lo" : "=s"(pc), "=s"(bk), "=s"(nb));
        if (w == 1) touch(mkr((const void*)((pc - bk) & ~127ull), min(nb, 61440u)), (lane * 32 + jj) * 128, L); }
      TS(28);
      BAR();
#pragma unroll
      for (int i = 0; i < 24; ++i) cp[i >> 2][i & 3] = lr<bf16x8>(ca[(i >> 2) & 1][0] + (i >> 3) * 16384 + (i & 3) * 2048);
      WAITV(0); LBAR();
#pragma unroll
      for (int i = 0; i < 24; ++i) PIN(cp[i >> 2][i & 3]);
      TS(12);
      u64 cy0 = 0; CY0();
      {
        bf16x8 H[2][2][4], Cf[2][2][4];
        auto rd = [&](int u, int i) { const int ks = i & 1, f = (i >> 1) & 3;
          if (i < 8) { if (u >= 3) Cf[u & 1][ks][f] = lr<bf16x8>(ca[ks][(u % 6) >> 2] + ((u % 6) & 3) * 16384 + f * 2048); } else H[u & 1][ks][f] = lr<bf16x8>(ha + (u & 3) * 16384 + f * 2048 + ks * 1024); };
#pragma unroll
        for (int i = 8; i < 16; ++i) rd(0, i);
#pragma unroll
        for (int u = 0; u < 16 - SE; ++u) {
#pragma unroll
          for (int qq = 0; qq < 32; ++qq) {
            const int p = qq >> 1, m = p >> 2, n = (m & 1) ? 3 - (p & 3) : (p & 3), ks = (qq ^ p) & 1;
            if (qq == BQ && u < 15) LBAR();
            SB();
            const bf16x8 cf = u < 3 ? cp[(2 * u + ks) % 6][n] : Cf[u & 1][ks][n];
            if (u || (qq & 1)) mmc(acc[m][n], cf, H[u & 1][ks][m]); else mmz(acc[m][n], cf, H[u & 1][ks][m]);
            SB();
            if (u < 15 && qq >= BQ && qq < BQ + 16) rd(u + 1, qq - BQ);
          }
        }
        u32 xs = g * 256 + (l15 ^ (4 * g)) * 16; PIN(xs);
#pragma unroll
        for (int qq = 0; qq < 32 * SE; ++qq) {
          const int u = 16 - SE + ((qq >> 4) % SE), n0 = qq < 16 * SE ? 2 : 0, p = (qq & 15) >> 1, m = p >> 1, n = n0 + ((p ^ m) & 1), ks = (qq ^ p) & 1;
          if (SE == 2 && qq == 0) LBAR();
          SB();
          mmc(acc[m][n], Cf[u & 1][ks][n], H[u & 1][ks][m]);
          SB();
          if (SE == 2 && qq < 16) rd(15, qq < 4 ? 4 + qq : qq < 12 ? 4 + qq : qq - 12);
          if (qq >= 16 * SE + 2 && qq < 16 * SE + 14 && (qq - 16 * SE - 2) % 3 == 0) { const int mm = (qq - 16 * SE - 2) / 3; st4c(__builtin_bit_cast(u32x4, pk8(acc[mm][2], acc[mm][3])), wsr, xs, WS_X + (((bid ^ 1) * 4 + w) * 4 + mm) * 1024); }
        }
      }
      NOPS();
      CY1(21);
    }
    TS(13);
    {
      const u32 vl = lane * 16;
#pragma unroll
      for (int m = 0; m < 4; ++m) if (!SE) st4c(__builtin_bit_cast(u32x4, pk8(acc[m][2], acc[m][3])), wsr, g * 256 + (l15 ^ (4 * g)) * 16, WS_X + (((bid ^ 1) * 4 + w) * 4 + m) * 1024);
      gq[0] = bl1c(wsr, vGo, 0); gq[1] = bl1c(wsr, vGo + 128, 0);
      BAR();
      WAITV(2);
      st2c(tag, wsr, lane == 0 ? 0 : WS_END, WS_FP + ((bid ^ 1) * 4 + w) * 128);
      TS(29);
      const u32 pa = L + 98304 + wm * 16384 + wn * 2048 + g * 256 + (l15 ^ (4 * g)) * 16;
#pragma unroll
      for (int i = 0; i < 8; ++i) lw<f32x4>(pa + (i >> 1) * 4096 + (i & 1) * 1024, acc[i >> 1][i & 1]);
      NOUNROLL for (u32 spn = 0; spn < SPIN_CAP; ++spn) {
        u64 a; asm volatile("buffer_load_dwordx2 %0, off, %1, %2 sc1\n\ts_waitcnt vmcnt(0)" : "=&v"(a) : "s"(wsr), "s"(WS_FP + (bid * 4 + w) * 128) : "memory");
        if (__all(a == tag)) break;
      }
#pragma unroll
      for (int m = 0; m < 4; ++m) bldsc(wsr, vl, WS_X + (bid * 4 + w) * 4096 + m * 1024, L + w * 4096, 131072 + m * 1024);
      PIN(gq[0]); PIN(gq[1]);
      TS(31);
    }
  }
  if (!rh) TS(14);
  {
    const rsrc_t outr = mkr((const void*)outp, 8u << 20);
    u32 pa[2], xa[2], ba[4], oa[2]; float og[2];
#pragma unroll
    for (int hi = 0; hi < 2; ++hi) {
      const int row = lr8 + 8 * hi, s1 = 32 * wv + row + 16 * (sh ^ (w & 1));
      pa[hi] = L + 98304 + wv * 4096 + lc8 * 256 + (row ^ (4 * pq)) * 16; oa[hi] = (u32)(s2 + 8 * s1) * 4096u + lc8 * 16; og[hi] = rbf(1.f - __uint_as_float(gq[hi]));
      ba[2 * hi] = L + 65536 + (wv * 16 + row) * 256 + 16 * (lc8 ^ row); ba[2 * hi + 1] = ba[2 * hi] ^ 128;
      xa[hi] = L + 131072 + rh * 8192 + w * 1024 + pq * 256 + (row ^ (4 * pq)) * 16 + (lc8 >> 2) * 8; PIN(xa[hi]);
      PIN(pa[hi]); PIN(oa[hi]); PIN(og[hi]); PIN(ba[2 * hi]); PIN(ba[2 * hi + 1]);
    }
    WAITV(0); LBAR();
    f32x4 v[4][2]; u32x2 xv[4];
#pragma unroll
    for (int i = 0; i < 4; ++i) {
      v[i][0] = lr<f32x4>(pa[i >> 1] + (i & 1) * 2048); v[i][1] = lr<f32x4>(ba[i]); xv[i] = lr<u32x2>(xa[i >> 1] + (i & 1) * 4096);
    }
    SB();
#pragma unroll
    for (int i = 0; i < 4; ++i)
      st4(__builtin_bit_cast(u32x4, og[i >> 1] * (v[i][0] + f32x4{bf2f(xv[i].x & 0xffffu), bfhi(xv[i].x), bf2f(xv[i].y & 0xffffu), bfhi(xv[i].y)}) + v[i][1]), outr, oa[i >> 1], 128 * ct + 2048 * (i & 1));
  }
  if (!rh) TS(15);
}

extern "C" void kernel_launch(void* const* d_in, const int* in_sizes, int n_in, void* d_out, int out_size, void* d_ws, size_t ws_size, hipStream_t stream) {
  (void)in_sizes; (void)n_in; (void)out_size; (void)ws_size;
  (void)hipFuncSetAttribute((const void*)k_all, hipFuncAttributeMaxDynamicSharedMemorySize, L_TOT);
  const u16* const* in = (const u16* const*)d_in;
  k_all<<<256, 512, L_TOT, stream>>>(in[0], in[6], in[7], in[10], in[4], in[5], in[1], in[2], in[8], in[9], in[11], in[12], (char*)d_ws, (float*)d_out);
}
```

```cpp
#include <hip/hip_runtime.h>
#include <hip/hip_bf16.h>
#include <stdint.h>

typedef unsigned short u16;
typedef unsigned int u32;
typedef unsigned long long u64;
typedef __attribute__((ext_vector_type(4))) short s16x4;
typedef __attribute__((ext_vector_type(8))) __bf16 bf16x8;
typedef __attribute__((ext_vector_type(2))) __bf16 bf16x2;
typedef __attribute__((ext_vector_type(2))) float f32x2;
typedef __attribute__((ext_vector_type(4))) float f32x4;
typedef __attribute__((ext_vector_type(4))) u32 u32x4;
typedef __attribute__((ext_vector_type(2))) u32 u32x2;
#define DI __device__ __forceinline__
#define LDSP __attribute__((address_space(3)))
#define RLX __ATOMIC_RELAXED, __HIP_MEMORY_SCOPE_AGENT

#define NREP 8
constexpr u32 WS_E = 0, WS_AGG = 65536, WS_FP = 0x40000, WS_FU = 0x60000, WS_FH = WS_FU + NREP * 2048, WS_FT = WS_FH + NREP * 2048, WS_G = WS_FT + NREP * 2048;
constexpr u32 WS_U = 1u << 20, WS_T = 5u << 20, WS_H = 9u << 20, WS_X = 17u << 20, WS_END = 21u << 20;
constexpr u32 L_RA = 65536, L_W = 98304, L_WS = 17408, L_V = 133120, L_CH = 5264, L_PS = 144, L_SYNC = 154176, L_SEG = 154688, L_TOT = 163840;
constexpr u32 SPIN_CAP = 1u << 18, SENT = 0xffffffffu;

DI float bf2f(u32 h) { return __builtin_bit_cast(float, h << 16); }
DI float bfhi(u32 h) { return __builtin_bit_cast(float, h & 0xffff0000u); }
DI u32 pk2(float a, float b) { f32x2 v = {a, b}; return __builtin_bit_cast(u32, __builtin_convertvector(v, bf16x2)); }
DI u32 pk2(f32x2 v) { return __builtin_bit_cast(u32, __builtin_convertvector(v, bf16x2)); }
DI float rbf(float a) { return bf2f(pk2(a, a) & 0xffffu); }
typedef __amdgpu_buffer_rsrc_t rsrc_t;
DI rsrc_t mkr(const void* p, u32 bytes) { return __builtin_amdgcn_make_buffer_rsrc((void*)p, 0, bytes, 0x00020000); }
DI void bldsc(rsrc_t r, u32 vo, u32 so, u32 l, u32 lo) { asm volatile("s_add_u32 m0, %3, %4\n\ts_nop 0\n\tbuffer_load_dwordx4 %0, %1, %2 offen sc1 lds" :: "v"(vo), "s"(r), "s"(so), "s"(l), "i"(lo) : "memory", "scc"); }
DI void blds(rsrc_t r, u32 vo, u32 so, u32 l, u32 lo) { asm volatile("s_add_u32 m0, %3, %4\n\ts_nop 0\n\tbuffer_load_dwordx4 %0, %1, %2 offen lds" :: "v"(vo), "s"(r), "s"(so), "s"(l), "i"(lo) : "memory", "scc"); }
template <class T> DI T lr(u32 a) { return *(const LDSP T*)(uintptr_t)a; }
template <class T> DI void lw(u32 a, T v) { *(LDSP T*)(uintptr_t)a = v; }
DI u32x2 trr(u32 a) { return __builtin_bit_cast(u32x2, __builtin_amdgcn_ds_read_tr16_b64_v4i16((LDSP s16x4*)(uintptr_t)a)); }
DI bf16x8 cat(u32x2 a, u32x2 b) { u32x4 v = {a.x, a.y, b.x, b.y}; return __builtin_bit_cast(bf16x8, v); }
DI bf16x8 pk8(f32x4 a, f32x4 b) { u32x4 v = {pk2(a[0], a[1]), pk2(a[2], a[3]), pk2(b[0], b[1]), pk2(b[2], b[3])}; return __builtin_bit_cast(bf16x8, v); }
DI f32x4 mma(bf16x8 a, bf16x8 b, f32x4 c) { return __builtin_amdgcn_mfma_f32_16x16x32_bf16(a, b, c, 0, 0, 0); }
DI void mmz(f32x4& c, bf16x8 a, bf16x8 b) { asm volatile("v_mfma_f32_16x16x32_bf16 %0, %1, %2, 0" : "=&v"(c) : "v"(a), "v"(b)); }
DI void mmc(f32x4& c, bf16x8 a, bf16x8 b) { asm volatile("v_mfma_f32_16x16x32_bf16 %0, %1, %2, %0" : "+v"(c) : "v"(a), "v"(b)); }
DI bf16x8 asf(u32x4 v) { return __builtin_bit_cast(bf16x8, v); }
DI float cosr(float rev) { return __builtin_amdgcn_cosf(rev); }
DI float sinr(float rev) { return __builtin_amdgcn_sinf(rev); }
template <int O> DI u32x4 bl4(rsrc_t r, u32 vo, u32 so) { u32x4 v; asm volatile("buffer_load_dwordx4 %0, %1, %2, %3 offen offset:%4" : "=v"(v) : "v"(vo), "s"(r), "s"(so), "n"(O) : "memory"); return v; }
template <int O> DI u32x4 bl4c(rsrc_t r, u32 vo, u32 so) { u32x4 v; asm volatile("buffer_load_dwordx4 %0, %1, %2, %3 offen offset:%4 sc1" : "=v"(v) : "v"(vo), "s"(r), "s"(so), "n"(O) : "memory"); return v; }
DI u32 bl1c(rsrc_t r, u32 vo, u32 so) { u32 v; asm volatile("buffer_load_dword %0, %1, %2, %3 offen sc1" : "=v"(v) : "v"(vo), "s"(r), "s"(so) : "memory"); return v; }
DI void touch(rsrc_t r, u32 vo, u32 l) { asm volatile("s_add_u32 m0, %2, %3\n\ts_nop 0\n\tbuffer_load_dword %0, %1, 0 offen sc1 lds" :: "v"(vo), "s"(r), "s"(l), "i"(L_SYNC + 256) : "memory", "scc"); }
DI u64 bl2c(rsrc_t r) { u64 v; asm volatile("buffer_load_dwordx2 %0, off, %1, 0 sc1" : "=v"(v) : "s"(r) : "memory"); return v; }
DI u32 bl1(rsrc_t r, u32 vo, u32 so) { u32 v; asm volatile("buffer_load_dword %0, %1, %2, %3 offen" : "=v"(v) : "v"(vo), "s"(r), "s"(so) : "memory"); return v; }
DI void blhl(rsrc_t r, u32 vo, u32 l, u32 lo) { asm volatile("s_add_u32 m0, %2, %3\n\ts_nop 0\n\tbuffer_load_ushort %0, %1, 0 offen lds" :: "v"(vo), "s"(r), "s"(l), "i"(lo) : "memory", "scc"); }
DI void st4c(u32x4 v, rsrc_t r, u32 vo, u32 so) { asm volatile("buffer_store_dwordx4 %0, %1, %2, %3 offen sc1\n\ts_nop 1" :: "v"(v), "v"(vo), "s"(r), "s"(so) : "memory"); }
DI void st2c(u64 v, rsrc_t r, u32 vo, u32 so) { asm volatile("buffer_store_dwordx2 %0, %1, %2, %3 offen sc1" :: "v"(v), "v"(vo), "s"(r), "s"(so) : "memory"); }
DI void st4(u32x4 v, rsrc_t r, u32 vo, u32 so) { asm volatile("buffer_store_dwordx4 %0, %1, %2, %3 offen nt\n\ts_nop 1" :: "v"(v), "v"(vo), "s"(r), "s"(so) : "memory"); }
#define POLLSLP 2
#define NOUNROLL _Pragma("unroll 1")
#define SB() __builtin_amdgcn_sched_barrier(0)
template <class T> DI void PINS(T& v) { asm volatile("" : "+s"(v)); }
template <class T> DI void PIN(T& v) { asm volatile("" : "+v"(v)); }
#define NOPS() do { SB(); asm volatile("s_nop 7\n\ts_nop 1"); SB(); } while (0)
#define WAITV(n) asm volatile("s_waitcnt vmcnt(%0)" :: "n"(n) : "memory")
#define WAITL(n) asm volatile("s_waitcnt lgkmcnt(%0)" :: "n"(n) : "memory")
#define FENCE() asm volatile("" ::: "memory")
#define BAR() do { FENCE(); __builtin_amdgcn_s_barrier(); FENCE(); } while (0)
#define LBAR() do { WAITL(0); __builtin_amdgcn_s_barrier(); FENCE(); } while (0)
DI u32 umax3(u32 a, u32 b, u32 c) { return max(max(a, b), c); }
DI void gwait(u32 words, u32 phase) {
  NOUNROLL for (u32 s = 0; s < 16 * SPIN_CAP; ++s) {
    u32x4 v; asm volatile("ds_read_b128 %0, %1\n\ts_waitcnt lgkmcnt(0)" : "=v"(v) : "v"(words) : "memory");
    if (__builtin_amdgcn_readfirstlane(min(min(v.x, v.y), min(v.z, v.w))) >= phase) break;
  }
  FENCE();
}
DI void gpost(u32 words, int wq, u32 phase) { FENCE(); lw<u32>(words + 4 * wq, phase); FENCE(); }
DI void gbar(u32 words, int wq, u32 phase) { gpost(words, wq, phase); gwait(words, phase); }
DI void pollq(rsrc_t wr, u32 off, int lane, u64 tag) {
  NOUNROLL for (u32 s = 0; s < SPIN_CAP; ++s) {
    u64 a; asm volatile("buffer_load_dwordx2 %0, %1, %2, %3 offen sc1\n\ts_waitcnt vmcnt(0)" : "=&v"(a) : "v"(lane * 8), "s"(wr), "s"(off) : "memory");
    if (__all(a == tag)) break;
    __builtin_amdgcn_s_sleep(POLLSLP);
  }
}
#define TSW(n, t) do {} while (0)
#define CY0()
#define CY1(n)
#define ITS(p, j) do {} while (0)
#define TS(n) TSW(n, 0)
#define TX(n) TSW(n, 256)
#define NC0 1
#define NW13 4
#define FMASK 63u
#define FTU 0
#define JF 3
struct c2 { f32x2 r, i; };
DI f32x2 fma2(f32x2 a, f32x2 b, f32x2 c) { return __builtin_elementwise_fma(a, b, c); }
DI c2 cfma(c2 a, c2 b, c2 c) { return {fma2(a.r, b.r, fma2(-a.i, b.i, c.r)), fma2(a.r, b.i, fma2(a.i, b.r, c.i))}; }
DI c2 cmul(c2 a, c2 b) { return {fma2(-a.i, b.i, a.r * b.r), fma2(a.r, b.i, a.i * b.r)}; }
DI c2 csq(c2 a) { return {fma2(a.r, a.r, -a.i * a.i), (a.r + a.r) * a.i}; }
DI c2 bc(c2 a, int ps) { return ps ? c2{a.r.yy, a.i.yy} : c2{a.r.xx, a.i.xx}; }
DI c2 csel(bool c, c2 a, c2 b) { return {c ? a.r : b.r, c ? a.i : b.i}; }
DI c2 asc(f32x4 v) { return {v.xy, v.zw}; }
DI void pinc(c2& a) { PIN(a.r); PIN(a.i); }
DI f32x2 spread16(float v) { const u32 u = __float_as_uint(v); const auto e = __builtin_amdgcn_permlane16_swap(u, u, false, false); return f32x2{__uint_as_float(e[0]), __uint_as_float(e[1])}; }
DI float sum16(float v) { const f32x2 e = spread16(v); return e.x + e.y; }
DI float sum32(float v) { const u32 u = __float_as_uint(v); const auto e = __builtin_amdgcn_permlane32_swap(u, u, false, false); return __uint_as_float(e[0]) + __uint_as_float(e[1]); }
template <int CTRL> DI float dppadd(float v) { return v + __int_as_float(__builtin_amdgcn_mov_dpp(__float_as_int(v), CTRL, 0xf, 0xf, true)); }

DI u32x4 cos8(float a0, float kk, float c2d) {
  float c[8]; c[0] = cosr(a0); c[1] = cosr(fmaf(kk, 4.f, a0));
#pragma unroll
  for (int j = 2; j < 8; ++j) c[j] = fmaf(c2d, c[j - 1], -c[j - 2]);
  return u32x4{pk2(c[0], c[1]), pk2(c[2], c[3]), pk2(c[4], c[5]), pk2(c[6], c[7])};
}
DI void hart_table(u32x4 (&tab)[2][4], int kap, bool sp, int g) {
  const float kp = (float)kap * (1.f / 256.f), km = sp ? 0.125f : kp, cp = 2.f * cosr(4.f * kp), cm = sp ? -2.f : cp;
#pragma unroll
  for (int r = 0; r < 4; ++r) {
    const float kk = r == 1 && sp ? km : -km, fr_ = (float)(32 * g + r);
    tab[0][r] = cos8(fmaf(kp, fr_, -0.125f), kp, cp); tab[1][r] = cos8(fmaf(kk, fr_, -0.125f), kk, cm);
  }
}
template <bool INV> DI void hart256(f32x2 (&o)[8][2], u32 ap, const u32x4 (&tab)[2][4], int kap, bool sp) {
  const float sg1 = (kap & 1) ? -1.f : 1.f, al1 = sp ? 0.f : 1.f;
  const f32x2 sgn = {sg1, sg1}, al = {al1, al1};
  bf16x8 a[4][2]; f32x4 m[2][4][2];
#pragma unroll
  for (int r = 0; r < 4; ++r) { a[r][0] = cat(trr(ap + r * 8192), trr(ap + r * 8192 + 512)); a[r][1] = cat(trr(ap + r * 8192 + 4096), trr(ap + r * 8192 + 4608)); }
  SB();
#pragma unroll
  for (int r = 0; r < 4; ++r)
#pragma unroll
    for (int i = 0; i < 4; ++i) m[i >> 1][r][i & 1] = mma(a[r][i & 1], asf(tab[(i >> 1) ^ (INV & i & 1)][r]), f32x4{});
  SB();
#pragma unroll
  for (int hh = 0; hh < 2; ++hh) {
    f32x2 p[2][4];
#pragma unroll
    for (int i = 0; i < 8; ++i) { const f32x2 sg_ = INV && i >= 4 ? -sgn : sgn; p[i >> 2][i & 3] = hh ? fma2(sg_, m[i >> 2][i & 3][1].zw, m[i >> 2][i & 3][0].zw) : fma2(sg_, m[i >> 2][i & 3][1].xy, m[i >> 2][i & 3][0].xy); }
    const f32x2 s0 = p[0][1] + p[0][3], d0 = p[0][1] - p[0][3], s1 = INV ? p[1][1] + p[1][3] : fma2(al, p[1][1], p[1][3]), d1 = INV ? p[1][1] - p[1][3] : fma2(-al, p[1][3], p[1][1]);
    const f32x2 x0 = sp ? d0 : d1, x1 = sp ? d1 : d0;
#pragma unroll
    for (int sg = 0; sg < 2; ++sg) {
      const f32x2 A = p[sg][0] + p[sg][2], B = p[sg][0] - p[sg][2], S = sg ? s1 : s0, X = sg ? x1 : x0;
      o[4 * sg][hh] = A + S; o[4 * sg + 2][hh] = A - S; o[4 * sg + 1][hh] = B + X; o[4 * sg + 3][hh] = B - X;
    }
  }
  SB();
#pragma unroll
  for (int i = 0; i < 8; ++i) {
    const auto e0 = __builtin_amdgcn_permlane32_swap(__float_as_uint(o[i][0].x), __float_as_uint(o[i][1].x), false, false);
    const auto e1 = __builtin_amdgcn_permlane32_swap(__float_as_uint(o[i][0].y), __float_as_uint(o[i][1].y), false, false);
    o[i][0] = f32x2{__uint_as_float(e0[0]), __uint_as_float(e1[0])}; o[i][1] = f32x2{__uint_as_float(e0[1]), __uint_as_float(e1[1])};
  }
  SB();
#pragma unroll
  for (int i = 0; i < 8; ++i) { const f32x2 a_ = o[i][0], b_ = o[i][1]; o[i][0] = a_ + b_; o[i][1] = a_ - b_; }
}

struct TileD { int k, c, src, w; };
DI TileD tile_desc(int tau, int k1, int kd1) {
  TileD t; const int i = tau >> 1, ka = tau & 1, hi = i >= 5;
  t.c = kd1 + 2 * ka;
  t.src = k1 ? -1 : (kd1 ? ka : hi);
  t.k = 256 * i + (k1 ? k1 : (kd1 ? 128 * ka : -1152 * hi));
  t.w = k1 ? 1 : (kd1 ? 2 : (tau >= 18 ? 0 : ((i & 3) == 0 && i < 8 ? 1 : 2)));
  return t;
}
DI void m0_fwd(bf16x8& o, int n, int l15, int g, int k1, int kd1) {
  const TileD t = tile_desc(8 * n + (l15 >> 1), k1, kd1); f32x4 v[2];
  const float ak = (float)t.k * (-1.f / 2048.f), a0 = fmaf(ak, (float)(2 * g), -0.25f * (float)(l15 & 1)), ac = -0.25f * (float)t.c;
#pragma unroll
  for (int j = 0; j < 8; ++j) {
    const int rs = j & 1;
    const float c = cosr(a0 + ((j & 2) ? ak : 0.f) + ((j & 4) ? ac : 0.f) + (t.src < 0 ? (rs ? 0.125f : -0.125f) : 0.f));
    v[j >> 2][j & 3] = (t.src >= 0 && (t.w == 0 || t.src != rs)) ? 0.f : c;
  }
  o = pk8(v[0], v[1]);
}
DI void tr2(bf16x8& X, bf16x8& Y, bf16x8 A, bf16x8 B, u32 wr, u32 rd) {
  const u32x4 a = __builtin_bit_cast(u32x4, A), b = __builtin_bit_cast(u32x4, B);
  FENCE(); lw<u32x2>(wr, a.xy); lw<u32x2>(wr + 512, b.xy); lw<u32x2>(wr + 1024, a.zw); lw<u32x2>(wr + 1536, b.zw); FENCE();
  X = cat(trr(rd), trr(rd + 512)); Y = cat(trr(rd + 1024), trr(rd + 1536)); FENCE();
}

template <int O> DI void ldhi(float& v, u32 a) { asm volatile("ds_read_u16_d16_hi %0, %1 offset:%2" : "+v"(v) : "v"(a), "n"(O) : "memory"); }
#define BQ 12
#define LEAD 2
constexpr int g1_wait(int j) { int c = j <= 1 ? 4 : 0; for (int u = j - LEAD + 1; u < j; ++u) c += (u + LEAD < 16 ? 8 : 0) + (u == 15 - 2 * LEAD ? 1 : 0); return c; }
#define SE 2
#define TU 12
#define NE 2
constexpr int g2_c0(int u) { return u >= 13 ? 64 : 16 + 4 * NE + u * (48 - 4 * NE) / 13; }
constexpr bool g2_ok() { for (int u = 0; u < 13; ++u) for (int e = g2_c0(u); e < g2_c0(u + 1); ++e) if (u > e / 4 - 3 || (e / 4 >= 9 && u < e / 4 - 5)) return false; return true; }
static_assert(g2_ok(), "C tile schedule");
constexpr int g2_pre(int u) { return u < 0 ? 0 : g2_c0(u + 1) - g2_c0(u) + (u == 0 || u == 3 ? 1 : 0); }
constexpr int g2_post(int u) { return (u >= TU && u < TU + 4 ? 3 : 0) + (u == TU - 5 ? 1 : 0); }
constexpr int g2_wait(int u) { int c = g2_post(u - 3); for (int v = u - 2; v < u; ++v) c += g2_pre(v) + (v + 3 < 16 ? 4 : 0) + g2_post(v); return c; }
template <int N> struct IC { static constexpr int v = N; };
template <int I, int N, class F> DI void sfor(F&& f) { if constexpr (I < N) { f(IC<I>{}); sfor<I + 1, N>(f); } }

typedef __attribute__((ext_vector_type(8))) u64 u64x8;
typedef __attribute__((ext_vector_type(4))) u64 u64x4;
typedef __attribute__((ext_vector_type(2))) u64 u64x2;
__global__ __launch_bounds__(512, 2) void k_all(const u16*, const u16*, const u16*, const u16*, const u16*, const u16*, const u16*, const u16*,
    const u16*, const u16*, const u16*, const u16*, char*, float*) {
#define GETARGS() u64x8 ka; u64x4 kb_; u64x2 kc; \
  asm volatile("s_load_dwordx16 %0, %3, 0x0\n\ts_load_dwordx8 %1, %3, 0x40\n\ts_load_dwordx4 %2, %3, 0x60" : "=&s"(ka), "=&s"(kb_), "=&s"(kc) : "s"(__builtin_amdgcn_kernarg_segment_ptr()) : "memory")
  extern __shared__ __attribute__((aligned(16))) char lds_[];
  const u32 L = (u32)(uintptr_t)(LDSP char*)lds_;
  const int lane = threadIdx.x & 63, wv = __builtin_amdgcn_readfirstlane(threadIdx.x >> 6), w = wv & 3, rh = wv >> 2, l15 = lane & 15, g = lane >> 4;
  const int bid = blockIdx.x, xcd = bid & 7, jj = bid >> 3, ti = jj & 3, pj = jj >> 2;
  const int tt = (xcd >> 1) * 4 + ti, pt = (xcd & 1) * 8 + pj;
  const int s2 = tt >> 1, sh = tt & 1, ct = pt;
  const int lr8 = lane >> 3, lc8 = lane & 7, q = l15 >> 2, pq = lane & 3;
  int rot = tt;
  u32 gq[2]; u64 outp, wsp;
#define ARGS() asm volatile("s_waitcnt lgkmcnt(0)" : "+s"(ka), "+s"(kb_), "+s"(kc) :: "memory"); \
  char* ws = (char*)kc[0]; outp = kc[1]; wsp = kc[0]; \
  const rsrc_t xr = mkr((const void*)ka[0], 4u << 20), wsr = mkr(ws, WS_END), cr = mkr((const void*)((ct & 8) ? kb_[1] : kb_[0]), 2u << 20); \
  auto issue_a = [&](int rr, int hf) { const int r = (rr + r0) & 3; blds(xr, vA, (8 * r + 1024 * hf) * 2048, L + r * 8192 + hf * 4096 + w * 1024, L_RA); }; \
  u32 vC[2]; vC[0] = (u32)(32 * (ct ^ ((w & 1) * 8)) + 512 * (w >> 1) + lr8) * 2048u + 16 * (lc8 ^ (lr8 >> 1)); vC[1] = vC[0] ^ 64; \
  auto c_piece = [&](int i, int e) { blds(cr, vC[e & 1], e * 8 * 2048 + ((i + rot) & 15) * 128, L + w * 4096, (i % 6) * 16384 + e * 1024); }; \
  const u32 vGo = WS_G + tt * 512 + (u32)((w & 1) * 64 + lr8 * 4 + 2 * rh + (w >> 1)) * 4

  if (rh) {
    GETARGS();
    {
      u64 pc; u32 nb; asm volatile("s_getpc_b64 %0\n.Lcode_lo:\n\ts_mov_b32 %1, .Lcode_hi-.Lcode_lo\n\t.subsection 1\n.Lcode_hi:\n\t.subsection 0" : "=s"(pc), "=s"(nb));
      if (w == 3) touch(mkr((const void*)(pc & ~127ull), min(nb, 61440u)), (lane * 32 + jj) * 128, L);
    }
    const int ii = 8 * (w & 1) + lr8, swz = 16 * (lc8 ^ ((ii >> 1) & 7)), ac = lc8 ^ (((lr8 >> 1) & 1) | ((w & 1) << 1)), r0 = 2 * sh + (ct & 1);
    u32 vB = (u32)(64 * pt + ii) * 2048u + swz, vA = (u32)(s2 + 32 * (8 * w + lr8)) * 2048u + 64 * ct + 16 * (ac & 3) + 1024 * (ac >> 2);
    PIN(vA); PIN(vB);
    lw<u32>(L + L_SYNC + 4 * wv, 0u);
    ARGS();
    const rsrc_t br = mkr((const void*)((w & 2) ? ka[2] : ka[1]), 2u << 20);
    auto issue_b1 = [&](int j, int i) { blds(br, vB, 16 * i * 2048 + ((j + rot) & 15) * 128, L + w * 1024, ((j + 3) & 3) * 32768 + 16384 + i * 4096); };
    const int kap = 16 * sh + l15, ecol = 8 * w + 4 * (g & 1) + 2 * (g >> 1);
    const u32 vl = lane * 16;
    u64 tag; u32 q_ds[2];
    auto smalls = [&]() {
    tag = bl2c(wsr);
    if (w == 0) st4c(u32x4{SENT, SENT, SENT, SENT}, wsr, lane < 32 ? vl : WS_END, WS_AGG + tt * 8192 + pt * 512);
    if (w == 2) touch(mkr(ws + WS_FP, WS_G + 8192 - WS_FP), (lane * 256 + bid) * 128, L);
    if (w & 1) touch(mkr((const void*)kb_[3], 2048), ((jj & 15) + 16 * lane) * 128, L);
    q_ds[0] = bl1(mkr((const void*)kb_[2], 2048), 2 * ecol, 64 * ct); q_ds[1] = bl1(mkr((const void*)kb_[2], 2048), 2 * ecol, 64 * ct + 1024);
    };
    auto xpars = [&]() { u64 p3 = ka[3], p4 = ka[4], p5 = ka[5]; PINS(p3); PINS(p4); PINS(p5); const u64 pa_ = (w & 1) ? p4 : p3; if (w < 3) blhl(mkr((const void*)((w & 2) ? p5 : pa_), 2048), (64 * pt + lane) * 2, L + w * 256, L_V + 16384); };
    xpars();
    issue_a(0, 0); issue_a(0, 1);
    issue_a(1, 0); issue_a(1, 1);
    SB();
    const u32 vX = (u32)(128 * tt + 16 * (ii >> 2) + 8 * (ii & 1) + 2 * (w >> 1) + ((ii >> 1) & 1)) * 2048u + swz;
    auto issue_x1 = [&](int j, int i) { blds(xr, vX, (64 * (i >> 1) + 4 * (i & 1)) * 2048 + ((j + rot) & 15) * 128, L + w * 1024, ((j + 3) & 3) * 32768 + i * 4096); };
    const rsrc_t w0 = mkr((const void*)ka[6], 2048 * 1026), w1 = mkr((const void*)ka[7], 2048 * 1026);
    const u32 rep = (bid & (NREP - 1)) * 2048;
    TX(16);
    const int k1 = xcd * 16 + (jj >> 1), kd1 = jj & 1, cc = kd1 + 2 * (w & 1);
    const bool sp0 = (k1 == 0 && kd1 == 0);
    bf16x8 m0[2], mi[2], m0x, mix[2], fa_r, fa_i, fb_r, fb_i, gb_r, gb_i, ga_r, ga_i; f32x4 t1c, t1s, t2c, t2s;
    u32 vW[5], wa[5], wo[5]; float fr[4][4], fi[4][4], er[4], ei[4];
    const float fl = (float)l15, fg = (float)(4 * g), fc = (float)cc;
    const u32 tw = L + L_SEG + w * 2048 + l15 * 32 + 8 * g, tr = L + L_SEG + w * 2048 + (4 * g + q) * 32 + 8 * pq;
    SB();
    {
      const float st = fmaf(fl, -4.f / 64.f, fc * (-1.f / 64.f));
#pragma unroll
      for (int e = 0; e < 4; ++e) { const float a = (fg + (float)e) * st; t2c[e] = cosr(a); t2s[e] = sinr(a); t1c[e] = cosr(a * (1.f / 16.f)); t1s[e] = sinr(a * (1.f / 16.f)); }
      fa_r = pk8(t2c, -t2s); fa_i = pk8(t2s, t2c); PIN(t1c); PIN(t1s);
      PIN(fa_r); PIN(fa_i);
      const float fm = (float)(l15 < 8 ? l15 : l15 ^ 3) * (-1.f / 16.f); const u32 sl = l15 < 8 ? 0u : 0x80008000u;
#pragma unroll
      for (int e = 0; e < 4; ++e) { const float a = (fg + (float)e) * fm; t2c[e] = cosr(a); t2s[e] = sinr(a); }
      fb_r = pk8(t2c, -t2s); fb_i = asf(__builtin_bit_cast(u32x4, pk8(t2s, t2c)) ^ sl);
      PIN(fb_r); PIN(fb_i);
    }
    u32 xa[2], ua, us, us0, fo, ap0, wp0, tr_, tw_;
    const u32 vo = L + L_V + w * L_CH;
    int wid, wsl; f32x4 uc, us_;
    auto fill = [&](auto k_) {
      constexpr int k = decltype(k_)::v;
      SB();
      if constexpr (k == -4) {
        const float mk = (float)(k1 + 256 * (l15 >> 2)) * (-1.f / 2048.f), mc = -0.25f * (float)(kd1 + (l15 & 2)), ma = fmaf(mk, 0.5f * fg, fmaf((float)(l15 & 1), -0.25f, -0.125f));
        u32x4 f;
#pragma unroll
        for (int e = 0; e < 4; ++e) { const float a = ma + ((e & 1) ? mk : 0.f) + ((e & 2) ? mc : 0.f); f[e] = pk2(cosr(a), -sinr(a)); }
        m0[0] = asf(f); m0[1] = asf(f ^ u32x4{0u, 0x80008000u, 0u, 0x80008000u});
        const float b = fmaf((float)(l15 >> 1), fmaf(fg, -1.f / 32.f, (float)k1 * (-1.f / 2048.f)), fmaf((float)(l15 & 1), 0.75f, fmaf((float)k1, 1.f / 512.f, -0.125f))), b1 = b - 0.25f * (float)kd1;
        const u32 p0 = pk2(cosr(b), sinr(b)), p1 = pk2(cosr(b1), sinr(b1)), sgn = (l15 & 2) ? 0x80008000u : 0u;
        mi[0] = asf(u32x4{p0, p0, p0 ^ sgn, p0 ^ sgn}); mi[1] = asf(u32x4{p1, p1 ^ 0x80008000u, p1 ^ sgn, p1 ^ sgn ^ 0x80008000u});
        if (__builtin_expect(k1 == 0, 0)) { m0_fwd(m0[0], 0, l15, g, 0, kd1); m0_fwd(m0[1], 1, l15, g, 0, kd1); m0_fwd(m0x, 2, l15, g, 0, 0); tr2(mix[0], mix[1], m0x, bf16x8{}, tw, tr); tr2(mi[0], mi[1], m0[0], m0[1], tw, tr); }
        PIN(m0[0]); PIN(m0[1]); PIN(mi[0]); PIN(mi[1]);
      }
      if constexpr (k == -2 || k == -1) { constexpr int i = k + 2; const int n = i ? 63 - kap : kap, rho = 64 * (n & 3) + (n >> 2); xa[i] = L + L_RA + rho * 128 + 16 * (w ^ (((rho >> 1) & 1) | ((rho >> 3) & 1) << 1)) + 2 * (ecol & 7); PIN(xa[i]); }
      if constexpr (k >= 0 && k < 4) {
        const float a = fl * fmaf(fg + (float)k, 4.f / 1024.f, fc * (1.f / 1024.f));
        t2c[k] = cosr(a) * (1.f / 1048576.f); t2s[k] = sinr(a) * (1.f / 1048576.f); uc[k] = cosr(16.f * a); us_[k] = sinr(16.f * a); PIN(t2c); PIN(t2s); PIN(uc); PIN(us_);
      }
      if constexpr (k == 4) { ga_r = pk8(uc, -us_); ga_i = pk8(us_, uc); PIN(ga_r); PIN(ga_i); }
      if constexpr (k >= 5 && k < 9) { const float a = (fg + (g < 2 ? (float)(k - 5) : (float)(8 - k))) * fl * (-1.f / 16.f); uc[k - 5] = cosr(a); us_[k - 5] = sinr(a); PIN(uc); PIN(us_); }
      if constexpr (k == 9) {
        const u32 sg = g < 2 ? 0u : 0x80008000u; const u32x4 t = __builtin_bit_cast(u32x4, pk8(uc, us_));
        gb_r = asf(u32x4{t.x, t.y, t.z ^ sg, t.w ^ sg}); gb_i = asf(u32x4{t.z ^ 0x80008000u, t.w ^ 0x80008000u, t.x ^ sg, t.y ^ sg}); PIN(gb_r); PIN(gb_i);
      }
      if constexpr (k == 12) { ua = L + L_V + (16 * w + g) * 256 + (lr8 & 1) * 128 + 16 * (lc8 ^ g); PIN(ua); }
      if constexpr (k == 13) { us = (u32)(64 * w + 2 * g + (lr8 & 1) + 32 * (sh ^ (w & 1))) * 16384u + lc8 * 16; PIN(us); }
      if constexpr (k == 14) { us0 = (w & 1) && g == 0 ? us ^ (32 * 16384) : us; PIN(us0); }
      if constexpr (k == 11) { fo = lane < NREP ? lane * 2048 + bid * 8 : WS_END; PIN(fo); }
      if constexpr (k == 33) { ap0 = vo + (4 * g + q) * 64 + 16 * pq; PIN(ap0); }
      if constexpr (k == 34) { wp0 = vo + l15 * L_PS + 16 * g; PIN(wp0); }
      if constexpr (k == 35) { tr_ = L + L_V + w * 2 * L_PS + g * L_CH + q * 32 + 8 * pq; PIN(tr_); }
      if constexpr (k == 36) { tw_ = L + L_V + w * 2 * L_PS + q * L_CH + pq * 32 + 8 * g; PIN(tw_); }
      if constexpr (k == 37) { PIN(vC[0]); PIN(vC[1]); }
      if constexpr (k == 40) { wid = 2 * cc + 8 * l15 + 512 * g; PIN(wid); }
      if constexpr (k == 41) { wa[0] = g < 2 ? L + L_W + (w >> 1) * 1056 + ((k1 * 1026) & 15) + wid : L + L_W + (8 + (w >> 1)) * 1056 + ((-k1 * 1026) & 15) + 1664 - wid; PIN(wa[0]); }
      if constexpr (k == 42) { wo[0] = g < 2 ? L + L_W + (8 + (w >> 1)) * 1056 + ((-k1 * 1026) & 15) : L + L_W + (w >> 1) * 1056 + ((k1 * 1026) & 15) + 1024; PIN(wo[0]); }
      if constexpr (k >= 43 && k < 46) { wa[k - 42] = wa[0] + 2112 * (k - 42); wo[k - 42] = wo[0] + 2112 * (k - 42); PIN(wa[k - 42]); PIN(wo[k - 42]); }
      if constexpr (k == 46) {
        if (__builtin_expect(k1 == 0, 0)) {
#pragma unroll
          for (int i = 0; i < 5; ++i) {
            const int kk = tile_desc(w + 4 * i, 0, kd1).k;
            const u32 wp = L + L_W + (kk >> 7) * 1056, wq = L + L_W + (((2048 - kk) & 2047) >> 7) * 1056;
            wa[i] = g < 2 ? wp + wid : wq + 1664 - wid; wo[i] = g < 2 ? wq : wp + 1024;
          }
        }
      }
      if constexpr (k >= 48 && k < 73) {
        constexpr int i = (k - 48) / 5, ph = (k - 48) % 5;
        if (ph == 0) { wid = 64 * (w + 4 * i) + lane; wsl = (wid * 993) >> 16; PIN(wid); PIN(wsl); }
        if (ph == 1) { wsl = min(wsl, 15); wid = 16 * wid - 1056 * wsl; PIN(wid); PIN(wsl); }
        if (ph == 2) { wsl = k1 + (k1 ? 256 : 128) * wsl; PIN(wsl); }
        if (ph == 3) { wsl = wsl < 2048 ? wsl : 4096 - wsl; PIN(wsl); }
        if (ph == 4) { vW[i] = ((wsl * 1026) & ~15) + wid; PIN(vW[i]); }
      }
      if constexpr (k >= 76 && k < 96) {
#pragma unroll
        for (int r = 2 * (k & 1); r < 2 * (k & 1) + 2; ++r) { constexpr int z_ = (k - 76) >> 1; float& z = z_ < 4 ? fr[z_ & 3][r] : z_ < 8 ? fi[z_ & 3][r] : z_ == 8 ? er[r] : ei[r]; z = 0.f; PIN(z); }
      }
      SB();
    };
    auto issue_t = [&](auto j_) {
      constexpr int j = decltype(j_)::v;
      sfor<0, 8>([&](auto i_) { constexpr int i = decltype(i_)::v; if (i < 4) issue_b1(j, i); else issue_x1(j, i - 4); if constexpr (j > 2) fill(IC<8 * j - 8 + i>{}); });
    };
    sfor<-4, 0>(fill);
    TX(17);
    WAITV(4); BAR();
    issue_a(2, 0); issue_a(2, 1); issue_a(3, 0); issue_a(3, 1); issue_b1(0, 0); issue_b1(0, 1); issue_b1(0, 2); issue_b1(0, 3);
    smalls();
    WAITV(8); LBAR();
    u32 sk[8][2];
    auto skf = [&]() {
#pragma unroll
    for (int i = 0; i < 8; ++i) { const u32 a = xa[i >> 2] + 2048 * (i < 4 ? i : (i + 3) & 3); sk[i][0] = lr<u32>(a); sk[i][1] = lr<u32>(a + 64); }
    };
    skf();
    SB();
    SB();
#pragma unroll
    for (int i = 0; i < 4; ++i) issue_x1(0, i);
    sfor<11, 15>(fill); sfor<0, 11>(fill);
    WAITV(4);
    issue_t(IC<1>{});
    {
      u32x4 uq[4];
      gwait(L + L_SYNC, 1);
#pragma unroll
      for (int i = 0; i < 4; ++i) uq[i] = lr<u32x4>((ua ^ (64 * (i & 1))) + 1024 * i);
#pragma unroll
      for (int i = 0; i < 4; ++i) st4c(uq[i], wsr, i == 0 ? us0 : us, WS_U + ct * 1024 + s2 * 128 + i * 8 * 16384);
    }
    if (LEAD == 3) issue_t(IC<2>{});
    TX(18);
    u64 fu;
    const f32x4 z4 = {};
    bf16x8 a0[2][2];
    sfor<0, 17 - LEAD>([&](auto j_) {
      constexpr int j = decltype(j_)::v;
      WAITV(g1_wait(j));
      if (j == 0) {
        PIN(tag); PIN(q_ds[0]); PIN(q_ds[1]);
        tag += 0x9e3779b97f4a7c15ull;
        if (w == 0) lw<u64>(L + L_SYNC + 32, tag);
        LBAR();
      } else BAR();
      ITS(1, j);
      PINS(rot);
      if (j == JF && w == 0) st2c(tag, wsr, fo, WS_FU);
      if (j == 15 - 2 * LEAD) asm volatile("buffer_load_dwordx2 %0, %1, %2, %3 offen sc1" : "=v"(fu) : "v"(lane * 8), "s"(wsr), "s"(WS_FU + rep + w * 512) : "memory");
      SB();
      if constexpr (j + LEAD < 16) issue_t(IC<j + LEAD>{});
      SB();
      if (j == 15 - LEAD) { PIN(fu); if (__builtin_expect(!__all(fu == tag), 0)) pollq(wsr, WS_FU + rep + w * 512, lane, tag); }
      SB();
    });
#pragma unroll
    for (int i = 0; i < 4; ++i) bldsc(wsr, vl, WS_U + (u32)(2 * k1 + kd1) * 16384u + w * 2048 + (i >> 1) * 8192 + (i & 1) * 1024, L + w * L_CH, L_V + i * 1024);
#pragma unroll
    for (int i = 0; i < NW13; ++i) blds(w0, vW[i], 0, L + w * 1024, L_W + i * 4096);
    WAITV(4 + NW13); BAR();
    ITS(1, 17 - LEAD);
    auto wissue = [&]() {
#pragma unroll
      for (int i = 0; i < 4; ++i) { if (i >= NW13) blds(w0, vW[i], 0, L + w * 1024, L_W + i * 4096); blds(w1, vW[i], 0, L + w * 1024, L_W + L_WS + i * 4096); }
      if (w == 0) { blds(w0, vW[4], 0, L, L_W + 16384); blds(w1, vW[4], 0, L, L_W + L_WS + 16384); }
#pragma unroll
      for (int e = 0; e < 4 * NC0; ++e) c_piece(e >> 2, e & 3);
    };
    {
      WAITV(NW13);
#pragma unroll
      for (int cb = 0; cb < 2; ++cb) { a0[cb][0] = cat(trr(ap0 + 1024 * cb), trr(ap0 + 1024 * cb + 2048)); a0[cb][1] = cat(trr(ap0 + 1024 * cb + 8), trr(ap0 + 1024 * cb + 2056)); }
      FENCE();
      wissue();
      f32x4 d[2][2][2];
#pragma unroll
      for (int i = 0; i < 8; ++i) d[i >> 2][(i >> 1) & 1][i & 1] = mma(a0[(i >> 1) & 1][i & 1], m0[i >> 2], z4);
#pragma unroll
      for (int i = 0; i < 4; ++i) lw<bf16x8>(wp0 + 16 * (i >> 1) * L_PS + 64 * (i & 1), pk8(d[i >> 1][i & 1][0], d[i >> 1][i & 1][1]));
      if (__builtin_expect(sp0, 0)) {
#pragma unroll
        for (int cb = 0; cb < 2; ++cb) { const bf16x8 o = pk8(mma(a0[cb][0], m0x, z4), mma(a0[cb][1], m0x, z4)); if (l15 < 4) lw<bf16x8>(wp0 + 32 * L_PS + 64 * cb, o); }
      }
      gbar(L + L_SYNC + 16, w, 3);
    }
    TX(8);
    auto tiles = [&](auto nt_, auto i0_) {
      constexpr int NT = decltype(nt_)::v, i0 = decltype(i0_)::v;
      const u32 pr = tr_ + i0 * 8 * L_PS, pw_ = tw_ + i0 * 8 * L_PS;
      bf16x8 a[NT]; f32x4 yr[NT], yi[NT];
#pragma unroll
      for (int i = 0; i < NT; ++i) a[i] = cat(trr(pr + i * 8 * L_PS), trr(pr + i * 8 * L_PS + L_PS));
      SB();
#pragma unroll
      for (int i = 0; i < NT; ++i) { yr[i] = mma(a[i], fa_r, z4); yi[i] = mma(a[i], fa_i, z4); }
      SB();
#pragma unroll
      for (int i = 0; i < NT; ++i) { a[i] = pk8(yr[i] * t1c - yi[i] * t1s, yr[i] * t1s + yi[i] * t1c); yr[i] = mma(fb_r, a[i], z4); yi[i] = mma(fb_i, a[i], z4); SB(); }
#pragma unroll
      for (int i = 0; i < NT; ++i) asm volatile("" :: "v"(yr[i]), "v"(yi[i]));
      if (!i0) {
        WAITV(4 * NC0); gbar(L + L_SYNC + 16, w, 4);
      }
      if (!i0) TX(9);
#pragma unroll
      for (int i = 0; i < NT; ++i) {
        if (i0) {
#pragma unroll
          for (int r = 0; r < 4; ++r) fr[i][r] = fi[i][r] = er[i] = ei[i] = 0.f;
        }
        ldhi<0>(fr[i][0], wa[i0 + i]); ldhi<128>(fr[i][1], wa[i0 + i]); ldhi<256>(fr[i][2], wa[i0 + i]); ldhi<384>(fr[i][3], wa[i0 + i]);
        ldhi<L_WS>(fi[i][0], wa[i0 + i]); ldhi<L_WS + 128>(fi[i][1], wa[i0 + i]); ldhi<L_WS + 256>(fi[i][2], wa[i0 + i]); ldhi<L_WS + 384>(fi[i][3], wa[i0 + i]);
        ldhi<0>(er[i], wo[i0 + i]); ldhi<L_WS>(ei[i], wo[i0 + i]);
      }
      if (!i0) gwait(L + L_SYNC, 2); else WAITL(0);
#pragma unroll
      for (int i = 0; i < NT; ++i)
#pragma unroll
        for (int r = 0; r < 4; ++r) { PIN(fr[i][r]); PIN(fi[i][r]); if (r == 0) { PIN(er[i]); PIN(ei[i]); } }
      if (cc == 0) {
        const float b0 = lane == 0 ? 0.5f : 0.f, b3 = lane == 32 ? 0.5f : 0.f;
#pragma unroll
        for (int i = 0; i < NT; ++i) {
          float t;
          asm volatile("v_sub_f32 %4, %5, %0\n\tv_fmac_f32 %0, %7, %4\n\tv_add_f32 %4, %6, %1\n\tv_fma_f32 %1, -%7, %4, %1\n\t"
                       "v_sub_f32 %4, %5, %2\n\tv_fmac_f32 %2, %8, %4\n\tv_add_f32 %4, %6, %3\n\tv_fma_f32 %3, -%8, %4, %3"
                       : "+v"(fr[i][0]), "+v"(fi[i][0]), "+v"(fr[i][3]), "+v"(fi[i][3]), "=&v"(t) : "v"(er[i]), "v"(ei[i]), "v"(b0), "v"(b3));
        }
      }
      if (__builtin_expect(k1 == 0, 0)) {
#pragma unroll
        for (int i = 0; i < NT; ++i) { const float wt_ = (float)tile_desc(w + 4 * (i0 + i), 0, kd1).w; yr[i] *= wt_; yi[i] *= wt_; }
      }
      SB();
#pragma unroll
      for (int i = 0; i < NT; ++i) {
        const f32x4 vr = {fr[i][0], fr[i][1], fr[i][2], fr[i][3]}, vi = {fi[i][0], fi[i][1], fi[i][2], fi[i][3]};
        a[i] = pk8(yr[i] * vr - yi[i] * vi, yr[i] * vi + yi[i] * vr); yr[i] = mma(a[i], gb_r, z4); yi[i] = mma(a[i], gb_i, z4);
        SB();
        if (!i0) {
#pragma unroll
          for (int e = 4 * NC0 + (4 - NC0) * i; e < 4 * NC0 + (4 - NC0) * (i + 1); ++e) c_piece(e >> 2, e & 3);
        }
        SB();
      }
#pragma unroll
      for (int i = 0; i < NT; ++i) {
        a[i] = pk8(yr[i] * t2c - yi[i] * t2s, yr[i] * t2s + yi[i] * t2c); yr[i] = mma(a[i], ga_r, z4); yi[i] = mma(a[i], ga_i, z4);
        SB();
      }
#pragma unroll
      for (int i = 0; i < NT; ++i) { lw<u32x2>(pw_ + i * 8 * L_PS, u32x2{pk2(yr[i].xy), pk2(yr[i].zw)}); lw<u32x2>(pw_ + i * 8 * L_PS + L_PS, u32x2{pk2(yi[i].xy), pk2(yi[i].zw)}); }
    };
    tiles(IC<4>{}, IC<0>{});
    if (__builtin_expect(sp0 && w < 2, 0)) tiles(IC<1>{}, IC<4>{});
    TX(23);
    gbar(L + L_SYNC + 16, w, 5);
    TX(24);
    u32x4 tq[4];
    {
      f32x4 o[2][2][2];
      const u32 ap = vo + (4 * g + q) * L_PS + 16 * pq;
#pragma unroll
      for (int cb = 0; cb < 2; ++cb) { a0[cb][0] = cat(trr(ap + 64 * cb), trr(ap + 64 * cb + 16 * L_PS)); a0[cb][1] = cat(trr(ap + 64 * cb + 8), trr(ap + 64 * cb + 16 * L_PS + 8)); }
#pragma unroll
      for (int i = 0; i < 8; ++i) o[i >> 2][(i >> 1) & 1][i & 1] = mma(a0[(i >> 1) & 1][i & 1], mi[i >> 2], z4);
      if (__builtin_expect(sp0, 0)) {
        const u32 ax = vo + (32 + q) * L_PS + 16 * pq;
#pragma unroll
        for (int i = 0; i < 4; ++i) { const u32x2 t = trr(ax + 64 * (i >> 1) + 8 * (i & 1)); const bf16x8 a = cat(t, t); o[0][i >> 1][i & 1] = mma(a, mix[0], o[0][i >> 1][i & 1]); o[1][i >> 1][i & 1] = mma(a, mix[1], o[1][i >> 1][i & 1]); }
      }
      FENCE();
#pragma unroll
      for (int i = 0; i < 4; ++i) lw<bf16x8>(vo + i * 1280 + l15 * 80 + 16 * g, pk8(o[i >> 1][i & 1][0], o[i >> 1][i & 1][1]));
      FENCE();
#pragma unroll
      for (int i = 0; i < 4; ++i) tq[i] = lr<u32x4>(vo + i * 1280 + (lane >> 2) * 80 + pq * 16);
#pragma unroll
      for (int i = 0; i < 4; ++i) PIN(tq[i]);
    }
    TX(10);
    u32x4 tab[2][4];
    if (__builtin_expect(bid == 0 && w == 0, 0)) st2c(tag, wsr, lane == 0 ? WS_E : WS_END, 0);
#pragma unroll
    for (int i = 0; i < 16; ++i) { const u32 xv = sk[i >> 1][i & 1], dv = q_ds[i & 1]; sk[i >> 1][i & 1] = pk2(bf2f(xv & 0xffffu) * bf2f(dv & 0xffffu), bfhi(xv) * bfhi(dv)); PIN(sk[i >> 1][i & 1]); }
    TX(11);
    const u32 hb = WS_H + (u32)(tt * 8 + 2 * w) * 65536u + ((ct & 8) ? 32768 : 0);
    u32 fa_ = lane < 32 ? (4 * w + 2 * (lane & 1) + (sh ^ (lane & 1))) * 128 + (((lane >> 1) + rot) & 15) * 8 : WS_END, miss; PIN(fa_);
    u64 fh;
    auto fload = [&]() { asm volatile("buffer_load_dwordx2 %0, %1, %2, %3 offen sc1" : "=v"(fh) : "v"(fa_), "s"(wsr), "s"(WS_FH + rep) : "memory"); };
    auto fneed = [&](u32 bits) {
      if (__builtin_expect((miss & bits) != 0, 0)) {
        NOUNROLL for (u32 spn = 0; spn < SPIN_CAP; ++spn) {
          u64 a; asm volatile("buffer_load_dwordx2 %0, %1, %2, %3 offen sc1\n\ts_waitcnt vmcnt(0)" : "=&v"(a) : "v"(fa_), "s"(wsr), "s"(WS_FH + rep) : "memory");
          miss = (u32)__builtin_amdgcn_ballot_w64(a != tag);
          if (!(miss & bits)) break;
          __builtin_amdgcn_s_sleep(POLLSLP);
        }
      }
    };
    auto h_issue = [&](int u) {
      const u32 so = hb + ((u + rot) & 15) * 2048;
#pragma unroll
      for (int m = 0; m < 4; ++m) bldsc(wsr, vl, so + (m >> 1) * 65536 + (m & 1) * 1024, L + w * 4096, 98304 + (u & 3) * 16384 + m * 1024);
    };
    auto extras = [&]() {
      const u32 to = (u32)lr8 * (16 * 32768) + (u32)(2 * k1 + kd1) * 128 + lc8 * 16;
#pragma unroll
      for (int i = 0; i < 4; ++i) st4c(tq[i], wsr, to, WS_T + (8 * (i >> 1) + 2 * w + (i & 1)) * 32768);
#pragma unroll
      for (int e = 0; e < 4 * NE; ++e) c_piece(4 + (e >> 2), e & 3);
    };
    WAITV(0); BAR();
    TX(25);
    extras();
    fload();
    SB();
    {
      const float tk = (float)(2 * kap + 1) * (1.f / 512.f), tc = 2.f * cosr(4.f * tk);
#pragma unroll
      for (int i = 0; i < 8; ++i) {
        const float kk = (i & 4) ? -tk : tk;
        tab[i >> 2][i & 3] = cos8(fmaf(kk, 8.f * fg + (float)(i & 3), -0.125f), kk, tc); PIN(tab[i >> 2][i & 3]);
      }
    }
    TX(26);
    WAITV(0); PIN(fh); miss = (u32)__builtin_amdgcn_ballot_w64(fh != tag);
    fneed(FMASK); h_issue(0); h_issue(1); h_issue(2);
    SB();
    u32 ta[8];
    auto taf = [&]() {
#pragma unroll
    for (int i = 0; i < 8; ++i) {
      const int kb = 16 * i + 4 * w + g, k = (lane & 4) ? (kb ? 256 - kb : 128) : kb, rho = 64 * (k & 3) + (k >> 2);
      ta[i] = L + rho * 128 + 16 * ((((lane >> 1) & 4) | pq) ^ (((rho >> 1) & 1) | ((rho >> 3) & 1) << 1)); PIN(ta[i]);
    }
    };
    WAITV(8);
    TX(27);
    u64 ft;
    u32x4 tv[8], gv[2];
    f32x2 o[8][2];
    sfor<0, 16>([&](auto u_) {
      constexpr int u = decltype(u_)::v;
      if (u) WAITV(g2_wait(u));
      BAR();
      ITS(2, u);
      PINS(rot);
      if (u == FTU && w == 0) st2c(tag, wsr, fo, WS_FT);
      if (u == 3 || u == 6) { PIN(fh); miss &= (u32)__builtin_amdgcn_ballot_w64(fh != tag); }
      if constexpr (u + 3 < 16) fneed(3u << (2 * u + 6));
      if (u == 0 || u == 3) fload();
#pragma unroll
      for (int e = g2_c0(u); e < g2_c0(u + 1); ++e) c_piece(e >> 2, e & 3);
      if (u + 3 < 16) h_issue(u + 3);
      if constexpr (u >= TU && u < TU + 4) {
#pragma unroll
        for (int i = 2 * (u - TU); i < 2 * (u - TU) + 2; ++i) tv[i] = bl4c<0>(wsr, vl, WS_T + (s2 * 16 + ct) * 32768 + i * 4096 + w * 1024);
        if (u < TU + 2) gq[(u - TU) & 1] = bl1c(wsr, vGo + 128 * (u - TU), 0); else if (u == TU + 2) gv[0] = bl4c<0>(wsr, WS_G + tt * 512 + l15 * 16, 0); else gv[1] = bl4c<256>(wsr, WS_G + tt * 512 + (15 - l15) * 16, 0);
      }
      if (u == TU - 5) asm volatile("buffer_load_dwordx2 %0, %1, %2, %3 offen sc1" : "=v"(ft) : "v"(lane * 8), "s"(wsr), "s"(WS_FT + rep + w * 512) : "memory");
      if (u == TU - 1) { PIN(ft); if (__builtin_expect(!__all(ft == tag), 0)) pollq(wsr, WS_FT + rep + w * 512, lane, tag); }
      SB();
    });
    taf();
    WAITV(0);
#pragma unroll
    for (int i = 0; i < 8; ++i) { PIN(tv[i]); lw<u32x4>(ta[i], tv[i]); }
    LBAR();
    hart256<true>(o, L + (8 * g + q) * 128 + 16 * ((4 * (pq >> 1) + w) ^ (((q >> 1) & 1) | ((g & 1) << 1))) + 8 * (pq & 1), tab, kap, false);
    PIN(gq[0]); PIN(gq[1]); PIN(gv[0]); PIN(gv[1]);
#pragma unroll
    for (int i = 0; i < 8; ++i) {
      const float gg = __uint_as_float(gv[i >> 2][i < 4 ? i : (i + 3) & 3]), og = rbf(1.f - gg);
      const int pos = i < 4 ? l15 : 15 - l15;
#pragma unroll
      for (int hf = 0; hf < 2; ++hf)
        lw<f32x2>(L + 65536 + pos * 256 + 16 * ((8 * hf + 2 * w + (g & 1)) ^ pos) + 8 * (g >> 1) + 4096 * (i < 4 ? 2 * i : 2 * ((i + 3) & 3) + 1), fma2(f32x2{og, og}, f32x2{bf2f(sk[i][hf] & 0xffffu), bfhi(sk[i][hf])}, gg * o[i][hf]));
    }
    TX(30);
  } else {
    GETARGS();
    lw<u32>(L + L_SYNC + 4 * wv, 0u);
    const int r0 = 0; const u32 vA = 0;
    ARGS();
    const int wn = w & 1, wm = w >> 1;
    const int hpos = 8 * wm + lc8, hsh = (tt ^ (tt >> 1)) & 1;
    u32 vH = (u32)(lr8 & 1) * (1u << 20) + hsh * (1u << 19) + (tt >> 1) * 65536 + (2 * pt + wn) * 1024 + g * 256 + hpos * 16;
    const u32 vAg = WS_AGG + (u32)((pt * 2 + wn) * 16 + l15) * 16;
    const int kap = 16 * sh + l15, np = (16 - l15) & 15; const bool sp = kap == 0;
    const int ecol = 8 * w + 4 * (g & 1) + 2 * (g >> 1);
    TS(19);
    u32x4 tab[2][4];
    hart_table(tab, kap, sp, g);
#pragma unroll
    for (int i = 0; i < 8; ++i) PIN(tab[i >> 2][i & 3]);
    u32 ua[6], hap = L + L_RA + (8 * g + q) * 128 + 16 * ((4 * (pq >> 1) + w) ^ (((q >> 1) & 1) | ((g & 1) << 1))) + 8 * (pq & 1);
#pragma unroll
    for (int i = 0; i < 2; ++i) {
      const int lo = i ? np : l15;
      ua[i] = L + L_V + lo * 256 + ((2 * ecol) ^ ((lo & 7) << 4)); PIN(ua[i]);
    }
    ua[2] = (ua[1] ^ 64) + (sp ? 0 : 48 * 256); ua[3] = (ua[1] ^ 64) + (sp ? 32 * 256 : 16 * 256); ua[4] = (ua[0] ^ 64) + (sp ? 16 * 256 : 0);
    PIN(ua[2]); PIN(ua[3]); PIN(ua[4]); PIN(hap);
    c2 lam, coef, l8, l16;
    auto lamf = [&]() {
    const u32 qa = L + L_V + 16384 + (32 * wn + (lane & 31)) * 4, q_dt = lr<u16>(qa), q_lr = lr<u16>(qa + 256), q_li = lr<u16>(qa + 512);
    {
      const float lre = bf2f(q_lr), lim = bf2f(q_li);
      const float dt = rbf(expf(bf2f(q_dt))), mg = expf(dt * lre);
      float sn, cs; sincosf(dt * lim, &sn, &cs);
      const float nr = mg * cs - 1.f, ni = mg * sn;
      const bool big = fabsf(lre) >= fabsf(lim);
      const float u = big ? lre : lim, v = big ? lim : lre, t = v * __builtin_amdgcn_rcpf(u), rd = 2.f * __builtin_amdgcn_rcpf(u + v * t);
      lam = {spread16(mg * cs), spread16(ni)};
      coef = {spread16(rd * (big ? nr + ni * t : nr * t + ni)), spread16(rd * (big ? ni - nr * t : ni * t - nr))};
      l8 = csq(csq(csq(lam))); l16 = csq(l8);
      pinc(lam); pinc(coef); pinc(l8); pinc(l16);
    }
    };
    TS(22);
    SB();
      u32 aa[2][2], ab[2][2];
#pragma unroll
      for (int i = 0; i < 4; ++i) {
        const int co = 16 * ((4 * (i & 1) + g) ^ (l15 >> 1)) + (i >> 1) * 65536;
        aa[i & 1][i >> 1] = L + (64 * wm + l15) * 128 + co; ab[i & 1][i >> 1] = L + 16384 + (64 * wn + l15) * 128 + co; PIN(aa[i & 1][i >> 1]); PIN(ab[i & 1][i >> 1]);
      }
    SB();
    BAR();
    lamf();
    SB();
    LBAR();
    TS(1);
    {
      f32x2 o[8][2];
      hart256<false>(o, hap, tab, kap, sp);
#pragma unroll
      for (int i = 0; i < 8; ++i) {
        const u32 a = i == 0 ? ua[0] : i == 1 ? ua[0] + 32 * 256 : i == 2 ? ua[2] : i == 3 ? ua[3] : i == 4 ? ua[4] : i == 5 ? ua[1] + 16 * 256 : i == 6 ? ua[1] + 48 * 256 : ua[4] + 32 * 256;
        lw<u32>(a, pk2(o[i][0])); lw<u32>(a + 128, pk2(o[i][1]));
      }
    }
    gpost(L + L_SYNC, w, 1);
    SB();
    c2 pw[8], wt[4], lsa, lsb, l32 = csq(l16), l64 = csq(l32);
    u32 wa[8], ra[8], pa[4], vAs = lane < 16 ? vAg : WS_END, sa = L + L_SEG + wn * 256 + l15 * 16;
#pragma unroll
    for (int n = 0; n < 4; ++n) { pa[n] = vAg + max(tt - 1 - g - 4 * n, 0) * 8192; PIN(pa[n]); }
    u32 sw = sa + (4 * wm + g) * 512;
    pinc(l32); pinc(l64); PIN(vAs); PIN(sa); PIN(sw); PIN(vH);
    auto pwf = [&]() {
      const c2 one = {{1.f, 1.f}, {0.f, 0.f}}, zero = {{0.f, 0.f}, {0.f, 0.f}};
      const c2 l128 = csq(l64), l256 = csq(l128), l512 = csq(l256);
      lsa = cmul(cmul(coef, csel(g & 1, l16, one)), cmul(csel(g & 2, l32, one), csel(wm, l64, one))); lsb = cmul(lsa, l8);
      wt[0] = cmul(csel(g & 1, l128, one), csel(g & 2, l256, one));
#pragma unroll
      for (int n = 1; n < 4; ++n) wt[n] = cmul(wt[n - 1], l512);
#pragma unroll
      for (int n = 0; n < 4; ++n) { wt[n] = csel(tt - 1 - g - 4 * n >= 0, wt[n], zero); pinc(wt[n]); }
      pinc(lsa); pinc(lsb);
    };
    auto pwg = [&]() {
      pw[0] = lam; pw[1] = csq(lam); pw[2] = cmul(pw[1], lam); pw[3] = csq(pw[1]); pw[4] = cmul(pw[3], lam); pw[5] = csq(pw[2]); pw[6] = cmul(pw[3], pw[2]); pw[7] = l8;
#pragma unroll
      for (int s = 1; s < 7; ++s) pinc(pw[s]);
    };
    u64 tag;
    f32x4 acc[4][4];
    {
      LBAR();
      TS(2);
      u64 cy0 = 0; CY0();
      {
        bf16x8 A[2][2][4], B[2][2][4];
        auto rd = [&](int j, int i) { const int sl = (j + 3) & 3, ks = i & 1, f = (i >> 1) & 3; const u32 bs = (sl & 1) * 32768 + f * 2048;
          if (i < 8) B[j & 1][ks][f] = lr<bf16x8>(ab[ks][sl >> 1] + bs); else A[j & 1][ks][f] = lr<bf16x8>(aa[ks][sl >> 1] + bs); };
#pragma unroll
        for (int i = 0; i < 16; ++i) rd(0, i);
#pragma unroll
        for (int j = 0; j < 16; ++j) {
#pragma unroll
          for (int qq = 0; qq < 32; ++qq) {
            const int p = qq >> 1, m = p >> 2, n = (m & 1) ? 3 - (p & 3) : (p & 3), ks = (qq ^ p) & 1;
            if (qq == BQ && j < 17 - LEAD) LBAR();
            SB();
            if (j || (qq & 1)) mmc(acc[m][n], A[j & 1][ks][m], B[j & 1][ks][n]); else mmz(acc[m][n], A[j & 1][ks][m], B[j & 1][ks][n]);
            SB();
            if (j < 15 && qq >= BQ && qq < BQ + 16) rd(j + 1, qq - BQ);
          }
        }
      }
      NOPS();
      CY1(20);
    }
    TS(3);
    c2 h[2][8];
#pragma unroll
    for (int s = 0; s < 8; ++s) {
#pragma unroll
      for (int ps = 0; ps < 2; ++ps) {
        const f32x4 vr = acc[s >> 1][2 * ps], vi = acc[s >> 1][2 * ps + 1];
        h[ps][s] = (s & 1) ? c2{vr.zw, vi.zw} : c2{vr.xy, vi.xy};
        if (s) { const c2 a = bc(lam, ps); h[ps][s].r = fma2(-a.i, h[ps][s - 1].i, h[ps][s].r); h[ps][s].i = fma2(a.i, h[ps][s - 1].r, h[ps][s].i); }
      }
      SB();
#pragma unroll
      for (int ps = 0; ps < 2; ++ps) if (s) { const c2 a = bc(lam, ps); h[ps][s].r = fma2(a.r, h[ps][s - 1].r, h[ps][s].r); h[ps][s].i = fma2(a.r, h[ps][s - 1].i, h[ps][s].i); }
      SB();
    }
    const c2 ea = {{h[0][7].r.x, h[1][7].r.x}, {h[0][7].i.x, h[1][7].i.x}};
    { const c2 e = cfma(l8, ea, c2{{h[0][7].r.y, h[1][7].r.y}, {h[0][7].i.y, h[1][7].i.y}});
      lw<f32x4>(sw, f32x4{e.r.x, e.r.y, e.i.x, e.i.y}); }
    gbar(L + L_SYNC, w, 2);
    u32x4 gx[4], gw[2];
    c2 cin;
    {
      c2 e[8];
#pragma unroll
      for (int j = 0; j < 8; ++j) e[j] = asc(lr<f32x4>(sa + j * 512));
      SB();
      const c2 e01 = cfma(l16, e[0], e[1]), e23 = cfma(l16, e[2], e[3]), e45 = cfma(l16, e[4], e[5]), e67 = cfma(l16, e[6], e[7]);
      const c2 e03 = cfma(l32, e01, e23), e47 = cfma(l32, e45, e67), tot = cfma(l64, e03, e47);
      st4c(__builtin_bit_cast(u32x4, f32x4{tot.r.x, tot.r.y, tot.i.x, tot.i.y}), wsr, vAs, wm ? tt * 8192 : WS_END);
      SB();
      const c2 zero = {{0.f, 0.f}, {0.f, 0.f}};
      const c2 c0 = csel(wm, e03, zero), c2_ = cfma(l32, c0, csel(wm, e45, e01)), cb = csel(g & 2, c2_, c0);
      cin = csel(g & 1, cfma(l16, cb, csel(wm, csel(g & 2, e[6], e[4]), csel(g & 2, e[2], e[0]))), cb);
    }
    pinc(cin);
    TS(4);
    SB();
    auto gload = [&]()
      { const u32 xo = (bid * 8 + 2 * w) * 2048, vl = lane * 16; const rsrc_t gr = mkr((const void*)kb_[3], 2048);
        gx[0] = bl4<0>(xr, vl, xo); gx[1] = bl4<1024>(xr, vl, xo); gx[2] = bl4<2048>(xr, vl, xo); gx[3] = bl4<3072>(xr, vl, xo); gw[0] = bl4<0>(gr, vl, 0); gw[1] = bl4<1024>(gr, vl, 0); };
    gload();
    SB();
    c2 ca, cz; u32x4 v[4];
    auto poll = [&]() {
#pragma unroll
      for (int n = 0; n < 4; ++n) v[n] = bl4c<0>(wsr, pa[n], 0);
    };
    {
#pragma unroll
      for (int s = 0; s < 8; ++s) {
        f32x2 t[4];
#pragma unroll
        for (int ps = 0; ps < 2; ++ps) { const c2 a = bc(coef, ps); t[2 * ps] = a.r * h[ps][s].r; t[2 * ps + 1] = a.i * h[ps][s].r; }
        SB();
#pragma unroll
        for (int ps = 0; ps < 2; ++ps) { const c2 a = bc(coef, ps); h[ps][s].r = fma2(-a.i, h[ps][s].i, t[2 * ps]); h[ps][s].i = fma2(-a.r, h[ps][s].i, -t[2 * ps + 1]); }
        SB();
      }
#pragma unroll
      for (int s = 0; s < 16; ++s) pinc(h[s & 1][s >> 1]);
      SB();
      poll();
      SB();
      ca = cmul(coef, cin); cz = cmul(coef, cfma(l8, cin, ea));
      pinc(ca); pinc(cz);
      const u32 hs = L + 65536 + w * 8192;
#pragma unroll
      for (int i = 0; i < 8; ++i) {
        wa[i] = hs + l15 * 128 + ((4 * g + 16 * i) ^ (8 * (l15 & 1) + 4 * (l15 & 4) + 16 * (l15 & 2) + 8 * (l15 & 8))); PIN(wa[i]);
        ra[i] = hs + (8 * g + q) * 128 + ((8 * pq) ^ (8 * (q & 1) + 16 * (q & 2) + 64 * (g & 1)) ^ (16 * i)); PIN(ra[i]);
      }
    }
    SB();
    pwf();
    SB();
    pwg();
    SB();
    auto gate = [&]()
    {
      const u32 vG = WS_G + (u32)(((2 * w + (lane & 1)) * 2 + (((bid >> 4) ^ (bid >> 5)) & 1)) * 128 + ((bid >> 5) & 1) * 64 + (bid & 15) * 4 + (bid >> 6)) * 4;
      float sm[2] = {0.f, 0.f};
      PIN(gw[0]); PIN(gw[1]);
#pragma unroll
      for (int i = 0; i < 4; ++i) {
        PIN(gx[i]);
#pragma unroll
        for (int e = 0; e < 4; ++e) { const u32 a = gx[i][e], b = gw[i & 1][e]; sm[i >> 1] = __builtin_amdgcn_fdot2_f32_bf16(__builtin_bit_cast(bf16x2, a), __builtin_bit_cast(bf16x2, b), sm[i >> 1], false); }
      }
      sm[0] = dppadd<0xB1>(sm[0]); sm[1] = dppadd<0xB1>(sm[1]);
      float t = (lane & 1) ? sm[1] : sm[0];
      t = dppadd<0x4E>(t); t = dppadd<0x124>(t); t = dppadd<0x128>(t); t = sum32(sum16(t));
      __builtin_amdgcn_raw_buffer_store_b32(__float_as_uint(rbf(1.f / (1.f + expf(-rbf(t))))), wsr, lane < 2 ? vG : WS_END, 0, 16);
    };
    WAITV(4); gate();
    SB();
    TS(5);
    WAITV(1);
    if (tt > 0) {
      auto vok = [&]() { u32 mx = 0;
#pragma unroll
        for (int n = 0; n < 4; ++n) { PIN(v[n]); mx = umax3(umax3(mx, v[n].x, v[n].y), v[n].z, v[n].w); }
        return __all(mx != SENT); };
      if (!vok()) {
        NOUNROLL for (u32 spn = 0; spn < SPIN_CAP; ++spn) {
#pragma unroll
          for (int n = 0; n < 4; ++n) v[n] = bl4c<0>(wsr, pa[n], 0);
          WAITV(0);
          if (vok()) break;
        }
      }
      c2 cb = cfma(wt[0], asc(__builtin_bit_cast(f32x4, v[0])), cmul(wt[1], asc(__builtin_bit_cast(f32x4, v[1]))));
      const c2 cd = cfma(wt[2], asc(__builtin_bit_cast(f32x4, v[2])), cmul(wt[3], asc(__builtin_bit_cast(f32x4, v[3]))));
      cb.r += cd.r; cb.i += cd.i;
      cb = c2{{sum32(sum16(cb.r.x)), sum32(sum16(cb.r.y))}, {sum32(sum16(cb.i.x)), sum32(sum16(cb.i.y))}};
      ca = cfma(lsa, cb, ca); cz = cfma(lsb, cb, cz);
    } else {
#pragma unroll
      for (int n = 0; n < 4; ++n) PIN(v[n]);
    }
    TS(6);
    {
      const c2 cc[2] = {{{ca.r.x, cz.r.x}, {ca.i.x, cz.i.x}}, {{ca.r.y, cz.r.y}, {ca.i.y, cz.i.y}}};
      u32x4 hq[8];
#pragma unroll
      for (int s = 0; s < 8; ++s) {
#pragma unroll
        for (int ps = 0; ps < 2; ++ps) { const c2 a = bc(pw[s], ps); h[ps][s].r = fma2(-a.i, cc[ps].i, h[ps][s].r); h[ps][s].i = fma2(-a.i, cc[ps].r, h[ps][s].i); }
        SB();
#pragma unroll
        for (int ps = 0; ps < 2; ++ps) { const c2 a = bc(pw[s], ps); h[ps][s].r = fma2(a.r, cc[ps].r, h[ps][s].r); h[ps][s].i = fma2(-a.r, cc[ps].i, h[ps][s].i); }
        SB();
#pragma unroll
        for (int ps = 0; ps < 2; ++ps) { lw<u32>(wa[s] + ps * 2048, pk2(h[ps][s].r)); lw<u32>(wa[s] + ps * 2048 + 4096, pk2(h[ps][s].i)); }
        FENCE();
        if (s & 1) {
          hq[s - 1] = __builtin_bit_cast(u32x4, cat(trr(ra[s - 1]), trr(ra[s] + 512))); hq[s] = __builtin_bit_cast(u32x4, cat(trr(ra[s - 1] + 4096), trr(ra[s] + 4608)));
          if (s > 1) { st4c(hq[s - 3], wsr, vH, WS_H + ((s - 3) >> 1) * (2u << 20)); st4c(hq[s - 2], wsr, vH, WS_H + ((s - 3) >> 1) * (2u << 20) + 32768); }
        }
        SB();
      }
      st4c(hq[6], wsr, vH, WS_H + 3 * (2u << 20)); st4c(hq[7], wsr, vH, WS_H + 3 * (2u << 20) + 32768);
    }
    TS(7);
    {
      bf16x8 cp[6][4];
      u32 ha = L + 98304 + wm * 8192 + lane * 16, ca[2][2];
#pragma unroll
      for (int i = 0; i < 4; ++i) { ca[i & 1][i >> 1] = L + (i >> 1) * 65536 + wn * 8192 + l15 * 128 + 16 * ((4 * (i & 1) + g) ^ (l15 >> 1)); PIN(ca[i & 1][i >> 1]); }
      PIN(ha);
      tag = lr<u64>(L + L_SYNC + 32); PIN(tag);
      WAITV(0);
      gbar(L + L_SYNC, w, 3);
      if (w == 0) st2c(tag, wsr, lane < NREP ? lane * 2048 : WS_END, WS_FH + (tt * 16 + pt) * 8);
      { u64 pc; u32 bk, nb; asm volatile("s_getpc_b64 %0\n.Lcode_m%=:\n\ts_mov_b32 %1, .Lcode_m%=-.Lcode_lo\n\ts_mov_b32 %2, .Lcode_hi-.Lcode_lo" : "=s"(pc), "=s"(bk), "=s"(nb));
        if (w == 1) touch(mkr((const void*)((pc - bk) & ~127ull), min(nb, 61440u)), (lane * 32 + jj) * 128, L); }
      TS(28);
      BAR();
#pragma unroll
      for (int i = 0; i < 24; ++i) cp[i >> 2][i & 3] = lr<bf16x8>(ca[(i >> 2) & 1][0] + (i >> 3) * 16384 + (i & 3) * 2048);
      WAITV(0); LBAR();
#pragma unroll
      for (int i = 0; i < 24; ++i) PIN(cp[i >> 2][i & 3]);
      TS(12);
      u64 cy0 = 0; CY0();
      {
        bf16x8 H[2][2][4], Cf[2][2][4];
        auto rd = [&](int u, int i) { const int ks = i & 1, f = (i >> 1) & 3;
          if (i < 8) { if (u >= 3) Cf[u & 1][ks][f] = lr<bf16x8>(ca[ks][(u % 6) >> 2] + ((u % 6) & 3) * 16384 + f * 2048); } else H[u & 1][ks][f] = lr<bf16x8>(ha + (u & 3) * 16384 + f * 2048 + ks * 1024); };
#pragma unroll
        for (int i = 8; i < 16; ++i) rd(0, i);
#pragma unroll
        for (int u = 0; u < 16 - SE; ++u) {
#pragma unroll
          for (int qq = 0; qq < 32; ++qq) {
            const int p = qq >> 1, m = p >> 2, n = (m & 1) ? 3 - (p & 3) : (p & 3), ks = (qq ^ p) & 1;
            if (qq == BQ && u < 15) LBAR();
            SB();
            const bf16x8 cf = u < 3 ? cp[(2 * u + ks) % 6][n] : Cf[u & 1][ks][n];
            if (u || (qq & 1)) mmc(acc[m][n], cf, H[u & 1][ks][m]); else mmz(acc[m][n], cf, H[u & 1][ks][m]);
            SB();
            if (u < 15 && qq >= BQ && qq < BQ + 16) rd(u + 1, qq - BQ);
          }
        }
        u32 xs = g * 256 + (l15 ^ (4 * g)) * 16; PIN(xs);
#pragma unroll
        for (int qq = 0; qq < 32 * SE; ++qq) {
          const int u = 16 - SE + ((qq >> 4) % SE), n0 = qq < 16 * SE ? 2 : 0, p = (qq & 15) >> 1, m = p >> 1, n = n0 + ((p ^ m) & 1), ks = (qq ^ p) & 1;
          if (SE == 2 && qq == 0) LBAR();
          SB();
          mmc(acc[m][n], Cf[u & 1][ks][n], H[u & 1][ks][m]);
          SB();
          if (SE == 2 && qq < 16) rd(15, qq < 4 ? 4 + qq : qq < 12 ? 4 + qq : qq - 12);
          if (qq >= 16 * SE + 2 && qq < 16 * SE + 14 && (qq - 16 * SE - 2) % 3 == 0) { const int mm = (qq - 16 * SE - 2) / 3; st4c(__builtin_bit_cast(u32x4, pk8(acc[mm][2], acc[mm][3])), wsr, xs, WS_X + (((bid ^ 1) * 4 + w) * 4 + mm) * 1024); }
        }
      }
      NOPS();
      CY1(21);
    }
    TS(13);
    {
      const u32 vl = lane * 16;
#pragma unroll
      for (int m = 0; m < 4; ++m) if (!SE) st4c(__builtin_bit_cast(u32x4, pk8(acc[m][2], acc[m][3])), wsr, g * 256 + (l15 ^ (4 * g)) * 16, WS_X + (((bid ^ 1) * 4 + w) * 4 + m) * 1024);
      gq[0] = bl1c(wsr, vGo, 0); gq[1] = bl1c(wsr, vGo + 128, 0);
      BAR();
      WAITV(2);
      st2c(tag, wsr, lane == 0 ? 0 : WS_END, WS_FP + ((bid ^ 1) * 4 + w) * 128);
      TS(29);
      const u32 pa = L + 98304 + wm * 16384 + wn * 2048 + g * 256 + (l15 ^ (4 * g)) * 16;
#pragma unroll
      for (int i = 0; i < 8; ++i) lw<f32x4>(pa + (i >> 1) * 4096 + (i & 1) * 1024, acc[i >> 1][i & 1]);
      NOUNROLL for (u32 spn = 0; spn < SPIN_CAP; ++spn) {
        u64 a; asm volatile("buffer_load_dwordx2 %0, off, %1, %2 sc1\n\ts_waitcnt vmcnt(0)" : "=&v"(a) : "s"(wsr), "s"(WS_FP + (bid * 4 + w) * 128) : "memory");
        if (__all(a == tag)) break;
      }
#pragma unroll
      for (int m = 0; m < 4; ++m) bldsc(wsr, vl, WS_X + (bid * 4 + w) * 4096 + m * 1024, L + w * 4096, 131072 + m * 1024);
      PIN(gq[0]); PIN(gq[1]);
      TS(31);
    }
  }
  if (!rh) TS(14);
  {
    const rsrc_t outr = mkr((const void*)outp, 8u << 20);
    u32 pa[2], xa[2], ba[4], oa[2]; float og[2];
#pragma unroll
    for (int hi = 0; hi < 2; ++hi) {
      const int row = lr8 + 8 * hi, s1 = 32 * wv + row + 16 * (sh ^ (w & 1));
      pa[hi] = L + 98304 + wv * 4096 + lc8 * 256 + (row ^ (4 * pq)) * 16; oa[hi] = (u32)(s2 + 8 * s1) * 4096u + lc8 * 16; og[hi] = rbf(1.f - __uint_as_float(gq[hi]));
      ba[2 * hi] = L + 65536 + (wv * 16 + row) * 256 + 16 * (lc8 ^ row); ba[2 * hi + 1] = ba[2 * hi] ^ 128;
      xa[hi] = L + 131072 + rh * 8192 + w * 1024 + pq * 256 + (row ^ (4 * pq)) * 16 + (lc8 >> 2) * 8; PIN(xa[hi]);
      PIN(pa[hi]); PIN(oa[hi]); PIN(og[hi]); PIN(ba[2 * hi]); PIN(ba[2 * hi + 1]);
    }
    WAITV(0); LBAR();
    f32x4 v[4][2]; u32x2 xv[4];
#pragma unroll
    for (int i = 0; i < 4; ++i) {
      v[i][0] = lr<f32x4>(pa[i >> 1] + (i & 1) * 2048); v[i][1] = lr<f32x4>(ba[i]); xv[i] = lr<u32x2>(xa[i >> 1] + (i & 1) * 4096);
    }
    SB();
#pragma unroll
    for (int i = 0; i < 4; ++i)
      st4(__builtin_bit_cast(u32x4, og[i >> 1] * (v[i][0] + f32x4{bf2f(xv[i].x & 0xffffu), bfhi(xv[i].x), bf2f(xv[i].y & 0xffffu), bfhi(xv[i].y)}) + v[i][1]), outr, oa[i >> 1], 128 * ct + 2048 * (i & 1));
  }
  if (!rh) TS(15);
}

extern "C" void kernel_launch(void* const* d_in, const int* in_sizes, int n_in, void* d_out, int out_size, void* d_ws, size_t ws_size, hipStream_t stream) {
  (void)in_sizes; (void)n_in; (void)out_size; (void)ws_size;
  (void)hipFuncSetAttribute((const void*)k_all, hipFuncAttributeMaxDynamicSharedMemorySize, L_TOT);
  const u16* const* in = (const u16* const*)d_in;
  k_all<<<256, 512, L_TOT, stream>>>(in[0], in[6], in[7], in[10], in[4], in[5], in[1], in[2], in[8], in[9], in[11], in[12], (char*)d_ws, (float*)d_out);
}
```

```cpp
#include <hip/hip_runtime.h>
#include <hip/hip_bf16.h>
#include <stdint.h>

typedef unsigned short u16;
typedef unsigned int u32;
typedef unsigned long long u64;
typedef __attribute__((ext_vector_type(4))) short s16x4;
typedef __attribute__((ext_vector_type(8))) __bf16 bf16x8;
typedef __attribute__((ext_vector_type(2))) __bf16 bf16x2;
typedef __attribute__((ext_vector_type(2))) float f32x2;
typedef __attribute__((ext_vector_type(4))) float f32x4;
typedef __attribute__((ext_vector_type(4))) u32 u32x4;
typedef __attribute__((ext_vector_type(2))) u32 u32x2;
#define DI __device__ __forceinline__
#define LDSP __attribute__((address_space(3)))

#define NREP 8
constexpr u32 WS_E = 0, WS_AGG = 65536, WS_FP = 0x40000, WS_FU = 0x60000, WS_FH = WS_FU + NREP * 2048, WS_FT = WS_FH + NREP * 2048, WS_G = WS_FT + NREP * 2048;
constexpr u32 WS_U = 1u << 20, WS_T = 5u << 20, WS_H = 9u << 20, WS_X = 17u << 20, WS_END = 21u << 20;
constexpr u32 L_RA = 65536, L_W = 98304, L_WS = 17408, L_V = 133120, L_CH = 5264, L_PS = 144, L_SYNC = 154176, L_SEG = 154688, L_TOT = 163840;
constexpr u32 SPIN_CAP = 1u << 18, SENT = 0xffffffffu;

DI float bf2f(u32 h) { return __builtin_bit_cast(float, h << 16); }
DI float bfhi(u32 h) { return __builtin_bit_cast(float, h & 0xffff0000u); }
DI u32 pk2(float a, float b) { f32x2 v = {a, b}; return __builtin_bit_cast(u32, __builtin_convertvector(v, bf16x2)); }
DI u32 pk2(f32x2 v) { return __builtin_bit_cast(u32, __builtin_convertvector(v, bf16x2)); }
DI float rbf(float a) { return bf2f(pk2(a, a) & 0xffffu); }
typedef __amdgpu_buffer_rsrc_t rsrc_t;
DI rsrc_t mkr(const void* p, u32 bytes) { return __builtin_amdgcn_make_buffer_rsrc((void*)p, 0, bytes, 0x00020000); }
DI void bldsc(rsrc_t r, u32 vo, u32 so, u32 l, u32 lo) { asm volatile("s_add_u32 m0, %3, %4\n\ts_nop 0\n\tbuffer_load_dwordx4 %0, %1, %2 offen sc1 lds" :: "v"(vo), "s"(r), "s"(so), "s"(l), "i"(lo) : "memory", "scc"); }
DI void blds(rsrc_t r, u32 vo, u32 so, u32 l, u32 lo) { asm volatile("s_add_u32 m0, %3, %4\n\ts_nop 0\n\tbuffer_load_dwordx4 %0, %1, %2 offen lds" :: "v"(vo), "s"(r), "s"(so), "s"(l), "i"(lo) : "memory", "scc"); }
template <class T> DI T lr(u32 a) { return *(const LDSP T*)(uintptr_t)a; }
template <class T> DI void lw(u32 a, T v) { *(LDSP T*)(uintptr_t)a = v; }
DI u32x2 trr(u32 a) { return __builtin_bit_cast(u32x2, __builtin_amdgcn_ds_read_tr16_b64_v4i16((LDSP s16x4*)(uintptr_t)a)); }
DI bf16x8 cat(u32x2 a, u32x2 b) { u32x4 v = {a.x, a.y, b.x, b.y}; return __builtin_bit_cast(bf16x8, v); }
DI bf16x8 pk8(f32x4 a, f32x4 b) { u32x4 v = {pk2(a[0], a[1]), pk2(a[2], a[3]), pk2(b[0], b[1]), pk2(b[2], b[3])}; return __builtin_bit_cast(bf16x8, v); }
DI f32x4 mma(bf16x8 a, bf16x8 b, f32x4 c) { return __builtin_amdgcn_mfma_f32_16x16x32_bf16(a, b, c, 0, 0, 0); }
DI void mmz(f32x4& c, bf16x8 a, bf16x8 b) { asm volatile("v_mfma_f32_16x16x32_bf16 %0, %1, %2, 0" : "=&v"(c) : "v"(a), "v"(b)); }
DI void mmc(f32x4& c, bf16x8 a, bf16x8 b) { asm volatile("v_mfma_f32_16x16x32_bf16 %0, %1, %2, %0" : "+v"(c) : "v"(a), "v"(b)); }
DI bf16x8 asf(u32x4 v) { return __builtin_bit_cast(bf16x8, v); }
DI float cosr(float rev) { return __builtin_amdgcn_cosf(rev); }
DI float sinr(float rev) { return __builtin_amdgcn_sinf(rev); }
template <int O> DI u32x4 bl4(rsrc_t r, u32 vo, u32 so) { u32x4 v; asm volatile("buffer_load_dwordx4 %0, %1, %2, %3 offen offset:%4" : "=v"(v) : "v"(vo), "s"(r), "s"(so), "n"(O) : "memory"); return v; }
template <int O> DI u32x4 bl4c(rsrc_t r, u32 vo, u32 so) { u32x4 v; asm volatile("buffer_load_dwordx4 %0, %1, %2, %3 offen offset:%4 sc1" : "=v"(v) : "v"(vo), "s"(r), "s"(so), "n"(O) : "memory"); return v; }
DI u32 bl1c(rsrc_t r, u32 vo, u32 so) { u32 v; asm volatile("buffer_load_dword %0, %1, %2, %3 offen sc1" : "=v"(v) : "v"(vo), "s"(r), "s"(so) : "memory"); return v; }
DI void touch(rsrc_t r, u32 vo, u32 l) { asm volatile("s_add_u32 m0, %2, %3\n\ts_nop 0\n\tbuffer_load_dword %0, %1, 0 offen sc1 lds" :: "v"(vo), "s"(r), "s"(l), "i"(L_SYNC + 256) : "memory", "scc"); }
DI u64 bl2c(rsrc_t r) { u64 v; asm volatile("buffer_load_dwordx2 %0, off, %1, 0 sc1" : "=v"(v) : "s"(r) : "memory"); return v; }
DI u32 bl1(rsrc_t r, u32 vo, u32 so) { u32 v; asm volatile("buffer_load_dword %0, %1, %2, %3 offen" : "=v"(v) : "v"(vo), "s"(r), "s"(so) : "memory"); return v; }
DI void blhl(rsrc_t r, u32 vo, u32 l, u32 lo) { asm volatile("s_add_u32 m0, %2, %3\n\ts_nop 0\n\tbuffer_load_ushort %0, %1, 0 offen lds" :: "v"(vo), "s"(r), "s"(l), "i"(lo) : "memory", "scc"); }
DI void st4c(u32x4 v, rsrc_t r, u32 vo, u32 so) { asm volatile("buffer_store_dwordx4 %0, %1, %2, %3 offen sc1\n\ts_nop 1" :: "v"(v), "v"(vo), "s"(r), "s"(so) : "memory"); }
DI void st2c(u64 v, rsrc_t r, u32 vo, u32 so) { asm volatile("buffer_store_dwordx2 %0, %1, %2, %3 offen sc1" :: "v"(v), "v"(vo), "s"(r), "s"(so) : "memory"); }
DI void st4(u32x4 v, rsrc_t r, u32 vo, u32 so) { asm volatile("buffer_store_dwordx4 %0, %1, %2, %3 offen nt\n\ts_nop 1" :: "v"(v), "v"(vo), "s"(r), "s"(so) : "memory"); }
#define POLLSLP 2
#define NOUNROLL _Pragma("unroll 1")
#define SB() __builtin_amdgcn_sched_barrier(0)
template <class T> DI void PINS(T& v) { asm volatile("" : "+s"(v)); }
template <class T> DI void PIN(T& v) { asm volatile("" : "+v"(v)); }
#define NOPS() do { SB(); asm volatile("s_nop 7\n\ts_nop 1"); SB(); } while (0)
#define WAITV(n) asm volatile("s_waitcnt vmcnt(%0)" :: "n"(n) : "memory")
#define WAITL(n) asm volatile("s_waitcnt lgkmcnt(%0)" :: "n"(n) : "memory")
#define FENCE() asm volatile("" ::: "memory")
#define BAR() do { FENCE(); __builtin_amdgcn_s_barrier(); FENCE(); } while (0)
#define LBAR() do { WAITL(0); __builtin_amdgcn_s_barrier(); FENCE(); } while (0)
DI u32 umax3(u32 a, u32 b, u32 c) { return max(max(a, b), c); }
DI void gwait(u32 words, u32 phase) {
  NOUNROLL for (u32 s = 0; s < 16 * SPIN_CAP; ++s) {
    u32x4 v; asm volatile("ds_read_b128 %0, %1\n\ts_waitcnt lgkmcnt(0)" : "=v"(v) : "v"(words) : "memory");
    if (__builtin_amdgcn_readfirstlane(min(min(v.x, v.y), min(v.z, v.w))) >= phase) break;
  }
  FENCE();
}
DI void gpost(u32 words, int wq, u32 phase) { FENCE(); lw<u32>(words + 4 * wq, phase); FENCE(); }
DI void gbar(u32 words, int wq, u32 phase) { gpost(words, wq, phase); gwait(words, phase); }
DI void pollq(rsrc_t wr, u32 off, int lane, u64 tag) {
  NOUNROLL for (u32 s = 0; s < SPIN_CAP; ++s) {
    u64 a; asm volatile("buffer_load_dwordx2 %0, %1, %2, %3 offen sc1\n\ts_waitcnt vmcnt(0)" : "=&v"(a) : "v"(lane * 8), "s"(wr), "s"(off) : "memory");
    if (__all(a == tag)) break;
    __builtin_amdgcn_s_sleep(POLLSLP);
  }
}
#define TSW(n, t) do {} while (0)
#define CY0()
#define CY1(n)
#define ITS(p, j) do {} while (0)
#define TS(n) TSW(n, 0)
#define TX(n) TSW(n, 256)
#define NC0 1
#define NW13 4
#define FMASK 63u
#define FTU 0
#define JF 3
struct c2 { f32x2 r, i; };
DI f32x2 fma2(f32x2 a, f32x2 b, f32x2 c) { return __builtin_elementwise_fma(a, b, c); }
DI c2 cfma(c2 a, c2 b, c2 c) { return {fma2(a.r, b.r, fma2(-a.i, b.i, c.r)), fma2(a.r, b.i, fma2(a.i, b.r, c.i))}; }
DI c2 cmul(c2 a, c2 b) { return {fma2(-a.i, b.i, a.r * b.r), fma2(a.r, b.i, a.i * b.r)}; }
DI c2 csq(c2 a) { return {fma2(a.r, a.r, -a.i * a.i), (a.r + a.r) * a.i}; }
DI c2 bc(c2 a, int ps) { return ps ? c2{a.r.yy, a.i.yy} : c2{a.r.xx, a.i.xx}; }
DI c2 csel(bool c, c2 a, c2 b) { return {c ? a.r : b.r, c ? a.i : b.i}; }
DI c2 asc(f32x4 v) { return {v.xy, v.zw}; }
DI void pinc(c2& a) { PIN(a.r); PIN(a.i); }
DI f32x2 spread16(float v) { const u32 u = __float_as_uint(v); const auto e = __builtin_amdgcn_permlane16_swap(u, u, false, false); return f32x2{__uint_as_float(e[0]), __uint_as_float(e[1])}; }
DI float sum16(float v) { const f32x2 e = spread16(v); return e.x + e.y; }
DI float sum32(float v) { const u32 u = __float_as_uint(v); const auto e = __builtin_amdgcn_permlane32_swap(u, u, false, false); return __uint_as_float(e[0]) + __uint_as_float(e[1]); }
template <int CTRL> DI float dppadd(float v) { return v + __int_as_float(__builtin_amdgcn_mov_dpp(__float_as_int(v), CTRL, 0xf, 0xf, true)); }

DI u32x4 cos8(float a0, float kk, float c2d) {
  float c[8]; c[0] = cosr(a0); c[1] = cosr(fmaf(kk, 4.f, a0));
#pragma unroll
  for (int j = 2; j < 8; ++j) c[j] = fmaf(c2d, c[j - 1], -c[j - 2]);
  return u32x4{pk2(c[0], c[1]), pk2(c[2], c[3]), pk2(c[4], c[5]), pk2(c[6], c[7])};
}
DI void hart_table(u32x4 (&tab)[2][4], int kap, bool sp, int g) {
  const float kp = (float)kap * (1.f / 256.f), km = sp ? 0.125f : kp, cp = 2.f * cosr(4.f * kp), cm = sp ? -2.f : cp;
#pragma unroll
  for (int r = 0; r < 4; ++r) {
    const float kk = r == 1 && sp ? km : -km, fr_ = (float)(32 * g + r);
    tab[0][r] = cos8(fmaf(kp, fr_, -0.125f), kp, cp); tab[1][r] = cos8(fmaf(kk, fr_, -0.125f), kk, cm);
  }
}
template <bool INV> DI void hart256(f32x2 (&o)[8][2], u32 ap, const u32x4 (&tab)[2][4], int kap, bool sp) {
  const float sg1 = (kap & 1) ? -1.f : 1.f, al1 = sp ? 0.f : 1.f;
  const f32x2 sgn = {sg1, sg1}, al = {al1, al1};
  bf16x8 a[4][2]; f32x4 m[2][4][2];
#pragma unroll
  for (int r = 0; r < 4; ++r) { a[r][0] = cat(trr(ap + r * 8192), trr(ap + r * 8192 + 512)); a[r][1] = cat(trr(ap + r * 8192 + 4096), trr(ap + r * 8192 + 4608)); }
  SB();
#pragma unroll
  for (int r = 0; r < 4; ++r)
#pragma unroll
    for (int i = 0; i < 4; ++i) m[i >> 1][r][i & 1] = mma(a[r][i & 1], asf(tab[(i >> 1) ^ (INV & i & 1)][r]), f32x4{});
  SB();
#pragma unroll
  for (int hh = 0; hh < 2; ++hh) {
    f32x2 p[2][4];
#pragma unroll
    for (int i = 0; i < 8; ++i) { const f32x2 sg_ = INV && i >= 4 ? -sgn : sgn; p[i >> 2][i & 3] = hh ? fma2(sg_, m[i >> 2][i & 3][1].zw, m[i >> 2][i & 3][0].zw) : fma2(sg_, m[i >> 2][i & 3][1].xy, m[i >> 2][i & 3][0].xy); }
    const f32x2 s0 = p[0][1] + p[0][3], d0 = p[0][1] - p[0][3], s1 = INV ? p[1][1] + p[1][3] : fma2(al, p[1][1], p[1][3]), d1 = INV ? p[1][1] - p[1][3] : fma2(-al, p[1][3], p[1][1]);
    const f32x2 x0 = sp ? d0 : d1, x1 = sp ? d1 : d0;
#pragma unroll
    for (int sg = 0; sg < 2; ++sg) {
      const f32x2 A = p[sg][0] + p[sg][2], B = p[sg][0] - p[sg][2], S = sg ? s1 : s0, X = sg ? x1 : x0;
      o[4 * sg][hh] = A + S; o[4 * sg + 2][hh] = A - S; o[4 * sg + 1][hh] = B + X; o[4 * sg + 3][hh] = B - X;
    }
  }
  SB();
#pragma unroll
  for (int i = 0; i < 8; ++i) {
    const auto e0 = __builtin_amdgcn_permlane32_swap(__float_as_uint(o[i][0].x), __float_as_uint(o[i][1].x), false, false);
    const auto e1 = __builtin_amdgcn_permlane32_swap(__float_as_uint(o[i][0].y), __float_as_uint(o[i][1].y), false, false);
    o[i][0] = f32x2{__uint_as_float(e0[0]), __uint_as_float(e1[0])}; o[i][1] = f32x2{__uint_as_float(e0[1]), __uint_as_float(e1[1])};
  }
  SB();
#pragma unroll
  for (int i = 0; i < 8; ++i) { const f32x2 a_ = o[i][0], b_ = o[i][1]; o[i][0] = a_ + b_; o[i][1] = a_ - b_; }
}

struct TileD { int k, c, src, w; };
DI TileD tile_desc(int tau, int k1, int kd1) {
  TileD t; const int i = tau >> 1, ka = tau & 1, hi = i >= 5;
  t.c = kd1 + 2 * ka;
  t.src = k1 ? -1 : (kd1 ? ka : hi);
  t.k = 256 * i + (k1 ? k1 : (kd1 ? 128 * ka : -1152 * hi));
  t.w = k1 ? 1 : (kd1 ? 2 : (tau >= 18 ? 0 : ((i & 3) == 0 && i < 8 ? 1 : 2)));
  return t;
}
DI void m0_fwd(bf16x8& o, int n, int l15, int g, int k1, int kd1) {
  const TileD t = tile_desc(8 * n + (l15 >> 1), k1, kd1); f32x4 v[2];
  const float ak = (float)t.k * (-1.f / 2048.f), a0 = fmaf(ak, (float)(2 * g), -0.25f * (float)(l15 & 1)), ac = -0.25f * (float)t.c;
#pragma unroll
  for (int j = 0; j < 8; ++j) {
    const int rs = j & 1;
    const float c = cosr(a0 + ((j & 2) ? ak : 0.f) + ((j & 4) ? ac : 0.f) + (t.src < 0 ? (rs ? 0.125f : -0.125f) : 0.f));
    v[j >> 2][j & 3] = (t.src >= 0 && (t.w == 0 || t.src != rs)) ? 0.f : c;
  }
  o = pk8(v[0], v[1]);
}
DI void tr2(bf16x8& X, bf16x8& Y, bf16x8 A, bf16x8 B, u32 wr, u32 rd) {
  const u32x4 a = __builtin_bit_cast(u32x4, A), b = __builtin_bit_cast(u32x4, B);
  FENCE(); lw<u32x2>(wr, a.xy); lw<u32x2>(wr + 512, b.xy); lw<u32x2>(wr + 1024, a.zw); lw<u32x2>(wr + 1536, b.zw); FENCE();
  X = cat(trr(rd), trr(rd + 512)); Y = cat(trr(rd + 1024), trr(rd + 1536)); FENCE();
}

template <int O> DI void ldhi(float& v, u32 a) { asm volatile("ds_read_u16_d16_hi %0, %1 offset:%2" : "+v"(v) : "v"(a), "n"(O) : "memory"); }
#define BQ 12
#define LEAD 2
constexpr int g1_wait(int j) { int c = j <= 1 ? 4 : 0; for (int u = j - LEAD + 1; u < j; ++u) c += (u + LEAD < 16 ? 8 : 0) + (u == 15 - 2 * LEAD ? 1 : 0); return c; }
#define SE 2
#define TU 12
#define NE 2
constexpr int g2_c0(int u) { return u >= 13 ? 64 : 16 + 4 * NE + u * (48 - 4 * NE) / 13; }
constexpr bool g2_ok() { for (int u = 0; u < 13; ++u) for (int e = g2_c0(u); e < g2_c0(u + 1); ++e) if (u > e / 4 - 3 || (e / 4 >= 9 && u < e / 4 - 5)) return false; return true; }
static_assert(g2_ok(), "C tile schedule");
constexpr int g2_pre(int u) { return u < 0 ? 0 : g2_c0(u + 1) - g2_c0(u) + (u == 0 || u == 3 ? 1 : 0); }
constexpr int g2_post(int u) { return (u >= TU && u < TU + 4 ? 3 : 0) + (u == TU - 5 ? 1 : 0); }
constexpr int g2_wait(int u) { int c = g2_post(u - 3); for (int v = u - 2; v < u; ++v) c += g2_pre(v) + (v + 3 < 16 ? 4 : 0) + g2_post(v); return c; }
template <int N> struct IC { static constexpr int v = N; };
template <int I, int N, class F> DI void sfor(F&& f) { if constexpr (I < N) { f(IC<I>{}); sfor<I + 1, N>(f); } }

typedef __attribute__((ext_vector_type(8))) u64 u64x8;
typedef __attribute__((ext_vector_type(4))) u64 u64x4;
typedef __attribute__((ext_vector_type(2))) u64 u64x2;
__global__ __launch_bounds__(512, 2) void k_all(const u16*, const u16*, const u16*, const u16*, const u16*, const u16*, const u16*, const u16*,
    const u16*, const u16*, const u16*, const u16*, char*, float*) {
#define GETARGS() u64x8 ka; u64x4 kb_; u64x2 kc; \
  asm volatile("s_load_dwordx16 %0, %3, 0x0\n\ts_load_dwordx8 %1, %3, 0x40\n\ts_load_dwordx4 %2, %3, 0x60" : "=&s"(ka), "=&s"(kb_), "=&s"(kc) : "s"(__builtin_amdgcn_kernarg_segment_ptr()) : "memory")
  extern __shared__ __attribute__((aligned(16))) char lds_[];
  const u32 L = (u32)(uintptr_t)(LDSP char*)lds_;
  const int lane = threadIdx.x & 63, wv = __builtin_amdgcn_readfirstlane(threadIdx.x >> 6), w = wv & 3, rh = wv >> 2, l15 = lane & 15, g = lane >> 4;
  const int bid = blockIdx.x, xcd = bid & 7, jj = bid >> 3, ti = jj & 3, pj = jj >> 2;
  const int tt = (xcd >> 1) * 4 + ti, pt = (xcd & 1) * 8 + pj;
  const int s2 = tt >> 1, sh = tt & 1, ct = pt;
  const int lr8 = lane >> 3, lc8 = lane & 7, q = l15 >> 2, pq = lane & 3;
  int rot = tt;
  u32 gq[2]; u64 outp, wsp;
#define ARGS() asm volatile("s_waitcnt lgkmcnt(0)" : "+s"(ka), "+s"(kb_), "+s"(kc) :: "memory"); \
  char* ws = (char*)kc[0]; outp = kc[1]; wsp = kc[0]; \
  const rsrc_t xr = mkr((const void*)ka[0], 4u << 20), wsr = mkr(ws, WS_END), cr = mkr((const void*)((ct & 8) ? kb_[1] : kb_[0]), 2u << 20); \
  auto issue_a = [&](int rr, int hf) { const int r = (rr + r0) & 3; blds(xr, vA, (8 * r + 1024 * hf) * 2048, L + r * 8192 + hf * 4096 + w * 1024, L_RA); }; \
  u32 vC[2]; vC[0] = (u32)(32 * (ct ^ ((w & 1) * 8)) + 512 * (w >> 1) + lr8) * 2048u + 16 * (lc8 ^ (lr8 >> 1)); vC[1] = vC[0] ^ 64; \
  auto c_piece = [&](int i, int e) { blds(cr, vC[e & 1], e * 8 * 2048 + ((i + rot) & 15) * 128, L + w * 4096, (i % 6) * 16384 + e * 1024); }; \
  const u32 vGo = WS_G + tt * 512 + (u32)((w & 1) * 64 + lr8 * 4 + 2 * rh + (w >> 1)) * 4

  if (rh) {
    GETARGS();
    {
      u64 pc; u32 nb; asm volatile("s_getpc_b64 %0\n.Lcode_lo:\n\ts_mov_b32 %1, .Lcode_hi-.Lcode_lo\n\t.subsection 1\n.Lcode_hi:\n\t.subsection 0" : "=s"(pc), "=s"(nb));
      if (w == 3) touch(mkr((const void*)(pc & ~127ull), min(nb, 61440u)), (lane * 32 + jj) * 128, L);
    }
    const int ii = 8 * (w & 1) + lr8, swz = 16 * (lc8 ^ ((ii >> 1) & 7)), ac = lc8 ^ (((lr8 >> 1) & 1) | ((w & 1) << 1)), r0 = 2 * sh + (ct & 1);
    u32 vB = (u32)(64 * pt + ii) * 2048u + swz, vA = (u32)(s2 + 32 * (8 * w + lr8)) * 2048u + 64 * ct + 16 * (ac & 3) + 1024 * (ac >> 2);
    PIN(vA); PIN(vB);
    lw<u32>(L + L_SYNC + 4 * wv, 0u);
    ARGS();
    const rsrc_t br = mkr((const void*)((w & 2) ? ka[2] : ka[1]), 2u << 20);
    auto issue_b1 = [&](int j, int i) { blds(br, vB, 16 * i * 2048 + ((j + rot) & 15) * 128, L + w * 1024, ((j + 3) & 3) * 32768 + 16384 + i * 4096); };
    const int kap = 16 * sh + l15, ecol = 8 * w + 4 * (g & 1) + 2 * (g >> 1);
    const u32 vl = lane * 16;
    u64 tag; u32 q_ds[2];
    auto smalls = [&]() {
    tag = bl2c(wsr);
    if (w == 0) st4c(u32x4{SENT, SENT, SENT, SENT}, wsr, lane < 32 ? vl : WS_END, WS_AGG + tt * 8192 + pt * 512);
    if (w == 2) touch(mkr(ws + WS_FP, WS_G + 8192 - WS_FP), (lane * 256 + bid) * 128, L);
    if (w & 1) touch(mkr((const void*)kb_[3], 2048), ((jj & 15) + 16 * lane) * 128, L);
    q_ds[0] = bl1(mkr((const void*)kb_[2], 2048), 2 * ecol, 64 * ct); q_ds[1] = bl1(mkr((const void*)kb_[2], 2048), 2 * ecol, 64 * ct + 1024);
    };
    auto xpars = [&]() { u64 p3 = ka[3], p4 = ka[4], p5 = ka[5]; PINS(p3); PINS(p4); PINS(p5); const u64 pa_ = (w & 1) ? p4 : p3; if (w < 3) blhl(mkr((const void*)((w & 2) ? p5 : pa_), 2048), (64 * pt + lane) * 2, L + w * 256, L_V + 16384); };
    xpars();
    issue_a(0, 0); issue_a(0, 1);
    issue_a(1, 0); issue_a(1, 1);
    SB();
    const u32 vX = (u32)(128 * tt + 16 * (ii >> 2) + 8 * (ii & 1) + 2 * (w >> 1) + ((ii >> 1) & 1)) * 2048u + swz;
    auto issue_x1 = [&](int j, int i) { blds(xr, vX, (64 * (i >> 1) + 4 * (i & 1)) * 2048 + ((j + rot) & 15) * 128, L + w * 1024, ((j + 3) & 3) * 32768 + i * 4096); };
    const rsrc_t w0 = mkr((const void*)ka[6], 2048 * 1026), w1 = mkr((const void*)ka[7], 2048 * 1026);
    const u32 rep = (bid & (NREP - 1)) * 2048;
    TX(16);
    const int k1 = xcd * 16 + (jj >> 1), kd1 = jj & 1, cc = kd1 + 2 * (w & 1);
    const bool sp0 = (k1 == 0 && kd1 == 0);
    bf16x8 m0[2], mi[2], m0x, mix[2], fa_r, fa_i, fb_r, fb_i, gb_r, gb_i, ga_r, ga_i; f32x4 t1c, t1s, t2c, t2s;
    u32 vW[5], wa[5], wo[5]; float fr[4][4], fi[4][4], er[4], ei[4];
    const float fl = (float)l15, fg = (float)(4 * g), fc = (float)cc;
    const u32 tw = L + L_SEG + w * 2048 + l15 * 32 + 8 * g, tr = L + L_SEG + w * 2048 + (4 * g + q) * 32 + 8 * pq;
    SB();
    {
      const float st = fmaf(fl, -4.f / 64.f, fc * (-1.f / 64.f));
#pragma unroll
      for (int e = 0; e < 4; ++e) { const float a = (fg + (float)e) * st; t2c[e] = cosr(a); t2s[e] = sinr(a); t1c[e] = cosr(a * (1.f / 16.f)); t1s[e] = sinr(a * (1.f / 16.f)); }
      fa_r = pk8(t2c, -t2s); fa_i = pk8(t2s, t2c); PIN(t1c); PIN(t1s);
      PIN(fa_r); PIN(fa_i);
      const float fm = (float)(l15 < 8 ? l15 : l15 ^ 3) * (-1.f / 16.f); const u32 sl = l15 < 8 ? 0u : 0x80008000u;
#pragma unroll
      for (int e = 0; e < 4; ++e) { const float a = (fg + (float)e) * fm; t2c[e] = cosr(a); t2s[e] = sinr(a); }
      fb_r = pk8(t2c, -t2s); fb_i = asf(__builtin_bit_cast(u32x4, pk8(t2s, t2c)) ^ sl);
      PIN(fb_r); PIN(fb_i);
    }
    u32 xa[2], ua, us, us0, fo, ap0, wp0, tr_, tw_;
    const u32 vo = L + L_V + w * L_CH;
    int wid, wsl; f32x4 uc, us_;
    auto fill = [&](auto k_) {
      constexpr int k = decltype(k_)::v;
      SB();
      if constexpr (k == -4) {
        const float mk = (float)(k1 + 256 * (l15 >> 2)) * (-1.f / 2048.f), mc = -0.25f * (float)(kd1 + (l15 & 2)), ma = fmaf(mk, 0.5f * fg, fmaf((float)(l15 & 1), -0.25f, -0.125f));
        u32x4 f;
#pragma unroll
        for (int e = 0; e < 4; ++e) { const float a = ma + ((e & 1) ? mk : 0.f) + ((e & 2) ? mc : 0.f); f[e] = pk2(cosr(a), -sinr(a)); }
        m0[0] = asf(f); m0[1] = asf(f ^ u32x4{0u, 0x80008000u, 0u, 0x80008000u});
        const float b = fmaf((float)(l15 >> 1), fmaf(fg, -1.f / 32.f, (float)k1 * (-1.f / 2048.f)), fmaf((float)(l15 & 1), 0.75f, fmaf((float)k1, 1.f / 512.f, -0.125f))), b1 = b - 0.25f * (float)kd1;
        const u32 p0 = pk2(cosr(b), sinr(b)), p1 = pk2(cosr(b1), sinr(b1)), sgn = (l15 & 2) ? 0x80008000u : 0u;
        mi[0] = asf(u32x4{p0, p0, p0 ^ sgn, p0 ^ sgn}); mi[1] = asf(u32x4{p1, p1 ^ 0x80008000u, p1 ^ sgn, p1 ^ sgn ^ 0x80008000u});
        if (__builtin_expect(k1 == 0, 0)) { m0_fwd(m0[0], 0, l15, g, 0, kd1); m0_fwd(m0[1], 1, l15, g, 0, kd1); m0_fwd(m0x, 2, l15, g, 0, 0); tr2(mix[0], mix[1], m0x, bf16x8{}, tw, tr); tr2(mi[0], mi[1], m0[0], m0[1], tw, tr); }
        PIN(m0[0]); PIN(m0[1]); PIN(mi[0]); PIN(mi[1]);
      }
      if constexpr (k == -2 || k == -1) { constexpr int i = k + 2; const int n = i ? 63 - kap : kap, rho = 64 * (n & 3) + (n >> 2); xa[i] = L + L_RA + rho * 128 + 16 * (w ^ (((rho >> 1) & 1) | ((rho >> 3) & 1) << 1)) + 2 * (ecol & 7); PIN(xa[i]); }
      if constexpr (k >= 0 && k < 4) {
        const float a = fl * fmaf(fg + (float)k, 4.f / 1024.f, fc * (1.f / 1024.f));
        t2c[k] = cosr(a) * (1.f / 1048576.f); t2s[k] = sinr(a) * (1.f / 1048576.f); uc[k] = cosr(16.f * a); us_[k] = sinr(16.f * a); PIN(t2c); PIN(t2s); PIN(uc); PIN(us_);
      }
      if constexpr (k == 4) { ga_r = pk8(uc, -us_); ga_i = pk8(us_, uc); PIN(ga_r); PIN(ga_i); }
      if constexpr (k >= 5 && k < 9) { const float a = (fg + (g < 2 ? (float)(k - 5) : (float)(8 - k))) * fl * (-1.f / 16.f); uc[k - 5] = cosr(a); us_[k - 5] = sinr(a); PIN(uc); PIN(us_); }
      if constexpr (k == 9) {
        const u32 sg = g < 2 ? 0u : 0x80008000u; const u32x4 t = __builtin_bit_cast(u32x4, pk8(uc, us_));
        gb_r = asf(u32x4{t.x, t.y, t.z ^ sg, t.w ^ sg}); gb_i = asf(u32x4{t.z ^ 0x80008000u, t.w ^ 0x80008000u, t.x ^ sg, t.y ^ sg}); PIN(gb_r); PIN(gb_i);
      }
      if constexpr (k == 12) { ua = L + L_V + (16 * w + g) * 256 + (lr8 & 1) * 128 + 16 * (lc8 ^ g); PIN(ua); }
      if constexpr (k == 13) { us = (u32)(64 * w + 2 * g + (lr8 & 1) + 32 * (sh ^ (w & 1))) * 16384u + lc8 * 16; PIN(us); }
      if constexpr (k == 14) { us0 = (w & 1) && g == 0 ? us ^ (32 * 16384) : us; PIN(us0); }
      if constexpr (k == 11) { fo = lane < NREP ? lane * 2048 + bid * 8 : WS_END; PIN(fo); }
      if constexpr (k == 33) { ap0 = vo + (4 * g + q) * 64 + 16 * pq; PIN(ap0); }
      if constexpr (k == 34) { wp0 = vo + l15 * L_PS + 16 * g; PIN(wp0); }
      if constexpr (k == 35) { tr_ = L + L_V + w * 2 * L_PS + g * L_CH + q * 32 + 8 * pq; PIN(tr_); }
      if constexpr (k == 36) { tw_ = L + L_V + w * 2 * L_PS + q * L_CH + pq * 32 + 8 * g; PIN(tw_); }
      if constexpr (k == 37) { PIN(vC[0]); PIN(vC[1]); }
      if constexpr (k == 40) { wid = 2 * cc + 8 * l15 + 512 * g; PIN(wid); }
      if constexpr (k == 41) { wa[0] = g < 2 ? L + L_W + (w >> 1) * 1056 + ((k1 * 1026) & 15) + wid : L + L_W + (8 + (w >> 1)) * 1056 + ((-k1 * 1026) & 15) + 1664 - wid; PIN(wa[0]); }
      if constexpr (k == 42) { wo[0] = g < 2 ? L + L_W + (8 + (w >> 1)) * 1056 + ((-k1 * 1026) & 15) : L + L_W + (w >> 1) * 1056 + ((k1 * 1026) & 15) + 1024; PIN(wo[0]); }
      if constexpr (k >= 43 && k < 46) { wa[k - 42] = wa[0] + 2112 * (k - 42); wo[k - 42] = wo[0] + 2112 * (k - 42); PIN(wa[k - 42]); PIN(wo[k - 42]); }
      if constexpr (k == 46) {
        if (__builtin_expect(k1 == 0, 0)) {
#pragma unroll
          for (int i = 0; i < 5; ++i) {
            const int kk = tile_desc(w + 4 * i, 0, kd1).k;
            const u32 wp = L + L_W + (kk >> 7) * 1056, wq = L + L_W + (((2048 - kk) & 2047) >> 7) * 1056;
            wa[i] = g < 2 ? wp + wid : wq + 1664 - wid; wo[i] = g < 2 ? wq : wp + 1024;
          }
        }
      }
      if constexpr (k >= 48 && k < 73) {
        constexpr int i = (k - 48) / 5, ph = (k - 48) % 5;
        if (ph == 0) { wid = 64 * (w + 4 * i) + lane; wsl = (wid * 993) >> 16; PIN(wid); PIN(wsl); }
        if (ph == 1) { wsl = min(wsl, 15); wid = 16 * wid - 1056 * wsl; PIN(wid); PIN(wsl); }
        if (ph == 2) { wsl = k1 + (k1 ? 256 : 128) * wsl; PIN(wsl); }
        if (ph == 3) { wsl = wsl < 2048 ? wsl : 4096 - wsl; PIN(wsl); }
        if (ph == 4) { vW[i] = ((wsl * 1026) & ~15) + wid; PIN(vW[i]); }
      }
      if constexpr (k >= 76 && k < 96) {
#pragma unroll
        for (int r = 2 * (k & 1); r < 2 * (k & 1) + 2; ++r) { constexpr int z_ = (k - 76) >> 1; float& z = z_ < 4 ? fr[z_ & 3][r] : z_ < 8 ? fi[z_ & 3][r] : z_ == 8 ? er[r] : ei[r]; z = 0.f; PIN(z); }
      }
      SB();
    };
    auto issue_t = [&](auto j_) {
      constexpr int j = decltype(j_)::v;
      sfor<0, 8>([&](auto i_) { constexpr int i = decltype(i_)::v; if (i < 4) issue_b1(j, i); else issue_x1(j, i - 4); if constexpr (j > 2) fill(IC<8 * j - 8 + i>{}); });
    };
    sfor<-4, 0>(fill);
    TX(17);
    WAITV(4); BAR();
    issue_a(2, 0); issue_a(2, 1); issue_a(3, 0); issue_a(3, 1); issue_b1(0, 0); issue_b1(0, 1); issue_b1(0, 2); issue_b1(0, 3);
    smalls();
    WAITV(8); LBAR();
    u32 sk[8][2];
#pragma unroll
    for (int i = 0; i < 8; ++i) { const u32 a = xa[i >> 2] + 2048 * (i < 4 ? i : (i + 3) & 3); sk[i][0] = lr<u32>(a); sk[i][1] = lr<u32>(a + 64); }
    SB();
#pragma unroll
    for (int i = 0; i < 4; ++i) issue_x1(0, i);
    issue_t(IC<1>{});
    sfor<11, 15>(fill); sfor<0, 11>(fill);
    {
      u32x4 uq[4];
      gwait(L + L_SYNC, 1);
#pragma unroll
      for (int i = 0; i < 4; ++i) uq[i] = lr<u32x4>((ua ^ (64 * (i & 1))) + 1024 * i);
#pragma unroll
      for (int i = 0; i < 4; ++i) st4c(uq[i], wsr, i == 0 ? us0 : us, WS_U + ct * 1024 + s2 * 128 + i * 8 * 16384);
    }
    if (LEAD == 3) issue_t(IC<2>{});
    TX(18);
    u64 fu;
    const f32x4 z4 = {};
    bf16x8 a0[2][2];
    sfor<0, 17 - LEAD>([&](auto j_) {
      constexpr int j = decltype(j_)::v;
      WAITV(g1_wait(j));
      if (j == 0) {
        PIN(tag); PIN(q_ds[0]); PIN(q_ds[1]);
        tag += 0x9e3779b97f4a7c15ull;
        if (w == 0) lw<u64>(L + L_SYNC + 32, tag);
        LBAR();
      } else BAR();
      ITS(1, j);
      PINS(rot);
      if (j == JF && w == 0) st2c(tag, wsr, fo, WS_FU);
      if (j == 15 - 2 * LEAD) asm volatile("buffer_load_dwordx2 %0, %1, %2, %3 offen sc1" : "=v"(fu) : "v"(lane * 8), "s"(wsr), "s"(WS_FU + rep + w * 512) : "memory");
      SB();
      if constexpr (j + LEAD < 16) issue_t(IC<j + LEAD>{});
      SB();
      if (j == 15 - LEAD) { PIN(fu); if (__builtin_expect(!__all(fu == tag), 0)) pollq(wsr, WS_FU + rep + w * 512, lane, tag); }
      SB();
    });
#pragma unroll
    for (int i = 0; i < 4; ++i) bldsc(wsr, vl, WS_U + (u32)(2 * k1 + kd1) * 16384u + w * 2048 + (i >> 1) * 8192 + (i & 1) * 1024, L + w * L_CH, L_V + i * 1024);
#pragma unroll
    for (int i = 0; i < NW13; ++i) blds(w0, vW[i], 0, L + w * 1024, L_W + i * 4096);
    WAITV(4 + NW13); BAR();
    ITS(1, 17 - LEAD);
    auto wissue = [&]() {
#pragma unroll
      for (int i = 0; i < 4; ++i) { if (i >= NW13) blds(w0, vW[i], 0, L + w * 1024, L_W + i * 4096); blds(w1, vW[i], 0, L + w * 1024, L_W + L_WS + i * 4096); }
      if (w == 0) { blds(w0, vW[4], 0, L, L_W + 16384); blds(w1, vW[4], 0, L, L_W + L_WS + 16384); }
#pragma unroll
      for (int e = 0; e < 4 * NC0; ++e) c_piece(e >> 2, e & 3);
    };
    {
      WAITV(NW13);
#pragma unroll
      for (int cb = 0; cb < 2; ++cb) { a0[cb][0] = cat(trr(ap0 + 1024 * cb), trr(ap0 + 1024 * cb + 2048)); a0[cb][1] = cat(trr(ap0 + 1024 * cb + 8), trr(ap0 + 1024 * cb + 2056)); }
      FENCE();
      wissue();
      f32x4 d[2][2][2];
#pragma unroll
      for (int i = 0; i < 8; ++i) d[i >> 2][(i >> 1) & 1][i & 1] = mma(a0[(i >> 1) & 1][i & 1], m0[i >> 2], z4);
#pragma unroll
      for (int i = 0; i < 4; ++i) lw<bf16x8>(wp0 + 16 * (i >> 1) * L_PS + 64 * (i & 1), pk8(d[i >> 1][i & 1][0], d[i >> 1][i & 1][1]));
      if (__builtin_expect(sp0, 0)) {
#pragma unroll
        for (int cb = 0; cb < 2; ++cb) { const bf16x8 o = pk8(mma(a0[cb][0], m0x, z4), mma(a0[cb][1], m0x, z4)); if (l15 < 4) lw<bf16x8>(wp0 + 32 * L_PS + 64 * cb, o); }
      }
      gbar(L + L_SYNC + 16, w, 3);
    }
    TX(8);
    auto tiles = [&](auto nt_, auto i0_) {
      constexpr int NT = decltype(nt_)::v, i0 = decltype(i0_)::v;
      const u32 pr = tr_ + i0 * 8 * L_PS, pw_ = tw_ + i0 * 8 * L_PS;
      bf16x8 a[NT]; f32x4 yr[NT], yi[NT];
#pragma unroll
      for (int i = 0; i < NT; ++i) a[i] = cat(trr(pr + i * 8 * L_PS), trr(pr + i * 8 * L_PS + L_PS));
      SB();
#pragma unroll
      for (int i = 0; i < NT; ++i) { yr[i] = mma(a[i], fa_r, z4); yi[i] = mma(a[i], fa_i, z4); }
      SB();
#pragma unroll
      for (int i = 0; i < NT; ++i) { a[i] = pk8(yr[i] * t1c - yi[i] * t1s, yr[i] * t1s + yi[i] * t1c); yr[i] = mma(fb_r, a[i], z4); yi[i] = mma(fb_i, a[i], z4); SB(); }
#pragma unroll
      for (int i = 0; i < NT; ++i) asm volatile("" :: "v"(yr[i]), "v"(yi[i]));
      if (!i0) {
        WAITV(4 * NC0); gbar(L + L_SYNC + 16, w, 4);
      }
      if (!i0) TX(9);
#pragma unroll
      for (int i = 0; i < NT; ++i) {
        if (i0) {
#pragma unroll
          for (int r = 0; r < 4; ++r) fr[i][r] = fi[i][r] = er[i] = ei[i] = 0.f;
        }
        ldhi<0>(fr[i][0], wa[i0 + i]); ldhi<128>(fr[i][1], wa[i0 + i]); ldhi<256>(fr[i][2], wa[i0 + i]); ldhi<384>(fr[i][3], wa[i0 + i]);
        ldhi<L_WS>(fi[i][0], wa[i0 + i]); ldhi<L_WS + 128>(fi[i][1], wa[i0 + i]); ldhi<L_WS + 256>(fi[i][2], wa[i0 + i]); ldhi<L_WS + 384>(fi[i][3], wa[i0 + i]);
        ldhi<0>(er[i], wo[i0 + i]); ldhi<L_WS>(ei[i], wo[i0 + i]);
      }
      if (!i0) gwait(L + L_SYNC, 2); else WAITL(0);
#pragma unroll
      for (int i = 0; i < NT; ++i)
#pragma unroll
        for (int r = 0; r < 4; ++r) { PIN(fr[i][r]); PIN(fi[i][r]); if (r == 0) { PIN(er[i]); PIN(ei[i]); } }
      if (cc == 0) {
        const float b0 = lane == 0 ? 0.5f : 0.f, b3 = lane == 32 ? 0.5f : 0.f;
#pragma unroll
        for (int i = 0; i < NT; ++i) {
          float t;
          asm volatile("v_sub_f32 %4, %5, %0\n\tv_fmac_f32 %0, %7, %4\n\tv_add_f32 %4, %6, %1\n\tv_fma_f32 %1, -%7, %4, %1\n\t"
                       "v_sub_f32 %4, %5, %2\n\tv_fmac_f32 %2, %8, %4\n\tv_add_f32 %4, %6, %3\n\tv_fma_f32 %3, -%8, %4, %3"
                       : "+v"(fr[i][0]), "+v"(fi[i][0]), "+v"(fr[i][3]), "+v"(fi[i][3]), "=&v"(t) : "v"(er[i]), "v"(ei[i]), "v"(b0), "v"(b3));
        }
      }
      if (__builtin_expect(k1 == 0, 0)) {
#pragma unroll
        for (int i = 0; i < NT; ++i) { const float wt_ = (float)tile_desc(w + 4 * (i0 + i), 0, kd1).w; yr[i] *= wt_; yi[i] *= wt_; }
      }
      SB();
#pragma unroll
      for (int i = 0; i < NT; ++i) {
        const f32x4 vr = {fr[i][0], fr[i][1], fr[i][2], fr[i][3]}, vi = {fi[i][0], fi[i][1], fi[i][2], fi[i][3]};
        a[i] = pk8(yr[i] * vr - yi[i] * vi, yr[i] * vi + yi[i] * vr); yr[i] = mma(a[i], gb_r, z4); yi[i] = mma(a[i], gb_i, z4);
        SB();
        if (!i0) {
#pragma unroll
          for (int e = 4 * NC0 + (4 - NC0) * i; e < 4 * NC0 + (4 - NC0) * (i + 1); ++e) c_piece(e >> 2, e & 3);
        }
        SB();
      }
#pragma unroll
      for (int i = 0; i < NT; ++i) {
        a[i] = pk8(yr[i] * t2c - yi[i] * t2s, yr[i] * t2s + yi[i] * t2c); yr[i] = mma(a[i], ga_r, z4); yi[i] = mma(a[i], ga_i, z4);
        SB();
      }
#pragma unroll
      for (int i = 0; i < NT; ++i) { lw<u32x2>(pw_ + i * 8 * L_PS, u32x2{pk2(yr[i].xy), pk2(yr[i].zw)}); lw<u32x2>(pw_ + i * 8 * L_PS + L_PS, u32x2{pk2(yi[i].xy), pk2(yi[i].zw)}); }
    };
    tiles(IC<4>{}, IC<0>{});
    if (__builtin_expect(sp0 && w < 2, 0)) tiles(IC<1>{}, IC<4>{});
    TX(23);
    gbar(L + L_SYNC + 16, w, 5);
    TX(24);
    u32x4 tq[4];
    {
      f32x4 o[2][2][2];
      const u32 ap = vo + (4 * g + q) * L_PS + 16 * pq;
#pragma unroll
      for (int cb = 0; cb < 2; ++cb) { a0[cb][0] = cat(trr(ap + 64 * cb), trr(ap + 64 * cb + 16 * L_PS)); a0[cb][1] = cat(trr(ap + 64 * cb + 8), trr(ap + 64 * cb + 16 * L_PS + 8)); }
#pragma unroll
      for (int i = 0; i < 8; ++i) o[i >> 2][(i >> 1) & 1][i & 1] = mma(a0[(i >> 1) & 1][i & 1], mi[i >> 2], z4);
      if (__builtin_expect(sp0, 0)) {
        const u32 ax = vo + (32 + q) * L_PS + 16 * pq;
#pragma unroll
        for (int i = 0; i < 4; ++i) { const u32x2 t = trr(ax + 64 * (i >> 1) + 8 * (i & 1)); const bf16x8 a = cat(t, t); o[0][i >> 1][i & 1] = mma(a, mix[0], o[0][i >> 1][i & 1]); o[1][i >> 1][i & 1] = mma(a, mix[1], o[1][i >> 1][i & 1]); }
      }
      FENCE();
#pragma unroll
      for (int i = 0; i < 4; ++i) lw<bf16x8>(vo + i * 1280 + l15 * 80 + 16 * g, pk8(o[i >> 1][i & 1][0], o[i >> 1][i & 1][1]));
      FENCE();
#pragma unroll
      for (int i = 0; i < 4; ++i) tq[i] = lr<u32x4>(vo + i * 1280 + (lane >> 2) * 80 + pq * 16);
#pragma unroll
      for (int i = 0; i < 4; ++i) PIN(tq[i]);
    }
    TX(10);
    u32x4 tab[2][4];
    if (__builtin_expect(bid == 0 && w == 0, 0)) st2c(tag, wsr, lane == 0 ? WS_E : WS_END, 0);
#pragma unroll
    for (int i = 0; i < 16; ++i) { const u32 xv = sk[i >> 1][i & 1], dv = q_ds[i & 1]; sk[i >> 1][i & 1] = pk2(bf2f(xv & 0xffffu) * bf2f(dv & 0xffffu), bfhi(xv) * bfhi(dv)); PIN(sk[i >> 1][i & 1]); }
    TX(11);
    const u32 hb = WS_H + (u32)(tt * 8 + 2 * w) * 65536u + ((ct & 8) ? 32768 : 0);
    u32 fa_ = lane < 32 ? (4 * w + 2 * (lane & 1) + (sh ^ (lane & 1))) * 128 + (((lane >> 1) + rot) & 15) * 8 : WS_END, miss; PIN(fa_);
    u64 fh;
    auto fload = [&]() { asm volatile("buffer_load_dwordx2 %0, %1, %2, %3 offen sc1" : "=v"(fh) : "v"(fa_), "s"(wsr), "s"(WS_FH + rep) : "memory"); };
    auto fneed = [&](u32 bits) {
      if (__builtin_expect((miss & bits) != 0, 0)) {
        NOUNROLL for (u32 spn = 0; spn < SPIN_CAP; ++spn) {
          u64 a; asm volatile("buffer_load_dwordx2 %0, %1, %2, %3 offen sc1\n\ts_waitcnt vmcnt(0)" : "=&v"(a) : "v"(fa_), "s"(wsr), "s"(WS_FH + rep) : "memory");
          miss = (u32)__builtin_amdgcn_ballot_w64(a != tag);
          if (!(miss & bits)) break;
          __builtin_amdgcn_s_sleep(POLLSLP);
        }
      }
    };
    auto h_issue = [&](int u) {
      const u32 so = hb + ((u + rot) & 15) * 2048;
#pragma unroll
      for (int m = 0; m < 4; ++m) bldsc(wsr, vl, so + (m >> 1) * 65536 + (m & 1) * 1024, L + w * 4096, 98304 + (u & 3) * 16384 + m * 1024);
    };
    auto extras = [&]() {
      const u32 to = (u32)lr8 * (16 * 32768) + (u32)(2 * k1 + kd1) * 128 + lc8 * 16;
#pragma unroll
      for (int i = 0; i < 4; ++i) st4c(tq[i], wsr, to, WS_T + (8 * (i >> 1) + 2 * w + (i & 1)) * 32768);
#pragma unroll
      for (int e = 0; e < 4 * NE; ++e) c_piece(4 + (e >> 2), e & 3);
    };
    WAITV(0); BAR();
    TX(25);
    extras();
    fload();
    SB();
    {
      const float tk = (float)(2 * kap + 1) * (1.f / 512.f), tc = 2.f * cosr(4.f * tk);
#pragma unroll
      for (int i = 0; i < 8; ++i) {
        const float kk = (i & 4) ? -tk : tk;
        tab[i >> 2][i & 3] = cos8(fmaf(kk, 8.f * fg + (float)(i & 3), -0.125f), kk, tc); PIN(tab[i >> 2][i & 3]);
      }
    }
    TX(26);
    WAITV(0); PIN(fh); miss = (u32)__builtin_amdgcn_ballot_w64(fh != tag);
    fneed(FMASK); h_issue(0); h_issue(1); h_issue(2);
    SB();
    u32 ta[8];
    auto taf = [&]() {
#pragma unroll
    for (int i = 0; i < 8; ++i) {
      const int kb = 16 * i + 4 * w + g, k = (lane & 4) ? (kb ? 256 - kb : 128) : kb, rho = 64 * (k & 3) + (k >> 2);
      ta[i] = L + rho * 128 + 16 * ((((lane >> 1) & 4) | pq) ^ (((rho >> 1) & 1) | ((rho >> 3) & 1) << 1)); PIN(ta[i]);
    }
    };
    WAITV(8);
    TX(27);
    u64 ft;
    u32x4 tv[8], gv[2];
    f32x2 o[8][2];
    sfor<0, 16>([&](auto u_) {
      constexpr int u = decltype(u_)::v;
      if (u) WAITV(g2_wait(u));
      BAR();
      ITS(2, u);
      PINS(rot);
      if (u == FTU && w == 0) st2c(tag, wsr, fo, WS_FT);
      if (u == 3 || u == 6) { PIN(fh); miss &= (u32)__builtin_amdgcn_ballot_w64(fh != tag); }
      if constexpr (u + 3 < 16) fneed(3u << (2 * u + 6));
      if (u == 0 || u == 3) fload();
#pragma unroll
      for (int e = g2_c0(u); e < g2_c0(u + 1); ++e) c_piece(e >> 2, e & 3);
      if (u + 3 < 16) h_issue(u + 3);
      if constexpr (u >= TU && u < TU + 4) {
#pragma unroll
        for (int i = 2 * (u - TU); i < 2 * (u - TU) + 2; ++i) tv[i] = bl4c<0>(wsr, vl, WS_T + (s2 * 16 + ct) * 32768 + i * 4096 + w * 1024);
        if (u < TU + 2) gq[(u - TU) & 1] = bl1c(wsr, vGo + 128 * (u - TU), 0); else if (u == TU + 2) gv[0] = bl4c<0>(wsr, WS_G + tt * 512 + l15 * 16, 0); else gv[1] = bl4c<256>(wsr, WS_G + tt * 512 + (15 - l15) * 16, 0);
      }
      if (u == TU - 5) asm volatile("buffer_load_dwordx2 %0, %1, %2, %3 offen sc1" : "=v"(ft) : "v"(lane * 8), "s"(wsr), "s"(WS_FT + rep + w * 512) : "memory");
      if (u == TU - 1) { PIN(ft); if (__builtin_expect(!__all(ft == tag), 0)) pollq(wsr, WS_FT + rep + w * 512, lane, tag); }
      SB();
    });
    taf();
    WAITV(0);
#pragma unroll
    for (int i = 0; i < 8; ++i) { PIN(tv[i]); lw<u32x4>(ta[i], tv[i]); }
    LBAR();
    hart256<true>(o, L + (8 * g + q) * 128 + 16 * ((4 * (pq >> 1) + w) ^ (((q >> 1) & 1) | ((g & 1) << 1))) + 8 * (pq & 1), tab, kap, false);
    PIN(gq[0]); PIN(gq[1]); PIN(gv[0]); PIN(gv[1]);
#pragma unroll
    for (int i = 0; i < 8; ++i) {
      const float gg = __uint_as_float(gv[i >> 2][i < 4 ? i : (i + 3) & 3]), og = rbf(1.f - gg);
      const int pos = i < 4 ? l15 : 15 - l15;
#pragma unroll
      for (int hf = 0; hf < 2; ++hf)
        lw<f32x2>(L + 65536 + pos * 256 + 16 * ((8 * hf + 2 * w + (g & 1)) ^ pos) + 8 * (g >> 1) + 4096 * (i < 4 ? 2 * i : 2 * ((i + 3) & 3) + 1), fma2(f32x2{og, og}, f32x2{bf2f(sk[i][hf] & 0xffffu), bfhi(sk[i][hf])}, gg * o[i][hf]));
    }
    TX(30);
  } else {
    GETARGS();
    lw<u32>(L + L_SYNC + 4 * wv, 0u);
    const int r0 = 0; const u32 vA = 0;
    ARGS();
    const int wn = w & 1, wm = w >> 1;
    const int hpos = 8 * wm + lc8, hsh = (tt ^ (tt >> 1)) & 1;
    u32 vH = (u32)(lr8 & 1) * (1u << 20) + hsh * (1u << 19) + (tt >> 1) * 65536 + (2 * pt + wn) * 1024 + g * 256 + hpos * 16;
    const u32 vAg = WS_AGG + (u32)((pt * 2 + wn) * 16 + l15) * 16;
    const int kap = 16 * sh + l15, np = (16 - l15) & 15; const bool sp = kap == 0;
    const int ecol = 8 * w + 4 * (g & 1) + 2 * (g >> 1);
    TS(19);
    u32x4 tab[2][4];
    hart_table(tab, kap, sp, g);
#pragma unroll
    for (int i = 0; i < 8; ++i) PIN(tab[i >> 2][i & 3]);
    u32 ua[6], hap = L + L_RA + (8 * g + q) * 128 + 16 * ((4 * (pq >> 1) + w) ^ (((q >> 1) & 1) | ((g & 1) << 1))) + 8 * (pq & 1);
#pragma unroll
    for (int i = 0; i < 2; ++i) {
      const int lo = i ? np : l15;
      ua[i] = L + L_V + lo * 256 + ((2 * ecol) ^ ((lo & 7) << 4)); PIN(ua[i]);
    }
    ua[2] = (ua[1] ^ 64) + (sp ? 0 : 48 * 256); ua[3] = (ua[1] ^ 64) + (sp ? 32 * 256 : 16 * 256); ua[4] = (ua[0] ^ 64) + (sp ? 16 * 256 : 0);
    PIN(ua[2]); PIN(ua[3]); PIN(ua[4]); PIN(hap);
    c2 lam, coef, l8, l16;
    auto lamf = [&]() {
    const u32 qa = L + L_V + 16384 + (32 * wn + (lane & 31)) * 4, q_dt = lr<u16>(qa), q_lr = lr<u16>(qa + 256), q_li = lr<u16>(qa + 512);
    {
      const float lre = bf2f(q_lr), lim = bf2f(q_li);
      const float dt = rbf(expf(bf2f(q_dt))), mg = expf(dt * lre);
      float sn, cs; sincosf(dt * lim, &sn, &cs);
      const float nr = mg * cs - 1.f, ni = mg * sn;
      const bool big = fabsf(lre) >= fabsf(lim);
      const float u = big ? lre : lim, v = big ? lim : lre, t = v * __builtin_amdgcn_rcpf(u), rd = 2.f * __builtin_amdgcn_rcpf(u + v * t);
      lam = {spread16(mg * cs), spread16(ni)};
      coef = {spread16(rd * (big ? nr + ni * t : nr * t + ni)), spread16(rd * (big ? ni - nr * t : ni * t - nr))};
      l8 = csq(csq(csq(lam))); l16 = csq(l8);
      pinc(lam); pinc(coef); pinc(l8); pinc(l16);
    }
    };
    TS(22);
    SB();
      u32 aa[2][2], ab[2][2];
#pragma unroll
      for (int i = 0; i < 4; ++i) {
        const int co = 16 * ((4 * (i & 1) + g) ^ (l15 >> 1)) + (i >> 1) * 65536;
        aa[i & 1][i >> 1] = L + (64 * wm + l15) * 128 + co; ab[i & 1][i >> 1] = L + 16384 + (64 * wn + l15) * 128 + co; PIN(aa[i & 1][i >> 1]); PIN(ab[i & 1][i >> 1]);
      }
    SB();
    BAR();
    lamf();
    SB();
    LBAR();
    TS(1);
    {
      f32x2 o[8][2];
      hart256<false>(o, hap, tab, kap, sp);
#pragma unroll
      for (int i = 0; i < 8; ++i) {
        const u32 a = i == 0 ? ua[0] : i == 1 ? ua[0] + 32 * 256 : i == 2 ? ua[2] : i == 3 ? ua[3] : i == 4 ? ua[4] : i == 5 ? ua[1] + 16 * 256 : i == 6 ? ua[1] + 48 * 256 : ua[4] + 32 * 256;
        lw<u32>(a, pk2(o[i][0])); lw<u32>(a + 128, pk2(o[i][1]));
      }
    }
    gpost(L + L_SYNC, w, 1);
    SB();
    c2 pw[8], wt[4], lsa, lsb, l32 = csq(l16), l64 = csq(l32);
    u32 wa[8], ra[8], pa[4], vAs = lane < 16 ? vAg : WS_END, sa = L + L_SEG + wn * 256 + l15 * 16;
#pragma unroll
    for (int n = 0; n < 4; ++n) { pa[n] = vAg + max(tt - 1 - g - 4 * n, 0) * 8192; PIN(pa[n]); }
    u32 sw = sa + (4 * wm + g) * 512;
    pinc(l32); pinc(l64); PIN(vAs); PIN(sa); PIN(sw); PIN(vH);
    auto pwf = [&]() {
      const c2 one = {{1.f, 1.f}, {0.f, 0.f}}, zero = {{0.f, 0.f}, {0.f, 0.f}};
      const c2 l128 = csq(l64), l256 = csq(l128), l512 = csq(l256);
      lsa = cmul(cmul(coef, csel(g & 1, l16, one)), cmul(csel(g & 2, l32, one), csel(wm, l64, one))); lsb = cmul(lsa, l8);
      wt[0] = cmul(csel(g & 1, l128, one), csel(g & 2, l256, one));
#pragma unroll
      for (int n = 1; n < 4; ++n) wt[n] = cmul(wt[n - 1], l512);
#pragma unroll
      for (int n = 0; n < 4; ++n) { wt[n] = csel(tt - 1 - g - 4 * n >= 0, wt[n], zero); pinc(wt[n]); }
      pinc(lsa); pinc(lsb);
    };
    auto pwg = [&]() {
      pw[0] = lam; pw[1] = csq(lam); pw[2] = cmul(pw[1], lam); pw[3] = csq(pw[1]); pw[4] = cmul(pw[3], lam); pw[5] = csq(pw[2]); pw[6] = cmul(pw[3], pw[2]); pw[7] = l8;
#pragma unroll
      for (int s = 1; s < 7; ++s) pinc(pw[s]);
    };
    u64 tag;
    f32x4 acc[4][4];
    {
      LBAR();
      TS(2);
      u64 cy0 = 0; CY0();
      {
        bf16x8 A[2][2][4], B[2][2][4];
        auto rd = [&](int j, int i) { const int sl = (j + 3) & 3, ks = i & 1, f = (i >> 1) & 3; const u32 bs = (sl & 1) * 32768 + f * 2048;
          if (i < 8) B[j & 1][ks][f] = lr<bf16x8>(ab[ks][sl >> 1] + bs); else A[j & 1][ks][f] = lr<bf16x8>(aa[ks][sl >> 1] + bs); };
#pragma unroll
        for (int i = 0; i < 16; ++i) rd(0, i);
#pragma unroll
        for (int j = 0; j < 16; ++j) {
#pragma unroll
          for (int qq = 0; qq < 32; ++qq) {
            const int p = qq >> 1, m = p >> 2, n = (m & 1) ? 3 - (p & 3) : (p & 3), ks = (qq ^ p) & 1;
            if (qq == BQ && j < 17 - LEAD) LBAR();
            SB();
            if (j || (qq & 1)) mmc(acc[m][n], A[j & 1][ks][m], B[j & 1][ks][n]); else mmz(acc[m][n], A[j & 1][ks][m], B[j & 1][ks][n]);
            SB();
            if (j < 15 && qq >= BQ && qq < BQ + 16) rd(j + 1, qq - BQ);
          }
        }
      }
      NOPS();
      CY1(20);
    }
    TS(3);
    c2 h[2][8];
#pragma unroll
    for (int s = 0; s < 8; ++s) {
#pragma unroll
      for (int ps = 0; ps < 2; ++ps) {
        const f32x4 vr = acc[s >> 1][2 * ps], vi = acc[s >> 1][2 * ps + 1];
        h[ps][s] = (s & 1) ? c2{vr.zw, vi.zw} : c2{vr.xy, vi.xy};
        if (s) { const c2 a = bc(lam, ps); h[ps][s].r = fma2(-a.i, h[ps][s - 1].i, h[ps][s].r); h[ps][s].i = fma2(a.i, h[ps][s - 1].r, h[ps][s].i); }
      }
      SB();
#pragma unroll
      for (int ps = 0; ps < 2; ++ps) if (s) { const c2 a = bc(lam, ps); h[ps][s].r = fma2(a.r, h[ps][s - 1].r, h[ps][s].r); h[ps][s].i = fma2(a.r, h[ps][s - 1].i, h[ps][s].i); }
      SB();
    }
    const c2 ea = {{h[0][7].r.x, h[1][7].r.x}, {h[0][7].i.x, h[1][7].i.x}};
    { const c2 e = cfma(l8, ea, c2{{h[0][7].r.y, h[1][7].r.y}, {h[0][7].i.y, h[1][7].i.y}});
      lw<f32x4>(sw, f32x4{e.r.x, e.r.y, e.i.x, e.i.y}); }
    gbar(L + L_SYNC, w, 2);
    u32x4 gx[4], gw[2];
    c2 cin;
    {
      c2 e[8];
#pragma unroll
      for (int j = 0; j < 8; ++j) e[j] = asc(lr<f32x4>(sa + j * 512));
      SB();
      const c2 e01 = cfma(l16, e[0], e[1]), e23 = cfma(l16, e[2], e[3]), e45 = cfma(l16, e[4], e[5]), e67 = cfma(l16, e[6], e[7]);
      const c2 e03 = cfma(l32, e01, e23), e47 = cfma(l32, e45, e67), tot = cfma(l64, e03, e47);
      st4c(__builtin_bit_cast(u32x4, f32x4{tot.r.x, tot.r.y, tot.i.x, tot.i.y}), wsr, vAs, wm ? tt * 8192 : WS_END);
      SB();
      const c2 zero = {{0.f, 0.f}, {0.f, 0.f}};
      const c2 c0 = csel(wm, e03, zero), c2_ = cfma(l32, c0, csel(wm, e45, e01)), cb = csel(g & 2, c2_, c0);
      cin = csel(g & 1, cfma(l16, cb, csel(wm, csel(g & 2, e[6], e[4]), csel(g & 2, e[2], e[0]))), cb);
    }
    pinc(cin);
    TS(4);
    SB();
    auto gload = [&]()
      { const u32 xo = (bid * 8 + 2 * w) * 2048, vl = lane * 16; const rsrc_t gr = mkr((const void*)kb_[3], 2048);
        gx[0] = bl4<0>(xr, vl, xo); gx[1] = bl4<1024>(xr, vl, xo); gx[2] = bl4<2048>(xr, vl, xo); gx[3] = bl4<3072>(xr, vl, xo); gw[0] = bl4<0>(gr, vl, 0); gw[1] = bl4<1024>(gr, vl, 0); };
    gload();
    SB();
    c2 ca, cz; u32x4 v[4];
    auto poll = [&]() {
#pragma unroll
      for (int n = 0; n < 4; ++n) v[n] = bl4c<0>(wsr, pa[n], 0);
    };
    {
#pragma unroll
      for (int s = 0; s < 8; ++s) {
        f32x2 t[4];
#pragma unroll
        for (int ps = 0; ps < 2; ++ps) { const c2 a = bc(coef, ps); t[2 * ps] = a.r * h[ps][s].r; t[2 * ps + 1] = a.i * h[ps][s].r; }
        SB();
#pragma unroll
        for (int ps = 0; ps < 2; ++ps) { const c2 a = bc(coef, ps); h[ps][s].r = fma2(-a.i, h[ps][s].i, t[2 * ps]); h[ps][s].i = fma2(-a.r, h[ps][s].i, -t[2 * ps + 1]); }
        SB();
      }
#pragma unroll
      for (int s = 0; s < 16; ++s) pinc(h[s & 1][s >> 1]);
      SB();
      poll();
      SB();
      ca = cmul(coef, cin); cz = cmul(coef, cfma(l8, cin, ea));
      pinc(ca); pinc(cz);
      const u32 hs = L + 65536 + w * 8192;
#pragma unroll
      for (int i = 0; i < 8; ++i) {
        wa[i] = hs + l15 * 128 + ((4 * g + 16 * i) ^ (8 * (l15 & 1) + 4 * (l15 & 4) + 16 * (l15 & 2) + 8 * (l15 & 8))); PIN(wa[i]);
        ra[i] = hs + (8 * g + q) * 128 + ((8 * pq) ^ (8 * (q & 1) + 16 * (q & 2) + 64 * (g & 1)) ^ (16 * i)); PIN(ra[i]);
      }
    }
    SB();
    pwf();
    SB();
    pwg();
    SB();
    auto gate = [&]()
    {
      const u32 vG = WS_G + (u32)(((2 * w + (lane & 1)) * 2 + (((bid >> 4) ^ (bid >> 5)) & 1)) * 128 + ((bid >> 5) & 1) * 64 + (bid & 15) * 4 + (bid >> 6)) * 4;
      float sm[2] = {0.f, 0.f};
      PIN(gw[0]); PIN(gw[1]);
#pragma unroll
      for (int i = 0; i < 4; ++i) {
        PIN(gx[i]);
#pragma unroll
        for (int e = 0; e < 4; ++e) { const u32 a = gx[i][e], b = gw[i & 1][e]; sm[i >> 1] = __builtin_amdgcn_fdot2_f32_bf16(__builtin_bit_cast(bf16x2, a), __builtin_bit_cast(bf16x2, b), sm[i >> 1], false); }
      }
      sm[0] = dppadd<0xB1>(sm[0]); sm[1] = dppadd<0xB1>(sm[1]);
      float t = (lane & 1) ? sm[1] : sm[0];
      t = dppadd<0x4E>(t); t = dppadd<0x124>(t); t = dppadd<0x128>(t); t = sum32(sum16(t));
      __builtin_amdgcn_raw_buffer_store_b32(__float_as_uint(rbf(1.f / (1.f + expf(-rbf(t))))), wsr, lane < 2 ? vG : WS_END, 0, 16);
    };
    WAITV(4); gate();
    SB();
    TS(5);
    WAITV(1);
    if (tt > 0) {
      auto vok = [&]() { u32 mx = 0;
#pragma unroll
        for (int n = 0; n < 4; ++n) { PIN(v[n]); mx = umax3(umax3(mx, v[n].x, v[n].y), v[n].z, v[n].w); }
        return __all(mx != SENT); };
      if (!vok()) {
        NOUNROLL for (u32 spn = 0; spn < SPIN_CAP; ++spn) {
#pragma unroll
          for (int n = 0; n < 4; ++n) v[n] = bl4c<0>(wsr, pa[n], 0);
          WAITV(0);
          if (vok()) break;
        }
      }
      c2 cb = cfma(wt[0], asc(__builtin_bit_cast(f32x4, v[0])), cmul(wt[1], asc(__builtin_bit_cast(f32x4, v[1]))));
      const c2 cd = cfma(wt[2], asc(__builtin_bit_cast(f32x4, v[2])), cmul(wt[3], asc(__builtin_bit_cast(f32x4, v[3]))));
      cb.r += cd.r; cb.i += cd.i;
      cb = c2{{sum32(sum16(cb.r.x)), sum32(sum16(cb.r.y))}, {sum32(sum16(cb.i.x)), sum32(sum16(cb.i.y))}};
      ca = cfma(lsa, cb, ca); cz = cfma(lsb, cb, cz);
    } else {
#pragma unroll
      for (int n = 0; n < 4; ++n) PIN(v[n]);
    }
    TS(6);
    {
      const c2 cc[2] = {{{ca.r.x, cz.r.x}, {ca.i.x, cz.i.x}}, {{ca.r.y, cz.r.y}, {ca.i.y, cz.i.y}}};
      u32x4 hq[8];
#pragma unroll
      for (int s = 0; s < 8; ++s) {
#pragma unroll
        for (int ps = 0; ps < 2; ++ps) { const c2 a = bc(pw[s], ps); h[ps][s].r = fma2(-a.i, cc[ps].i, h[ps][s].r); h[ps][s].i = fma2(-a.i, cc[ps].r, h[ps][s].i); }
        SB();
#pragma unroll
        for (int ps = 0; ps < 2; ++ps) { const c2 a = bc(pw[s], ps); h[ps][s].r = fma2(a.r, cc[ps].r, h[ps][s].r); h[ps][s].i = fma2(-a.r, cc[ps].i, h[ps][s].i); }
        SB();
#pragma unroll
        for (int ps = 0; ps < 2; ++ps) { lw<u32>(wa[s] + ps * 2048, pk2(h[ps][s].r)); lw<u32>(wa[s] + ps * 2048 + 4096, pk2(h[ps][s].i)); }
        FENCE();
        if (s & 1) {
          hq[s - 1] = __builtin_bit_cast(u32x4, cat(trr(ra[s - 1]), trr(ra[s] + 512))); hq[s] = __builtin_bit_cast(u32x4, cat(trr(ra[s - 1] + 4096), trr(ra[s] + 4608)));
          if (s > 1) { st4c(hq[s - 3], wsr, vH, WS_H + ((s - 3) >> 1) * (2u << 20)); st4c(hq[s - 2], wsr, vH, WS_H + ((s - 3) >> 1) * (2u << 20) + 32768); }
        }
        SB();
      }
      st4c(hq[6], wsr, vH, WS_H + 3 * (2u << 20)); st4c(hq[7], wsr, vH, WS_H + 3 * (2u << 20) + 32768);
    }
    TS(7);
    {
      bf16x8 cp[6][4];
      u32 ha = L + 98304 + wm * 8192 + lane * 16, ca[2][2];
#pragma unroll
      for (int i = 0; i < 4; ++i) { ca[i & 1][i >> 1] = L + (i >> 1) * 65536 + wn * 8192 + l15 * 128 + 16 * ((4 * (i & 1) + g) ^ (l15 >> 1)); PIN(ca[i & 1][i >> 1]); }
      PIN(ha);
      tag = lr<u64>(L + L_SYNC + 32); PIN(tag);
      WAITV(0);
      gbar(L + L_SYNC, w, 3);
      if (w == 0) st2c(tag, wsr, lane < NREP ? lane * 2048 : WS_END, WS_FH + (tt * 16 + pt) * 8);
      { u64 pc; u32 bk, nb; asm volatile("s_getpc_b64 %0\n.Lcode_m%=:\n\ts_mov_b32 %1, .Lcode_m%=-.Lcode_lo\n\ts_mov_b32 %2, .Lcode_hi-.Lcode_lo" : "=s"(pc), "=s"(bk), "=s"(nb));
        if (w == 1) touch(mkr((const void*)((pc - bk) & ~127ull), min(nb, 61440u)), (lane * 32 + jj) * 128, L); }
      TS(28);
      BAR();
#pragma unroll
      for (int i = 0; i < 24; ++i) cp[i >> 2][i & 3] = lr<bf16x8>(ca[(i >> 2) & 1][0] + (i >> 3) * 16384 + (i & 3) * 2048);
      WAITV(0); LBAR();
#pragma unroll
      for (int i = 0; i < 24; ++i) PIN(cp[i >> 2][i & 3]);
      TS(12);
      u64 cy0 = 0; CY0();
      {
        bf16x8 H[2][2][4], Cf[2][2][4];
        auto rd = [&](int u, int i) { const int ks = i & 1, f = (i >> 1) & 3;
          if (i < 8) { if (u >= 3) Cf[u & 1][ks][f] = lr<bf16x8>(ca[ks][(u % 6) >> 2] + ((u % 6) & 3) * 16384 + f * 2048); } else H[u & 1][ks][f] = lr<bf16x8>(ha + (u & 3) * 16384 + f * 2048 + ks * 1024); };
#pragma unroll
        for (int i = 8; i < 16; ++i) rd(0, i);
#pragma unroll
        for (int u = 0; u < 16 - SE; ++u) {
#pragma unroll
          for (int qq = 0; qq < 32; ++qq) {
            const int p = qq >> 1, m = p >> 2, n = (m & 1) ? 3 - (p & 3) : (p & 3), ks = (qq ^ p) & 1;
            if (qq == BQ && u < 15) LBAR();
            SB();
            const bf16x8 cf = u < 3 ? cp[(2 * u + ks) % 6][n] : Cf[u & 1][ks][n];
            if (u || (qq & 1)) mmc(acc[m][n], cf, H[u & 1][ks][m]); else mmz(acc[m][n], cf, H[u & 1][ks][m]);
            SB();
            if (u < 15 && qq >= BQ && qq < BQ + 16) rd(u + 1, qq - BQ);
          }
        }
        u32 xs = g * 256 + (l15 ^ (4 * g)) * 16; PIN(xs);
#pragma unroll
        for (int qq = 0; qq < 32 * SE; ++qq) {
          const int u = 16 - SE + ((qq >> 4) % SE), n0 = qq < 16 * SE ? 2 : 0, p = (qq & 15) >> 1, m = p >> 1, n = n0 + ((p ^ m) & 1), ks = (qq ^ p) & 1;
          if (SE == 2 && qq == 0) LBAR();
          SB();
          mmc(acc[m][n], Cf[u & 1][ks][n], H[u & 1][ks][m]);
          SB();
          if (SE == 2 && qq < 16) rd(15, qq < 4 ? 4 + qq : qq < 12 ? 4 + qq : qq - 12);
          if (qq >= 16 * SE + 2 && qq < 16 * SE + 14 && (qq - 16 * SE - 2) % 3 == 0) { const int mm = (qq - 16 * SE - 2) / 3; st4c(__builtin_bit_cast(u32x4, pk8(acc[mm][2], acc[mm][3])), wsr, xs, WS_X + (((bid ^ 1) * 4 + w) * 4 + mm) * 1024); }
        }
      }
      NOPS();
      CY1(21);
    }
    TS(13);
    {
      const u32 vl = lane * 16;
#pragma unroll
      for (int m = 0; m < 4; ++m) if (!SE) st4c(__builtin_bit_cast(u32x4, pk8(acc[m][2], acc[m][3])), wsr, g * 256 + (l15 ^ (4 * g)) * 16, WS_X + (((bid ^ 1) * 4 + w) * 4 + m) * 1024);
      gq[0] = bl1c(wsr, vGo, 0); gq[1] = bl1c(wsr, vGo + 128, 0);
      BAR();
      WAITV(2);
      st2c(tag, wsr, lane == 0 ? 0 : WS_END, WS_FP + ((bid ^ 1) * 4 + w) * 128);
      TS(29);
      const u32 pa = L + 98304 + wm * 16384 + wn * 2048 + g * 256 + (l15 ^ (4 * g)) * 16;
#pragma unroll
      for (int i = 0; i < 8; ++i) lw<f32x4>(pa + (i >> 1) * 4096 + (i & 1) * 1024, acc[i >> 1][i & 1]);
      NOUNROLL for (u32 spn = 0; spn < SPIN_CAP; ++spn) {
        u64 a; asm volatile("buffer_load_dwordx2 %0, off, %1, %2 sc1\n\ts_waitcnt vmcnt(0)" : "=&v"(a) : "s"(wsr), "s"(WS_FP + (bid * 4 + w) * 128) : "memory");
        if (__all(a == tag)) break;
      }
#pragma unroll
      for (int m = 0; m < 4; ++m) bldsc(wsr, vl, WS_X + (bid * 4 + w) * 4096 + m * 1024, L + w * 4096, 131072 + m * 1024);
      PIN(gq[0]); PIN(gq[1]);
      TS(31);
    }
  }
  if (!rh) TS(14);
  {
    const rsrc_t outr = mkr((const void*)outp, 8u << 20);
    u32 pa[2], xa[2], ba[4], oa[2]; float og[2];
#pragma unroll
    for (int hi = 0; hi < 2; ++hi) {
      const int row = lr8 + 8 * hi, s1 = 32 * wv + row + 16 * (sh ^ (w & 1));
      pa[hi] = L + 98304 + wv * 4096 + lc8 * 256 + (row ^ (4 * pq)) * 16; oa[hi] = (u32)(s2 + 8 * s1) * 4096u + lc8 * 16; og[hi] = rbf(1.f - __uint_as_float(gq[hi]));
      ba[2 * hi] = L + 65536 + (wv * 16 + row) * 256 + 16 * (lc8 ^ row); ba[2 * hi + 1] = ba[2 * hi] ^ 128;
      xa[hi] = L + 131072 + rh * 8192 + w * 1024 + pq * 256 + (row ^ (4 * pq)) * 16 + (lc8 >> 2) * 8; PIN(xa[hi]);
      PIN(pa[hi]); PIN(oa[hi]); PIN(og[hi]); PIN(ba[2 * hi]); PIN(ba[2 * hi + 1]);
    }
    WAITV(0); LBAR();
    f32x4 v[4][2]; u32x2 xv[4];
#pragma unroll
    for (int i = 0; i < 4; ++i) {
      v[i][0] = lr<f32x4>(pa[i >> 1] + (i & 1) * 2048); v[i][1] = lr<f32x4>(ba[i]); xv[i] = lr<u32x2>(xa[i >> 1] + (i & 1) * 4096);
    }
    SB();
#pragma unroll
    for (int i = 0; i < 4; ++i)
      st4(__builtin_bit_cast(u32x4, og[i >> 1] * (v[i][0] + f32x4{bf2f(xv[i].x & 0xffffu), bfhi(xv[i].x), bf2f(xv[i].y & 0xffffu), bfhi(xv[i].y)}) + v[i][1]), outr, oa[i >> 1], 128 * ct + 2048 * (i & 1));
  }
  if (!rh) TS(15);
}

extern "C" void kernel_launch(void* const* d_in, const int* in_sizes, int n_in, void* d_out, int out_size, void* d_ws, size_t ws_size, hipStream_t stream) {
  (void)in_sizes; (void)n_in; (void)out_size; (void)ws_size;
  (void)hipFuncSetAttribute((const void*)k_all, hipFuncAttributeMaxDynamicSharedMemorySize, L_TOT);
  const u16* const* in = (const u16* const*)d_in;
  k_all<<<256, 512, L_TOT, stream>>>(in[0], in[6], in[7], in[10], in[4], in[5], in[1], in[2], in[8], in[9], in[11], in[12], (char*)d_ws, (float*)d_out);
}
```
